```python
import torch

def _softmax(x, axis=-1):
    return torch.softmax(x, dim=axis)

def _gelu(x):
    return torch.nn.functional.gelu(x, approximate='tanh')

def _silu(x):
    return torch.nn.functional.silu(x)

def _sigmoid(x):
    return torch.sigmoid(x)

def _layernorm(x, axis=-1, eps=1e-05):
    m = x.mean(dim=axis, keepdim=True)
    v = x.var(dim=axis, keepdim=True, unbiased=False)
    return (x - m) / torch.sqrt(v + eps)

def _scatter_add(tgt, idx, src, axis=0):
    return tgt.clone().index_add_(axis, idx, src)

def _segment_sum(src, seg, n):
    out = torch.zeros((n,) + src.shape[1:], dtype=src.dtype)
    return out.index_add_(0, seg, src)

def _pad(x, pw):
    flat = tuple((v for pair in reversed(pw) for v in pair))
    return torch.nn.functional.pad(x, flat)
kernel_name = 'parallel_sum__memory_aug_recurrent_mem_tr__geglu_ffn__prompt'

def setup_inputs():

    _w = lambda a: (lambda t: t.to(torch.bfloat16) if t.is_floating_point() else t)(torch.as_tensor(a))

    def _A_setup():
        B, S, Dm = (1, 2048, 512)
        H, Dh = (32, 128)
        M, seg = (8, 32)
        seg = min(seg, S)
        S = S // seg * seg
        return {'x': _w((lambda *s: torch.randn(*s, dtype=torch.bfloat16) * 0.05)(B, S, Dm)), 'mem0': _w((lambda *s: torch.randn(*s, dtype=torch.bfloat16) * 1.6)(B, M, Dm)), 'Wq': _w((lambda *s: torch.randn(*s, dtype=torch.bfloat16) * 0.8)(Dm, H, Dh) * Dm ** (-0.5)), 'Wk': _w((lambda *s: torch.randn(*s, dtype=torch.bfloat16) * 0.8)(Dm, H, Dh) * Dm ** (-0.5)), 'Wv': _w((lambda *s: torch.randn(*s, dtype=torch.bfloat16) * 1.6)(Dm, H, Dh) * Dm ** (-0.5)), 'Wo': _w((lambda *s: torch.randn(*s, dtype=torch.bfloat16) * 1.6)(H, Dh, Dm) * (H * Dh) ** (-0.5)), 'seg_len': seg}
    _A_in = _A_setup()

    def _B_setup():
        B, S, D, F = (1, 2048, 512, 1792)
        return {'x': _w((lambda *s: torch.randn(*s, dtype=torch.bfloat16) * 0.05)(B, S, D)), 'w_gate': _w((lambda *s: torch.randn(*s, dtype=torch.bfloat16) * 0.05)(D, F)), 'w_up': _w((lambda *s: torch.randn(*s, dtype=torch.bfloat16) * 0.05)(D, F)), 'w_down': _w((lambda *s: torch.randn(*s, dtype=torch.bfloat16) * 0.05)(F, D))}
    _B_in = _B_setup()
    return {'A_x': _A_in['x'], 'A_mem0': _A_in['mem0'], 'A_Wq': _A_in['Wq'], 'A_Wk': _A_in['Wk'], 'A_Wv': _A_in['Wv'], 'A_Wo': _A_in['Wo'], 'A_seg_len': _A_in['seg_len'], 'B_x': _B_in['x'], 'B_w_gate': _B_in['w_gate'], 'B_w_up': _B_in['w_up'], 'B_w_down': _B_in['w_down']}

def reference(A_x, A_mem0, A_Wq, A_Wk, A_Wv, A_Wo, A_seg_len, B_x, B_w_gate, B_w_up, B_w_down):

    def _A_ref(x, mem0, Wq, Wk, Wv, Wo, seg_len):
        B, S, Dm = x.shape
        H, Dh = (Wq.shape[1], Wq.shape[2])
        M = mem0.shape[1]
        scale = Dh ** (-0.5)
        n_seg = S // seg_len
        L = M + seg_len
        idx = torch.arange(L)
        mask = (idx[:, None] < M) | (idx[:, None] >= idx[None, :])
        mem = mem0
        outs = []
        for s in range(n_seg):
            xs = x[:, s * seg_len:(s + 1) * seg_len, :].clone()
            h = torch.concatenate([mem, xs], axis=1)
            q = torch.einsum('btd,dhe->bhte', h, Wq)
            k = torch.einsum('btd,dhe->bhte', h, Wk)
            v = torch.einsum('btd,dhe->bhte', h, Wv)
            scores = torch.einsum('bhqe,bhke->bhqk', q, k) * scale
            scores = torch.where(mask[None, None], scores, -1000000000.0)
            p = _softmax(scores, axis=-1)
            o = torch.einsum('bhqk,bhke->bhqe', p, v)
            o = torch.einsum('bhte,hed->btd', o, Wo)
            mem = o[:, :M, :].clone()
            outs.append(o[:, M:, :])
        return torch.concatenate(outs, axis=1)

    def _B_ref(x, w_gate, w_up, w_down):
        return _gelu(x @ w_gate) * (x @ w_up) @ w_down
    x = A_x
    xn = _layernorm(x, axis=-1)
    return x + _A_ref(xn, A_mem0, A_Wq, A_Wk, A_Wv, A_Wo, A_seg_len) + _B_ref(xn, B_w_gate, B_w_up, B_w_down)
```

```cpp
#include <hip/hip_runtime.h>
#include <hip/hip_bf16.h>
#include <stdint.h>

constexpr int LDS_BYTES = 163840;

struct Params {
  const void *x, *mem0, *wq, *wk, *wv, *wo, *wg, *wu, *wd;
  void *out, *ws;
  unsigned p0, p1, p2, p3;
};

__global__ void __launch_bounds__(256, 1) rmt_main(Params p) {
  const unsigned wave = __builtin_amdgcn_readfirstlane(threadIdx.x >> 6);
  asm volatile(
"s_load_dwordx16 s[8:23], %0, 0\n"
"s_load_dwordx8 s[24:31], %0, 64\n"
"s_load_dwordx2 s[32:33], %0, 96\n"
"s_xor_b32 s36, %1, 0\n"
"s_mov_b32 s37, %2\n"
"s_mov_b64 exec, -1\n"
"v_mbcnt_lo_u32_b32 v119, -1, 0\n"
"v_mbcnt_hi_u32_b32 v119, -1, v119\n"
"s_and_b32 s64, s36, 3\n"
"s_lshr_b32 s65, s36, 3\n"
"s_and_b32 s66, s65, 7\n"
"s_lshl_b32 s38, s64, 3\n"
"s_add_u32 s38, s38, s66\n"
"s_lshr_b32 s39, s65, 3\n"
"s_waitcnt lgkmcnt(0)\n"
"s_mov_b32 s60, s28\n"
"s_and_b32 s61, s29, 0xffff\n"
"s_mov_b32 s62, 0x5000000\n"
"s_mov_b32 s63, 0x20000\n"
"s_mov_b32 s66, 3145728\n"
"buffer_load_dword v118, off, s[60:63], s66 sc1\n"
"s_bitcmp1_b32 s36, 2\n"
"s_cbranch_scc1 .Lfar6_%=\n"
"s_cmp_ge_u32 s39, 2\n"
"s_cbranch_scc1 .Lsu9_%=\n"
".Lh02_%=:\n"
"s_mov_b32 s40, s12\n"
"s_and_b32 s41, s13, 0xffff\n"
"s_mov_b32 s42, 0x400000\n"
"s_mov_b32 s43, 0x20000\n"
"s_mov_b32 s44, s14\n"
"s_and_b32 s45, s15, 0xffff\n"
"s_mov_b32 s46, 0x400000\n"
"s_mov_b32 s47, 0x20000\n"
"s_mov_b32 s48, s16\n"
"s_and_b32 s49, s17, 0xffff\n"
"s_mov_b32 s50, 0x400000\n"
"s_mov_b32 s51, 0x20000\n"
"s_mov_b32 s52, s18\n"
"s_and_b32 s53, s19, 0xffff\n"
"s_mov_b32 s54, 0x400000\n"
"s_mov_b32 s55, 0x20000\n"
"s_mov_b32 s56, s10\n"
"s_and_b32 s57, s11, 0xffff\n"
"s_mov_b32 s58, 0x2000\n"
"s_mov_b32 s59, 0x20000\n"
"s_lshl_b32 s67, s38, 8\n"
"s_lshl_b32 s71, s39, 21\n"
"v_lshrrev_b32 v108, 4, v119\n"
"v_and_b32 v109, 1, v108\n"
"s_lshl_b32 s73, s37, 1\n"
"v_or_b32 v109, s73, v109\n"
"v_lshlrev_b32 v109, 1, v109\n"
"v_and_b32 v110, 15, v119\n"
"v_xor_b32 v109, v109, v110\n"
"v_lshlrev_b32 v109, 4, v109\n"
"s_lshl_b32 s73, s37, 2\n"
"v_add_u32 v108, s73, v108\n"
"v_mul_u32_u24 v108, 8192, v108\n"
"v_add3_u32 v108, v108, v109, s67\n"
"s_lshl_b32 s73, s37, 12\n"
"s_add_u32 s73, s73, 98304\n"
"v_add_u32 v109, 130048, v108\n"
"v_add_u32 v110, 260096, v108\n"
"v_add_u32 v111, 390144, v108\n"
"s_add_u32 m0, s73, 0\n"
"s_xor_b32 s72, s71, 0\n"
"buffer_load_dwordx4 v108, s[48:51], s72 offen offset:0 lds\n"
"buffer_load_dwordx4 v109, s[48:51], s72 offen offset:1024 lds\n"
"buffer_load_dwordx4 v110, s[48:51], s72 offen offset:2048 lds\n"
"buffer_load_dwordx4 v111, s[48:51], s72 offen offset:3072 lds\n"
"s_add_u32 m0, s73, 16384\n"
"s_xor_b32 s72, s71, 524288\n"
"buffer_load_dwordx4 v108, s[48:51], s72 offen offset:0 lds\n"
"buffer_load_dwordx4 v109, s[48:51], s72 offen offset:1024 lds\n"
"buffer_load_dwordx4 v110, s[48:51], s72 offen offset:2048 lds\n"
"buffer_load_dwordx4 v111, s[48:51], s72 offen offset:3072 lds\n"
"s_add_u32 m0, s73, 32768\n"
"s_xor_b32 s72, s71, 1048576\n"
"buffer_load_dwordx4 v108, s[48:51], s72 offen offset:0 lds\n"
"buffer_load_dwordx4 v109, s[48:51], s72 offen offset:1024 lds\n"
"buffer_load_dwordx4 v110, s[48:51], s72 offen offset:2048 lds\n"
"buffer_load_dwordx4 v111, s[48:51], s72 offen offset:3072 lds\n"
"v_bfe_u32 v112, v119, 4, 1\n"
"v_bfe_u32 v113, v119, 3, 1\n"
"v_lshl_add_u32 v112, v113, 1, v112\n"
"v_bfe_u32 v113, v119, 2, 1\n"
"v_lshl_add_u32 v114, v112, 1, v113\n"
"v_lshlrev_b32 v112, 12, v112\n"
"v_lshl_add_u32 v112, v113, 8, v112\n"
"v_bfe_u32 v113, v119, 5, 1\n"
"v_lshl_add_u32 v112, v113, 10, v112\n"
"v_and_b32 v113, 3, v119\n"
"v_lshl_add_u32 v112, v113, 3, v112\n"
"s_lshl_b32 s72, s37, 1\n"
"v_xor_b32 v114, s72, v114\n"
"v_lshl_add_u32 v112, v114, 5, v112\n"
"v_add_u32 v112, 98304, v112\n"
"v_xor_b32 v113, 32, v112\n"
"v_add_u32 v114, 0x10000, v112\n"
"v_add_u32 v115, 0x10000, v113\n"
"v_lshlrev_b32 v125, 4, v119\n"
"s_lshl_b32 s64, s37, 14\n"
"s_add_u32 s64, s64, 32768\n"
"v_add_u32 v127, s64, v125\n"
"s_mov_b32 s66, 0xaaaaaaaa\n"
"s_mov_b32 s67, 0xaaaaaaaa\n"
"v_mov_b32 v0, 0x44444444\n"
"v_mov_b32 v1, 0x1e1eeeee\n"
"v_cndmask_b32 v124, v0, v1, s[66:67]\n"
"s_mov_b32 s68, 0x3e0293ee\n"
"v_lshrrev_b32 v0, 4, v119\n"
"v_and_b32 v1, 15, v119\n"
"v_lshrrev_b32 v2, 2, v1\n"
"v_and_b32 v3, 3, v1\n"
"s_mul_i32 s64, s37, 1600\n"
"s_add_u32 s64, s64, 10240\n"
"v_mul_u32_u24 v4, 400, v2\n"
"v_lshl_add_u32 v4, v0, 5, v4\n"
"v_lshl_add_u32 v4, v3, 2, v4\n"
"v_add_u32 v67, s64, v4\n"
"s_lshl_b32 s64, s37, 9\n"
"s_add_u32 s64, s64, 8192\n"
"v_lshrrev_b32 v4, 3, v1\n"
"v_lshlrev_b32 v4, 8, v4\n"
"v_and_b32 v5, 1, v2\n"
"v_lshl_add_u32 v4, v5, 4, v4\n"
"v_lshl_add_u32 v4, v0, 6, v4\n"
"v_lshl_add_u32 v4, v3, 2, v4\n"
"v_add_u32 v71, s64, v4\n"
"v_mul_u32_u24 v4, 400, v0\n"
"v_add_u32 v4, 10240, v4\n"
"v_lshrrev_b32 v5, 1, v1\n"
"v_lshl_add_u32 v75, v5, 4, v4\n"
"v_lshrrev_b32 v6, 3, v1\n"
"v_xor_b32 v5, v5, v6\n"
"v_and_b32 v5, 1, v5\n"
"v_and_b32 v6, 1, v1\n"
"v_lshl_add_u32 v5, v6, 1, v5\n"
"v_and_b32 v6, 4, v1\n"
"v_add_u32 v5, v5, v6\n"
"v_lshl_add_u32 v4, v5, 4, v4\n"
"v_add_u32 v79, 128, v4\n"
"s_lshl_b32 s64, s39, 2\n"
"s_add_u32 s64, s64, s37\n"
"s_lshl_b32 s64, s64, 16\n"
"s_lshl_b32 s65, s38, 7\n"
"s_add_u32 s64, s64, s65\n"
"v_lshlrev_b32 v4, 14, v2\n"
"v_lshrrev_b32 v5, 1, v0\n"
"v_lshl_add_u32 v4, v5, 12, v4\n"
"v_and_b32 v5, 1, v0\n"
"v_lshl_add_u32 v4, v5, 6, v4\n"
"v_lshl_add_u32 v4, v3, 4, v4\n"
"v_add_u32 v126, s64, v4\n"
"v_mov_b32 v120, 0x3f803f80\n"
"v_mov_b32 v121, 0x3f00\n"
"v_mov_b32 v122, 0x3f803f80\n"
"v_mov_b32 v123, 0x3f00\n"
"v_lshrrev_b32 v116, 3, v119\n"
"v_lshlrev_b32 v116, 10, v116\n"
"v_and_b32 v117, 7, v119\n"
"v_lshl_add_u32 v116, v117, 4, v116\n"
"s_lshl_b32 s64, s38, 17\n"
"s_lshl_b32 s65, s39, 9\n"
"s_add_u32 s64, s64, s65\n"
"s_lshl_b32 s65, s37, 7\n"
"s_add_u32 s64, s64, s65\n"
"v_add_u32 v116, s64, v116\n"
"v_bfe_u32 v117, v119, 5, 1\n"
"v_bfe_u32 v0, v119, 4, 1\n"
"v_lshlrev_b32 v117, 10, v117\n"
"v_lshl_add_u32 v117, v0, 8, v117\n"
"v_lshl_add_u32 v117, v1, 3, v117\n"
"s_lshl_b32 s70, s37, 14\n"
"s_add_u32 s70, s70, 32768\n"
"v_add_u32 v117, s70, v117\n"
"s_lshl_b32 s97, s38, 15\n"
"s_lshl_b32 s64, s37, 11\n"
"s_add_u32 s97, s97, s64\n"
"s_add_u32 s97, s97, 75497472\n"
"s_lshl_b32 s100, s38, 8\n"
"s_lshl_b32 s64, s37, 5\n"
"s_add_u32 s100, s100, s64\n"
"s_add_u32 s100, s100, 3149824\n"
"s_waitcnt vmcnt(8) lgkmcnt(0)\n"
"s_barrier\n"
"s_add_u32 m0, s73, 49152\n"
"s_xor_b32 s72, s71, 1572864\n"
"buffer_load_dwordx4 v108, s[48:51], s72 offen offset:0 lds\n"
"buffer_load_dwordx4 v109, s[48:51], s72 offen offset:1024 lds\n"
"buffer_load_dwordx4 v110, s[48:51], s72 offen offset:2048 lds\n"
"buffer_load_dwordx4 v111, s[48:51], s72 offen offset:3072 lds\n"
"ds_read_b64_tr_b16 v[128:129], v112 offset:0\n"
"ds_read_b64_tr_b16 v[130:131], v112 offset:512\n"
"ds_read_b64_tr_b16 v[132:133], v112 offset:2048\n"
"ds_read_b64_tr_b16 v[134:135], v112 offset:2560\n"
"ds_read_b64_tr_b16 v[136:137], v113 offset:0\n"
"ds_read_b64_tr_b16 v[138:139], v113 offset:512\n"
"ds_read_b64_tr_b16 v[140:141], v113 offset:2048\n"
"ds_read_b64_tr_b16 v[142:143], v113 offset:2560\n"
"s_waitcnt vmcnt(8) lgkmcnt(0)\n"
"s_barrier\n"
"s_add_u32 m0, s73, 0\n"
"s_xor_b32 s72, s71, 2097152\n"
"buffer_load_dwordx4 v108, s[48:51], s72 offen offset:0 lds\n"
"buffer_load_dwordx4 v109, s[48:51], s72 offen offset:1024 lds\n"
"buffer_load_dwordx4 v110, s[48:51], s72 offen offset:2048 lds\n"
"buffer_load_dwordx4 v111, s[48:51], s72 offen offset:3072 lds\n"
"ds_read_b64_tr_b16 v[144:145], v112 offset:16384\n"
"ds_read_b64_tr_b16 v[146:147], v112 offset:16896\n"
"ds_read_b64_tr_b16 v[148:149], v112 offset:18432\n"
"ds_read_b64_tr_b16 v[150:151], v112 offset:18944\n"
"ds_read_b64_tr_b16 v[152:153], v113 offset:16384\n"
"ds_read_b64_tr_b16 v[154:155], v113 offset:16896\n"
"ds_read_b64_tr_b16 v[156:157], v113 offset:18432\n"
"ds_read_b64_tr_b16 v[158:159], v113 offset:18944\n"
"v_readfirstlane_b32 s75, v118\n"
"s_nop 0\n"
"s_add_u32 s75, s75, 1\n"
"s_mul_i32 s34, s75, 0x9e3779b1\n"
"s_xor_b32 s35, s34, 0x5bd1e995\n"
"s_lshl_b32 s64, s37, 11\n"
"buffer_load_dwordx4 v[96:99], v125, s[56:59], s64 offen offset:0\n"
"buffer_load_dwordx4 v[100:103], v125, s[56:59], s64 offen offset:1024\n"
"s_waitcnt vmcnt(10) lgkmcnt(0)\n"
"s_barrier\n"
"s_add_u32 m0, s73, 16384\n"
"s_xor_b32 s72, s71, 2621440\n"
"buffer_load_dwordx4 v108, s[48:51], s72 offen offset:0 lds\n"
"buffer_load_dwordx4 v109, s[48:51], s72 offen offset:1024 lds\n"
"buffer_load_dwordx4 v110, s[48:51], s72 offen offset:2048 lds\n"
"buffer_load_dwordx4 v111, s[48:51], s72 offen offset:3072 lds\n"
"ds_read_b64_tr_b16 v[160:161], v112 offset:32768\n"
"ds_read_b64_tr_b16 v[162:163], v112 offset:33280\n"
"ds_read_b64_tr_b16 v[164:165], v112 offset:34816\n"
"ds_read_b64_tr_b16 v[166:167], v112 offset:35328\n"
"ds_read_b64_tr_b16 v[168:169], v113 offset:32768\n"
"ds_read_b64_tr_b16 v[170:171], v113 offset:33280\n"
"ds_read_b64_tr_b16 v[172:173], v113 offset:34816\n"
"ds_read_b64_tr_b16 v[174:175], v113 offset:35328\n"
"s_add_u32 m0, s70, 0\n"
"s_mov_b32 s64, 0\n"
"buffer_load_dwordx4 v116, s[52:55], s64 offen lds\n"
"s_add_u32 m0, s70, 1024\n"
"s_mov_b32 s64, 8192\n"
"buffer_load_dwordx4 v116, s[52:55], s64 offen lds\n"
"s_add_u32 m0, s70, 2048\n"
"s_mov_b32 s64, 16384\n"
"buffer_load_dwordx4 v116, s[52:55], s64 offen lds\n"
"s_add_u32 m0, s70, 3072\n"
"s_mov_b32 s64, 24576\n"
"buffer_load_dwordx4 v116, s[52:55], s64 offen lds\n"
"s_waitcnt vmcnt(14) lgkmcnt(0)\n"
"s_barrier\n"
"s_add_u32 m0, s73, 32768\n"
"s_xor_b32 s72, s71, 3145728\n"
"buffer_load_dwordx4 v108, s[48:51], s72 offen offset:0 lds\n"
"buffer_load_dwordx4 v109, s[48:51], s72 offen offset:1024 lds\n"
"buffer_load_dwordx4 v110, s[48:51], s72 offen offset:2048 lds\n"
"buffer_load_dwordx4 v111, s[48:51], s72 offen offset:3072 lds\n"
"ds_read_b64_tr_b16 v[176:177], v112 offset:49152\n"
"ds_read_b64_tr_b16 v[178:179], v112 offset:49664\n"
"ds_read_b64_tr_b16 v[180:181], v112 offset:51200\n"
"ds_read_b64_tr_b16 v[182:183], v112 offset:51712\n"
"ds_read_b64_tr_b16 v[184:185], v113 offset:49152\n"
"ds_read_b64_tr_b16 v[186:187], v113 offset:49664\n"
"ds_read_b64_tr_b16 v[188:189], v113 offset:51200\n"
"ds_read_b64_tr_b16 v[190:191], v113 offset:51712\n"
"s_add_u32 m0, s70, 4096\n"
"s_mov_b32 s64, 32768\n"
"buffer_load_dwordx4 v116, s[52:55], s64 offen lds\n"
"s_add_u32 m0, s70, 5120\n"
"s_mov_b32 s64, 40960\n"
"buffer_load_dwordx4 v116, s[52:55], s64 offen lds\n"
"s_add_u32 m0, s70, 6144\n"
"s_mov_b32 s64, 49152\n"
"buffer_load_dwordx4 v116, s[52:55], s64 offen lds\n"
"s_add_u32 m0, s70, 7168\n"
"s_mov_b32 s64, 57344\n"
"buffer_load_dwordx4 v116, s[52:55], s64 offen lds\n"
"s_waitcnt vmcnt(18) lgkmcnt(0)\n"
"s_barrier\n"
"s_add_u32 m0, s73, 49152\n"
"s_xor_b32 s72, s71, 3670016\n"
"buffer_load_dwordx4 v108, s[48:51], s72 offen offset:0 lds\n"
"buffer_load_dwordx4 v109, s[48:51], s72 offen offset:1024 lds\n"
"buffer_load_dwordx4 v110, s[48:51], s72 offen offset:2048 lds\n"
"buffer_load_dwordx4 v111, s[48:51], s72 offen offset:3072 lds\n"
"ds_read_b64_tr_b16 v[192:193], v112 offset:0\n"
"ds_read_b64_tr_b16 v[194:195], v112 offset:512\n"
"ds_read_b64_tr_b16 v[196:197], v112 offset:2048\n"
"ds_read_b64_tr_b16 v[198:199], v112 offset:2560\n"
"ds_read_b64_tr_b16 v[200:201], v113 offset:0\n"
"ds_read_b64_tr_b16 v[202:203], v113 offset:512\n"
"ds_read_b64_tr_b16 v[204:205], v113 offset:2048\n"
"ds_read_b64_tr_b16 v[206:207], v113 offset:2560\n"
"s_add_u32 m0, s70, 8192\n"
"s_mov_b32 s64, 65536\n"
"buffer_load_dwordx4 v116, s[52:55], s64 offen lds\n"
"s_add_u32 m0, s70, 9216\n"
"s_mov_b32 s64, 73728\n"
"buffer_load_dwordx4 v116, s[52:55], s64 offen lds\n"
"s_add_u32 m0, s70, 10240\n"
"s_mov_b32 s64, 81920\n"
"buffer_load_dwordx4 v116, s[52:55], s64 offen lds\n"
"s_add_u32 m0, s70, 11264\n"
"s_mov_b32 s64, 90112\n"
"buffer_load_dwordx4 v116, s[52:55], s64 offen lds\n"
"s_waitcnt vmcnt(20) lgkmcnt(0)\n"
"s_barrier\n"
"s_add_u32 m0, s73, 0\n"
"s_xor_b32 s72, s71, 0\n"
"buffer_load_dwordx4 v108, s[40:43], s72 offen offset:0 lds\n"
"buffer_load_dwordx4 v109, s[40:43], s72 offen offset:1024 lds\n"
"buffer_load_dwordx4 v110, s[40:43], s72 offen offset:2048 lds\n"
"buffer_load_dwordx4 v111, s[40:43], s72 offen offset:3072 lds\n"
"ds_read_b64_tr_b16 v[208:209], v112 offset:16384\n"
"ds_read_b64_tr_b16 v[210:211], v112 offset:16896\n"
"ds_read_b64_tr_b16 v[212:213], v112 offset:18432\n"
"ds_read_b64_tr_b16 v[214:215], v112 offset:18944\n"
"ds_read_b64_tr_b16 v[216:217], v113 offset:16384\n"
"ds_read_b64_tr_b16 v[218:219], v113 offset:16896\n"
"ds_read_b64_tr_b16 v[220:221], v113 offset:18432\n"
"ds_read_b64_tr_b16 v[222:223], v113 offset:18944\n"
"s_add_u32 m0, s70, 12288\n"
"s_mov_b32 s64, 98304\n"
"buffer_load_dwordx4 v116, s[52:55], s64 offen lds\n"
"s_add_u32 m0, s70, 13312\n"
"s_mov_b32 s64, 106496\n"
"buffer_load_dwordx4 v116, s[52:55], s64 offen lds\n"
"s_add_u32 m0, s70, 14336\n"
"s_mov_b32 s64, 114688\n"
"buffer_load_dwordx4 v116, s[52:55], s64 offen lds\n"
"s_add_u32 m0, s70, 15360\n"
"s_mov_b32 s64, 122880\n"
"buffer_load_dwordx4 v116, s[52:55], s64 offen lds\n"
"s_waitcnt vmcnt(20) lgkmcnt(0)\n"
"s_barrier\n"
"s_add_u32 m0, s73, 16384\n"
"s_xor_b32 s72, s71, 524288\n"
"buffer_load_dwordx4 v108, s[40:43], s72 offen offset:0 lds\n"
"buffer_load_dwordx4 v109, s[40:43], s72 offen offset:1024 lds\n"
"buffer_load_dwordx4 v110, s[40:43], s72 offen offset:2048 lds\n"
"buffer_load_dwordx4 v111, s[40:43], s72 offen offset:3072 lds\n"
"ds_read_b64_tr_b16 v[224:225], v112 offset:32768\n"
"ds_read_b64_tr_b16 v[226:227], v112 offset:33280\n"
"ds_read_b64_tr_b16 v[228:229], v112 offset:34816\n"
"ds_read_b64_tr_b16 v[230:231], v112 offset:35328\n"
"ds_read_b64_tr_b16 v[232:233], v113 offset:32768\n"
"ds_read_b64_tr_b16 v[234:235], v113 offset:33280\n"
"ds_read_b64_tr_b16 v[236:237], v113 offset:34816\n"
"ds_read_b64_tr_b16 v[238:239], v113 offset:35328\n"
"s_waitcnt vmcnt(16) lgkmcnt(0)\n"
"s_barrier\n"
"s_add_u32 m0, s73, 32768\n"
"s_xor_b32 s72, s71, 1048576\n"
"buffer_load_dwordx4 v108, s[40:43], s72 offen offset:0 lds\n"
"buffer_load_dwordx4 v109, s[40:43], s72 offen offset:1024 lds\n"
"buffer_load_dwordx4 v110, s[40:43], s72 offen offset:2048 lds\n"
"buffer_load_dwordx4 v111, s[40:43], s72 offen offset:3072 lds\n"
"ds_read_b64_tr_b16 v[240:241], v112 offset:49152\n"
"ds_read_b64_tr_b16 v[242:243], v112 offset:49664\n"
"ds_read_b64_tr_b16 v[244:245], v112 offset:51200\n"
"ds_read_b64_tr_b16 v[246:247], v112 offset:51712\n"
"ds_read_b64_tr_b16 v[248:249], v113 offset:49152\n"
"ds_read_b64_tr_b16 v[250:251], v113 offset:49664\n"
"ds_read_b64_tr_b16 v[252:253], v113 offset:51200\n"
"ds_read_b64_tr_b16 v[254:255], v113 offset:51712\n"
"s_waitcnt vmcnt(12) lgkmcnt(0)\n"
"s_barrier\n"
"s_add_u32 m0, s73, 49152\n"
"s_xor_b32 s72, s71, 1572864\n"
"buffer_load_dwordx4 v108, s[40:43], s72 offen offset:0 lds\n"
"buffer_load_dwordx4 v109, s[40:43], s72 offen offset:1024 lds\n"
"buffer_load_dwordx4 v110, s[40:43], s72 offen offset:2048 lds\n"
"buffer_load_dwordx4 v111, s[40:43], s72 offen offset:3072 lds\n"
"ds_read_b64_tr_b16 a[0:1], v112 offset:0\n"
"ds_read_b64_tr_b16 a[2:3], v112 offset:512\n"
"ds_read_b64_tr_b16 a[4:5], v112 offset:2048\n"
"ds_read_b64_tr_b16 a[6:7], v112 offset:2560\n"
"ds_read_b64_tr_b16 a[8:9], v113 offset:0\n"
"ds_read_b64_tr_b16 a[10:11], v113 offset:512\n"
"ds_read_b64_tr_b16 a[12:13], v113 offset:2048\n"
"ds_read_b64_tr_b16 a[14:15], v113 offset:2560\n"
"s_waitcnt vmcnt(8) lgkmcnt(0)\n"
"s_barrier\n"
"s_add_u32 m0, s73, 0\n"
"s_xor_b32 s72, s71, 2097152\n"
"buffer_load_dwordx4 v108, s[40:43], s72 offen offset:0 lds\n"
"buffer_load_dwordx4 v109, s[40:43], s72 offen offset:1024 lds\n"
"buffer_load_dwordx4 v110, s[40:43], s72 offen offset:2048 lds\n"
"buffer_load_dwordx4 v111, s[40:43], s72 offen offset:3072 lds\n"
"ds_read_b64_tr_b16 a[16:17], v112 offset:16384\n"
"ds_read_b64_tr_b16 a[18:19], v112 offset:16896\n"
"ds_read_b64_tr_b16 a[20:21], v112 offset:18432\n"
"ds_read_b64_tr_b16 a[22:23], v112 offset:18944\n"
"ds_read_b64_tr_b16 a[24:25], v113 offset:16384\n"
"ds_read_b64_tr_b16 a[26:27], v113 offset:16896\n"
"ds_read_b64_tr_b16 a[28:29], v113 offset:18432\n"
"ds_read_b64_tr_b16 a[30:31], v113 offset:18944\n"
"ds_read_b64 v[0:1], v117 offset:0\n"
"ds_read_b64 v[8:9], v117 offset:128\n"
"ds_read_b64 v[2:3], v117 offset:2048\n"
"ds_read_b64 v[10:11], v117 offset:2176\n"
"ds_read_b64 v[4:5], v117 offset:512\n"
"ds_read_b64 v[12:13], v117 offset:640\n"
"ds_read_b64 v[6:7], v117 offset:2560\n"
"ds_read_b64 v[14:15], v117 offset:2688\n"
"s_waitcnt lgkmcnt(0)\n"
"ds_read_b64 v[16:17], v117 offset:4096\n"
"ds_read_b64 v[24:25], v117 offset:4224\n"
"ds_read_b64 v[18:19], v117 offset:6144\n"
"ds_read_b64 v[26:27], v117 offset:6272\n"
"ds_read_b64 v[20:21], v117 offset:4608\n"
"ds_read_b64 v[28:29], v117 offset:4736\n"
"ds_read_b64 v[22:23], v117 offset:6656\n"
"ds_read_b64 v[30:31], v117 offset:6784\n"
"s_waitcnt lgkmcnt(0)\n"
"s_waitcnt vmcnt(8) lgkmcnt(0)\n"
"s_barrier\n"
"s_add_u32 m0, s73, 16384\n"
"s_xor_b32 s72, s71, 2621440\n"
"buffer_load_dwordx4 v108, s[40:43], s72 offen offset:0 lds\n"
"buffer_load_dwordx4 v109, s[40:43], s72 offen offset:1024 lds\n"
"buffer_load_dwordx4 v110, s[40:43], s72 offen offset:2048 lds\n"
"buffer_load_dwordx4 v111, s[40:43], s72 offen offset:3072 lds\n"
"ds_read_b64_tr_b16 a[32:33], v112 offset:32768\n"
"ds_read_b64_tr_b16 a[34:35], v112 offset:33280\n"
"ds_read_b64_tr_b16 a[36:37], v112 offset:34816\n"
"ds_read_b64_tr_b16 a[38:39], v112 offset:35328\n"
"ds_read_b64_tr_b16 a[40:41], v113 offset:32768\n"
"ds_read_b64_tr_b16 a[42:43], v113 offset:33280\n"
"ds_read_b64_tr_b16 a[44:45], v113 offset:34816\n"
"ds_read_b64_tr_b16 a[46:47], v113 offset:35328\n"
"ds_read_b64 v[32:33], v117 offset:8192\n"
"ds_read_b64 v[40:41], v117 offset:8320\n"
"ds_read_b64 v[34:35], v117 offset:10240\n"
"ds_read_b64 v[42:43], v117 offset:10368\n"
"ds_read_b64 v[36:37], v117 offset:8704\n"
"ds_read_b64 v[44:45], v117 offset:8832\n"
"ds_read_b64 v[38:39], v117 offset:10752\n"
"ds_read_b64 v[46:47], v117 offset:10880\n"
"s_waitcnt lgkmcnt(0)\n"
"ds_read_b64 v[48:49], v117 offset:12288\n"
"ds_read_b64 v[56:57], v117 offset:12416\n"
"ds_read_b64 v[50:51], v117 offset:14336\n"
"ds_read_b64 v[58:59], v117 offset:14464\n"
"ds_read_b64 v[52:53], v117 offset:12800\n"
"ds_read_b64 v[60:61], v117 offset:12928\n"
"ds_read_b64 v[54:55], v117 offset:14848\n"
"ds_read_b64 v[62:63], v117 offset:14976\n"
"s_waitcnt lgkmcnt(0)\n"
"s_waitcnt vmcnt(8) lgkmcnt(0)\n"
"s_barrier\n"
"s_add_u32 m0, s73, 32768\n"
"s_xor_b32 s72, s71, 3145728\n"
"buffer_load_dwordx4 v108, s[40:43], s72 offen offset:0 lds\n"
"buffer_load_dwordx4 v109, s[40:43], s72 offen offset:1024 lds\n"
"buffer_load_dwordx4 v110, s[40:43], s72 offen offset:2048 lds\n"
"buffer_load_dwordx4 v111, s[40:43], s72 offen offset:3072 lds\n"
"ds_read_b64_tr_b16 a[48:49], v112 offset:49152\n"
"ds_read_b64_tr_b16 a[50:51], v112 offset:49664\n"
"ds_read_b64_tr_b16 a[52:53], v112 offset:51200\n"
"ds_read_b64_tr_b16 a[54:55], v112 offset:51712\n"
"ds_read_b64_tr_b16 a[56:57], v113 offset:49152\n"
"ds_read_b64_tr_b16 a[58:59], v113 offset:49664\n"
"ds_read_b64_tr_b16 a[60:61], v113 offset:51200\n"
"ds_read_b64_tr_b16 a[62:63], v113 offset:51712\n"
"s_mov_b32 s64, 84148480\n"
"v_perm_b32 v80, v2, v0, s64\n"
"v_perm_b32 v81, v6, v4, s64\n"
"v_perm_b32 v82, v10, v8, s64\n"
"v_perm_b32 v83, v14, v12, s64\n"
"v_perm_b32 v84, v18, v16, s64\n"
"v_perm_b32 v85, v22, v20, s64\n"
"v_perm_b32 v86, v26, v24, s64\n"
"v_perm_b32 v87, v30, v28, s64\n"
"ds_write_b128 v127, v[80:83] offset:0\n"
"ds_write_b128 v127, v[84:87] offset:1024\n"
"s_waitcnt vmcnt(8) lgkmcnt(0)\n"
"s_barrier\n"
"s_add_u32 m0, s73, 49152\n"
"s_xor_b32 s72, s71, 3670016\n"
"buffer_load_dwordx4 v108, s[40:43], s72 offen offset:0 lds\n"
"buffer_load_dwordx4 v109, s[40:43], s72 offen offset:1024 lds\n"
"buffer_load_dwordx4 v110, s[40:43], s72 offen offset:2048 lds\n"
"buffer_load_dwordx4 v111, s[40:43], s72 offen offset:3072 lds\n"
"ds_read_b64_tr_b16 a[64:65], v112 offset:0\n"
"ds_read_b64_tr_b16 a[66:67], v112 offset:512\n"
"ds_read_b64_tr_b16 a[68:69], v112 offset:2048\n"
"ds_read_b64_tr_b16 a[70:71], v112 offset:2560\n"
"ds_read_b64_tr_b16 a[72:73], v113 offset:0\n"
"ds_read_b64_tr_b16 a[74:75], v113 offset:512\n"
"ds_read_b64_tr_b16 a[76:77], v113 offset:2048\n"
"ds_read_b64_tr_b16 a[78:79], v113 offset:2560\n"
"s_mov_b32 s64, 117834498\n"
"v_perm_b32 v80, v2, v0, s64\n"
"v_perm_b32 v81, v6, v4, s64\n"
"v_perm_b32 v82, v10, v8, s64\n"
"v_perm_b32 v83, v14, v12, s64\n"
"v_perm_b32 v84, v18, v16, s64\n"
"v_perm_b32 v85, v22, v20, s64\n"
"v_perm_b32 v86, v26, v24, s64\n"
"v_perm_b32 v87, v30, v28, s64\n"
"ds_write_b128 v127, v[80:83] offset:2048\n"
"ds_write_b128 v127, v[84:87] offset:3072\n"
"s_waitcnt vmcnt(8) lgkmcnt(0)\n"
"s_barrier\n"
"s_add_u32 m0, s73, 0\n"
"s_xor_b32 s72, s71, 0\n"
"buffer_load_dwordx4 v108, s[44:47], s72 offen offset:0 lds\n"
"buffer_load_dwordx4 v109, s[44:47], s72 offen offset:1024 lds\n"
"buffer_load_dwordx4 v110, s[44:47], s72 offen offset:2048 lds\n"
"buffer_load_dwordx4 v111, s[44:47], s72 offen offset:3072 lds\n"
"ds_read_b64_tr_b16 a[80:81], v112 offset:16384\n"
"ds_read_b64_tr_b16 a[82:83], v112 offset:16896\n"
"ds_read_b64_tr_b16 a[84:85], v112 offset:18432\n"
"ds_read_b64_tr_b16 a[86:87], v112 offset:18944\n"
"ds_read_b64_tr_b16 a[88:89], v113 offset:16384\n"
"ds_read_b64_tr_b16 a[90:91], v113 offset:16896\n"
"ds_read_b64_tr_b16 a[92:93], v113 offset:18432\n"
"ds_read_b64_tr_b16 a[94:95], v113 offset:18944\n"
"s_mov_b32 s64, 84148480\n"
"v_perm_b32 v80, v3, v1, s64\n"
"v_perm_b32 v81, v7, v5, s64\n"
"v_perm_b32 v82, v11, v9, s64\n"
"v_perm_b32 v83, v15, v13, s64\n"
"v_perm_b32 v84, v19, v17, s64\n"
"v_perm_b32 v85, v23, v21, s64\n"
"v_perm_b32 v86, v27, v25, s64\n"
"v_perm_b32 v87, v31, v29, s64\n"
"ds_write_b128 v127, v[80:83] offset:4096\n"
"ds_write_b128 v127, v[84:87] offset:5120\n"
"s_waitcnt vmcnt(8) lgkmcnt(0)\n"
"s_barrier\n"
"s_add_u32 m0, s73, 16384\n"
"s_xor_b32 s72, s71, 524288\n"
"buffer_load_dwordx4 v108, s[44:47], s72 offen offset:0 lds\n"
"buffer_load_dwordx4 v109, s[44:47], s72 offen offset:1024 lds\n"
"buffer_load_dwordx4 v110, s[44:47], s72 offen offset:2048 lds\n"
"buffer_load_dwordx4 v111, s[44:47], s72 offen offset:3072 lds\n"
"ds_read_b64_tr_b16 a[96:97], v112 offset:32768\n"
"ds_read_b64_tr_b16 a[98:99], v112 offset:33280\n"
"ds_read_b64_tr_b16 a[100:101], v112 offset:34816\n"
"ds_read_b64_tr_b16 a[102:103], v112 offset:35328\n"
"ds_read_b64_tr_b16 a[104:105], v113 offset:32768\n"
"ds_read_b64_tr_b16 a[106:107], v113 offset:33280\n"
"ds_read_b64_tr_b16 a[108:109], v113 offset:34816\n"
"ds_read_b64_tr_b16 a[110:111], v113 offset:35328\n"
"s_mov_b32 s64, 117834498\n"
"v_perm_b32 v80, v3, v1, s64\n"
"v_perm_b32 v81, v7, v5, s64\n"
"v_perm_b32 v82, v11, v9, s64\n"
"v_perm_b32 v83, v15, v13, s64\n"
"v_perm_b32 v84, v19, v17, s64\n"
"v_perm_b32 v85, v23, v21, s64\n"
"v_perm_b32 v86, v27, v25, s64\n"
"v_perm_b32 v87, v31, v29, s64\n"
"ds_write_b128 v127, v[80:83] offset:6144\n"
"ds_write_b128 v127, v[84:87] offset:7168\n"
"s_waitcnt vmcnt(8) lgkmcnt(0)\n"
"s_barrier\n"
"s_add_u32 m0, s73, 32768\n"
"s_xor_b32 s72, s71, 1048576\n"
"buffer_load_dwordx4 v108, s[44:47], s72 offen offset:0 lds\n"
"buffer_load_dwordx4 v109, s[44:47], s72 offen offset:1024 lds\n"
"buffer_load_dwordx4 v110, s[44:47], s72 offen offset:2048 lds\n"
"buffer_load_dwordx4 v111, s[44:47], s72 offen offset:3072 lds\n"
"ds_read_b64_tr_b16 a[112:113], v112 offset:49152\n"
"ds_read_b64_tr_b16 a[114:115], v112 offset:49664\n"
"ds_read_b64_tr_b16 a[116:117], v112 offset:51200\n"
"ds_read_b64_tr_b16 a[118:119], v112 offset:51712\n"
"ds_read_b64_tr_b16 a[120:121], v113 offset:49152\n"
"ds_read_b64_tr_b16 a[122:123], v113 offset:49664\n"
"ds_read_b64_tr_b16 a[124:125], v113 offset:51200\n"
"ds_read_b64_tr_b16 a[126:127], v113 offset:51712\n"
"s_mov_b32 s64, 84148480\n"
"v_perm_b32 v80, v34, v32, s64\n"
"v_perm_b32 v81, v38, v36, s64\n"
"v_perm_b32 v82, v42, v40, s64\n"
"v_perm_b32 v83, v46, v44, s64\n"
"v_perm_b32 v84, v50, v48, s64\n"
"v_perm_b32 v85, v54, v52, s64\n"
"v_perm_b32 v86, v58, v56, s64\n"
"v_perm_b32 v87, v62, v60, s64\n"
"ds_write_b128 v127, v[80:83] offset:8192\n"
"ds_write_b128 v127, v[84:87] offset:9216\n"
"s_waitcnt vmcnt(8) lgkmcnt(0)\n"
"s_barrier\n"
"s_add_u32 m0, s73, 49152\n"
"s_xor_b32 s72, s71, 1572864\n"
"buffer_load_dwordx4 v108, s[44:47], s72 offen offset:0 lds\n"
"buffer_load_dwordx4 v109, s[44:47], s72 offen offset:1024 lds\n"
"buffer_load_dwordx4 v110, s[44:47], s72 offen offset:2048 lds\n"
"buffer_load_dwordx4 v111, s[44:47], s72 offen offset:3072 lds\n"
"ds_read_b64_tr_b16 a[128:129], v112 offset:0\n"
"ds_read_b64_tr_b16 a[130:131], v112 offset:512\n"
"ds_read_b64_tr_b16 a[132:133], v112 offset:2048\n"
"ds_read_b64_tr_b16 a[134:135], v112 offset:2560\n"
"ds_read_b64_tr_b16 a[136:137], v113 offset:0\n"
"ds_read_b64_tr_b16 a[138:139], v113 offset:512\n"
"ds_read_b64_tr_b16 a[140:141], v113 offset:2048\n"
"ds_read_b64_tr_b16 a[142:143], v113 offset:2560\n"
"s_mov_b32 s64, 117834498\n"
"v_perm_b32 v80, v34, v32, s64\n"
"v_perm_b32 v81, v38, v36, s64\n"
"v_perm_b32 v82, v42, v40, s64\n"
"v_perm_b32 v83, v46, v44, s64\n"
"v_perm_b32 v84, v50, v48, s64\n"
"v_perm_b32 v85, v54, v52, s64\n"
"v_perm_b32 v86, v58, v56, s64\n"
"v_perm_b32 v87, v62, v60, s64\n"
"ds_write_b128 v127, v[80:83] offset:10240\n"
"ds_write_b128 v127, v[84:87] offset:11264\n"
"s_waitcnt vmcnt(8) lgkmcnt(0)\n"
"s_barrier\n"
"s_add_u32 m0, s73, 0\n"
"s_xor_b32 s72, s71, 2097152\n"
"buffer_load_dwordx4 v108, s[44:47], s72 offen offset:0 lds\n"
"buffer_load_dwordx4 v109, s[44:47], s72 offen offset:1024 lds\n"
"buffer_load_dwordx4 v110, s[44:47], s72 offen offset:2048 lds\n"
"buffer_load_dwordx4 v111, s[44:47], s72 offen offset:3072 lds\n"
"ds_read_b64_tr_b16 a[144:145], v112 offset:16384\n"
"ds_read_b64_tr_b16 a[146:147], v112 offset:16896\n"
"ds_read_b64_tr_b16 a[148:149], v112 offset:18432\n"
"ds_read_b64_tr_b16 a[150:151], v112 offset:18944\n"
"ds_read_b64_tr_b16 a[152:153], v113 offset:16384\n"
"ds_read_b64_tr_b16 a[154:155], v113 offset:16896\n"
"ds_read_b64_tr_b16 a[156:157], v113 offset:18432\n"
"ds_read_b64_tr_b16 a[158:159], v113 offset:18944\n"
"s_mov_b32 s64, 84148480\n"
"v_perm_b32 v80, v35, v33, s64\n"
"v_perm_b32 v81, v39, v37, s64\n"
"v_perm_b32 v82, v43, v41, s64\n"
"v_perm_b32 v83, v47, v45, s64\n"
"v_perm_b32 v84, v51, v49, s64\n"
"v_perm_b32 v85, v55, v53, s64\n"
"v_perm_b32 v86, v59, v57, s64\n"
"v_perm_b32 v87, v63, v61, s64\n"
"ds_write_b128 v127, v[80:83] offset:12288\n"
"ds_write_b128 v127, v[84:87] offset:13312\n"
"s_waitcnt vmcnt(8) lgkmcnt(0)\n"
"s_barrier\n"
"s_add_u32 m0, s73, 16384\n"
"s_xor_b32 s72, s71, 2621440\n"
"buffer_load_dwordx4 v108, s[44:47], s72 offen offset:0 lds\n"
"buffer_load_dwordx4 v109, s[44:47], s72 offen offset:1024 lds\n"
"buffer_load_dwordx4 v110, s[44:47], s72 offen offset:2048 lds\n"
"buffer_load_dwordx4 v111, s[44:47], s72 offen offset:3072 lds\n"
"ds_read_b64_tr_b16 a[160:161], v112 offset:32768\n"
"ds_read_b64_tr_b16 a[162:163], v112 offset:33280\n"
"ds_read_b64_tr_b16 a[164:165], v112 offset:34816\n"
"ds_read_b64_tr_b16 a[166:167], v112 offset:35328\n"
"ds_read_b64_tr_b16 a[168:169], v113 offset:32768\n"
"ds_read_b64_tr_b16 a[170:171], v113 offset:33280\n"
"ds_read_b64_tr_b16 a[172:173], v113 offset:34816\n"
"ds_read_b64_tr_b16 a[174:175], v113 offset:35328\n"
"s_mov_b32 s64, 117834498\n"
"v_perm_b32 v80, v35, v33, s64\n"
"v_perm_b32 v81, v39, v37, s64\n"
"v_perm_b32 v82, v43, v41, s64\n"
"v_perm_b32 v83, v47, v45, s64\n"
"v_perm_b32 v84, v51, v49, s64\n"
"v_perm_b32 v85, v55, v53, s64\n"
"v_perm_b32 v86, v59, v57, s64\n"
"v_perm_b32 v87, v63, v61, s64\n"
"ds_write_b128 v127, v[80:83] offset:14336\n"
"ds_write_b128 v127, v[84:87] offset:15360\n"
"s_waitcnt vmcnt(8) lgkmcnt(0)\n"
"s_barrier\n"
"s_add_u32 m0, s73, 32768\n"
"s_xor_b32 s72, s71, 3145728\n"
"buffer_load_dwordx4 v108, s[44:47], s72 offen offset:0 lds\n"
"buffer_load_dwordx4 v109, s[44:47], s72 offen offset:1024 lds\n"
"buffer_load_dwordx4 v110, s[44:47], s72 offen offset:2048 lds\n"
"buffer_load_dwordx4 v111, s[44:47], s72 offen offset:3072 lds\n"
"ds_read_b64_tr_b16 a[176:177], v112 offset:49152\n"
"ds_read_b64_tr_b16 a[178:179], v112 offset:49664\n"
"ds_read_b64_tr_b16 a[180:181], v112 offset:51200\n"
"ds_read_b64_tr_b16 a[182:183], v112 offset:51712\n"
"ds_read_b64_tr_b16 a[184:185], v113 offset:49152\n"
"ds_read_b64_tr_b16 a[186:187], v113 offset:49664\n"
"ds_read_b64_tr_b16 a[188:189], v113 offset:51200\n"
"ds_read_b64_tr_b16 a[190:191], v113 offset:51712\n"
"v_lshlrev_b32 v100, 3, v119\n"
"s_mov_b32 s74, 3146752\n"
"buffer_load_dwordx2 v[96:97], v100, s[60:63], s74 offen sc1\n"
"s_waitcnt vmcnt(9) lgkmcnt(0)\n"
"s_barrier\n"
"s_add_u32 m0, s73, 49152\n"
"s_xor_b32 s72, s71, 3670016\n"
"buffer_load_dwordx4 v108, s[44:47], s72 offen offset:0 lds\n"
"buffer_load_dwordx4 v109, s[44:47], s72 offen offset:1024 lds\n"
"buffer_load_dwordx4 v110, s[44:47], s72 offen offset:2048 lds\n"
"buffer_load_dwordx4 v111, s[44:47], s72 offen offset:3072 lds\n"
"ds_read_b64_tr_b16 a[192:193], v112 offset:0\n"
"ds_read_b64_tr_b16 a[194:195], v112 offset:512\n"
"ds_read_b64_tr_b16 a[196:197], v112 offset:2048\n"
"ds_read_b64_tr_b16 a[198:199], v112 offset:2560\n"
"ds_read_b64_tr_b16 a[200:201], v113 offset:0\n"
"ds_read_b64_tr_b16 a[202:203], v113 offset:512\n"
"ds_read_b64_tr_b16 a[204:205], v113 offset:2048\n"
"ds_read_b64_tr_b16 a[206:207], v113 offset:2560\n"
"s_waitcnt vmcnt(9) lgkmcnt(0)\n"
"s_barrier\n"
"ds_read_b64_tr_b16 a[208:209], v112 offset:16384\n"
"ds_read_b64_tr_b16 a[210:211], v112 offset:16896\n"
"ds_read_b64_tr_b16 a[212:213], v112 offset:18432\n"
"ds_read_b64_tr_b16 a[214:215], v112 offset:18944\n"
"ds_read_b64_tr_b16 a[216:217], v113 offset:16384\n"
"ds_read_b64_tr_b16 a[218:219], v113 offset:16896\n"
"ds_read_b64_tr_b16 a[220:221], v113 offset:18432\n"
"ds_read_b64_tr_b16 a[222:223], v113 offset:18944\n"
"s_waitcnt vmcnt(5) lgkmcnt(0)\n"
"s_barrier\n"
"ds_read_b64_tr_b16 a[224:225], v112 offset:32768\n"
"ds_read_b64_tr_b16 a[226:227], v112 offset:33280\n"
"ds_read_b64_tr_b16 a[228:229], v112 offset:34816\n"
"ds_read_b64_tr_b16 a[230:231], v112 offset:35328\n"
"ds_read_b64_tr_b16 a[232:233], v113 offset:32768\n"
"ds_read_b64_tr_b16 a[234:235], v113 offset:33280\n"
"ds_read_b64_tr_b16 a[236:237], v113 offset:34816\n"
"ds_read_b64_tr_b16 a[238:239], v113 offset:35328\n"
"s_waitcnt vmcnt(4)\n"
"s_mov_b32 s78, 16384\n"
".Law10_%=:\n"
"v_xor_b32 v96, s34, v96\n"
"v_xor_b32 v97, s35, v97\n"
"v_and_or_b32 v96, v97, -4, v96\n"
"v_cmp_ne_u32 vcc, 0, v96\n"
"s_cbranch_vccz .Laok11_%=\n"
"s_sub_u32 s78, s78, 1\n"
"s_cbranch_scc1 .Lfail5_%=\n"
"s_sleep 8\n"
"buffer_load_dwordx2 v[96:97], v100, s[60:63], s74 offen sc1\n"
"s_waitcnt vmcnt(0)\n"
"s_branch .Law10_%=\n"
".Laok11_%=:\n"
"s_mov_b32 s77, 0\n"
"v_cmp_lt_u32 vcc, 1, v97\n"
"v_and_b32 v97, 1, v97\n"
"s_and_b32 s64, vcc_lo, 0xf00ff00f\n"
"s_and_b32 s65, vcc_hi, 0xf00ff00f\n"
"s_bcnt1_i32_b64 s64, s[64:65]\n"
"s_cmp_ge_u32 s64, 16\n"
"s_addc_u32 s77, s77, s77\n"
"s_and_b32 s64, vcc_lo, 0xff00ff0\n"
"s_and_b32 s65, vcc_hi, 0xff00ff0\n"
"s_bcnt1_i32_b64 s64, s[64:65]\n"
"s_cmp_ge_u32 s64, 16\n"
"s_addc_u32 s77, s77, s77\n"
"v_cmp_ne_u32 vcc, 0, v97\n"
"s_nop 0\n"
"s_and_b32 s64, vcc_lo, 0xffffffff\n"
"s_and_b32 s65, vcc_hi, 0xffffffff\n"
"s_bcnt1_i32_b64 s64, s[64:65]\n"
"s_cmp_ge_u32 s64, 32\n"
"s_addc_u32 s77, s77, s77\n"
"s_and_b32 s64, s36, 1\n"
"s_lshl_b32 s64, s64, 1\n"
"s_bfe_u32 s65, s36, 0x10001\n"
"s_or_b32 s64, s64, s65\n"
"s_xor_b32 s64, s64, s32\n"
"s_lshr_b32 s65, s77, 1\n"
"s_xor_b32 s33, s64, s65\n"
"s_lshr_b32 s64, s36, 3\n"
"s_lshl_b32 s64, s64, 2\n"
"s_or_b32 s64, s64, s33\n"
"s_lshr_b32 s65, s64, 1\n"
"s_lshl_b32 s65, s65, 14\n"
"s_and_b32 s79, s64, 1\n"
"s_lshl_b32 s79, s79, 12\n"
"s_add_u32 s79, s79, s65\n"
"v_bfe_u32 v12, v125, 4, 3\n"
"v_and_b32 v13, 1, v12\n"
"v_lshlrev_b32 v13, 4, v13\n"
"v_mov_b32 v14, 0x3f80\n"
"v_lshlrev_b32 v13, v13, v14\n"
"v_lshrrev_b32 v14, 1, v12\n"
"v_cmp_eq_u32 vcc, 0, v14\n"
"s_nop 1\n"
"v_cndmask_b32 v8, 0, v13, vcc\n"
"v_cmp_eq_u32 vcc, 1, v14\n"
"s_nop 1\n"
"v_cndmask_b32 v9, 0, v13, vcc\n"
"v_cmp_eq_u32 vcc, 2, v14\n"
"s_nop 1\n"
"v_cndmask_b32 v10, 0, v13, vcc\n"
"v_cmp_eq_u32 vcc, 3, v14\n"
"s_nop 1\n"
"v_cndmask_b32 v11, 0, v13, vcc\n"
"v_lshrrev_b32 v13, 2, v12\n"
"v_lshlrev_b32 v13, 6, v13\n"
"v_and_b32 v12, 3, v12\n"
"v_lshl_add_u32 v12, v12, 3, v13\n"
"v_bfe_u32 v13, v125, 8, 1\n"
"v_lshl_add_u32 v12, v13, 5, v12\n"
"v_and_b32 v13, 0x280, v125\n"
"v_lshl_add_u32 v12, v13, 5, v12\n"
"s_and_b32 s65, s64, 31\n"
"s_lshl_b32 s65, s65, 7\n"
"v_add_u32 v12, s65, v12\n"
"s_lshr_b32 s65, s64, 5\n"
"s_lshl_b32 s65, s65, 15\n"
"v_add_u32 v12, s65, v12\n"
"ds_write_b128 v125, v[8:11] offset:16640\n"
"ds_write_b32 v125, v12 offset:17664\n"
"s_lshl_b32 s13, s64, 4\n"
"s_mov_b32 s69, s64\n"
"s_andn2_b32 s12, 1, s77\n"
"s_lshl_b32 s12, s12, 13\n"
"s_add_u32 s12, s12, 2097152\n"
"s_add_u32 s13, s13, s12\n"
"s_mov_b32 s76, 0\n"
"s_and_b32 s96, s77, 1\n"
"s_lshl_b32 s96, s96, 13\n"
"s_add_u32 s77, s96, 1048576\n"
"s_and_b32 s64, s33, 2\n"
"s_lshl_b32 s64, s64, 13\n"
"s_and_b32 s65, s33, 1\n"
"s_xor_b32 s65, s65, 1\n"
"s_lshl_b32 s65, s65, 12\n"
"s_or_b32 s33, s64, s65\n"
"s_waitcnt vmcnt(0) lgkmcnt(0)\n"
"s_barrier\n"
"ds_read_b64_tr_b16 a[240:241], v112 offset:49152\n"
"ds_read_b64_tr_b16 a[242:243], v112 offset:49664\n"
"ds_read_b64_tr_b16 a[244:245], v112 offset:51200\n"
"ds_read_b64_tr_b16 a[246:247], v112 offset:51712\n"
"ds_read_b64_tr_b16 a[248:249], v113 offset:49152\n"
"ds_read_b64_tr_b16 a[250:251], v113 offset:49664\n"
"ds_read_b64_tr_b16 a[252:253], v113 offset:51200\n"
"ds_read_b64_tr_b16 a[254:255], v113 offset:51712\n"
"s_mov_b32 s101, 1\n"
"s_mov_b32 s71, s100\n"
"buffer_load_dwordx2 v[30:31], off, s[60:63], s71 sc1\n"
"v_mov_b32 v80, 0\n"
"v_mov_b32 v81, 0\n"
"v_mov_b32 v82, 0\n"
"v_mov_b32 v83, 0\n"
"v_mov_b32 v84, 0\n"
"v_mov_b32 v85, 0\n"
"v_mov_b32 v86, 0\n"
"v_mov_b32 v87, 0\n"
"v_mov_b32 v88, 0\n"
"v_mov_b32 v89, 0\n"
"v_mov_b32 v90, 0\n"
"v_mov_b32 v91, 0\n"
"v_mov_b32 v92, 0\n"
"v_mov_b32 v93, 0\n"
"v_mov_b32 v94, 0\n"
"v_mov_b32 v95, 0\n"
"v_mov_b32 v96, 0\n"
"v_mov_b32 v97, 0\n"
"v_mov_b32 v98, 0\n"
"v_mov_b32 v99, 0\n"
"v_mov_b32 v100, 0\n"
"v_mov_b32 v101, 0\n"
"v_mov_b32 v102, 0\n"
"v_mov_b32 v103, 0\n"
"v_mov_b32 v116, 0\n"
"s_waitcnt lgkmcnt(0)\n"
"s_barrier\n"
"s_waitcnt vmcnt(0)\n"
"v_readfirstlane_b32 s64, v30\n"
"v_readfirstlane_b32 s65, v31\n"
"s_nop 0\n"
"s_sub_u32 s65, s65, s35\n"
"s_cmp_le_u32 s65, 64\n"
"s_cselect_b32 s65, s65, 0\n"
"s_cmp_eq_u32 s64, s34\n"
"s_cselect_b32 s65, s65, 0\n"
"s_cmp_ge_u32 s65, 1\n"
"s_cbranch_scc1 .Lkv012_%=\n"
"s_mov_b32 s78, 16384\n"
".Lsw13_%=:\n"
"buffer_load_dwordx2 v[30:31], off, s[60:63], s71 sc1\n"
"s_waitcnt vmcnt(0)\n"
"v_readfirstlane_b32 s64, v30\n"
"v_readfirstlane_b32 s65, v31\n"
"s_nop 0\n"
"s_sub_u32 s65, s65, s35\n"
"s_cmp_le_u32 s65, 64\n"
"s_cselect_b32 s65, s65, 0\n"
"s_cmp_eq_u32 s64, s34\n"
"s_cselect_b32 s65, s65, 0\n"
"s_cmp_ge_u32 s65, s101\n"
"s_cbranch_scc1 .Lswok14_%=\n"
"s_sub_u32 s78, s78, 1\n"
"s_cbranch_scc1 .Lfail5_%=\n"
"s_sleep 2\n"
"s_branch .Lsw13_%=\n"
".Lswok14_%=:\n"
".Lkv012_%=:\n"
"s_and_b32 s64, s76, 7\n"
"s_lshl_b32 s65, s76, 12\n"
"s_xor_b32 s65, s65, s77\n"
"s_and_b32 s65, s65, 8192\n"
"s_lshl_b32 s64, s64, 20\n"
"s_add_u32 s64, s64, s65\n"
"s_add_u32 s64, s64, s97\n"
"s_add_u32 s65, s64, 16384\n"
"buffer_load_dwordx4 v[104:107], v125, s[60:63], s64 offen offset:0 sc1\n"
"buffer_load_dwordx4 v[108:111], v125, s[60:63], s64 offen offset:1024 sc1\n"
"v_lshrrev_b32 v118, 1, v125\n"
"buffer_load_dwordx2 v[64:65], v118, s[60:63], s65 offen offset:0 sc1\n"
"buffer_load_dwordx2 v[68:69], v118, s[60:63], s65 offen offset:512 sc1\n"
"buffer_load_dwordx2 v[72:73], v118, s[60:63], s65 offen offset:1024 sc1\n"
"buffer_load_dwordx2 v[76:77], v118, s[60:63], s65 offen offset:1536 sc1\n"
"v_mov_b32 v112, -1\n"
"v_mov_b32 v113, -1\n"
"v_mov_b32 v114, -1\n"
"v_mov_b32 v115, -1\n"
"ds_read_b32 v24, v125 offset:17664\n"
"s_add_u32 s75, s77, 131072\n"
"s_xor_b32 s14, s79, 540672\n"
"s_add_u32 s14, s14, s96\n"
"v_bfe_u32 v8, v125, 5, 3\n"
"v_lshlrev_b32 v8, 10, v8\n"
"v_lshrrev_b32 v9, 8, v125\n"
"v_lshl_add_u32 v8, v9, 5, v8\n"
"v_and_b32 v9, 16, v125\n"
"v_add_u32 v118, v8, v9\n"
"s_add_u32 s71, s100, 128\n"
"s_mov_b32 s78, 16384\n"
"s_mov_b32 s15, 880\n"
"s_cmp_eq_u32 s37, 1\n"
"s_cbranch_scc1 .Lent16_%=\n"
"s_cmp_eq_u32 s37, 2\n"
"s_cbranch_scc1 .Lent17_%=\n"
"s_cmp_eq_u32 s37, 3\n"
"s_cbranch_scc1 .Lent18_%=\n"
".Lent15_%=:\n"
"s_lshl_b32 s64, s39, 9\n"
"s_xor_b32 s65, s64, 0\n"
"buffer_load_dwordx4 v[0:3], v118, s[56:59], s65 offen\n"
"s_xor_b32 s65, s64, 128\n"
"buffer_load_dwordx4 v[4:7], v118, s[56:59], s65 offen\n"
"s_waitcnt lgkmcnt(0)\n"
"s_branch .LphA20_%=\n"
".Lretry21_%=:\n"
"s_sub_u32 s78, s78, 1\n"
"s_cbranch_scc1 .Lfail5_%=\n"
"s_waitcnt lgkmcnt(0)\n"
"buffer_load_dwordx4 v[0:3], v125, s[60:63], s94 offen sc1\n"
"buffer_load_dwordx4 v[4:7], v125, s[60:63], s95 offen sc1\n"
"s_nop 4\n"
"v_mov_b32 v80, 0\n"
"v_mov_b32 v81, 0\n"
"v_mov_b32 v82, 0\n"
"v_mov_b32 v83, 0\n"
"v_mov_b32 v84, 0\n"
"v_mov_b32 v85, 0\n"
"v_mov_b32 v86, 0\n"
"v_mov_b32 v87, 0\n"
"v_mov_b32 v88, 0\n"
"v_mov_b32 v89, 0\n"
"v_mov_b32 v90, 0\n"
"v_mov_b32 v91, 0\n"
"v_mov_b32 v92, 0\n"
"v_mov_b32 v93, 0\n"
"v_mov_b32 v94, 0\n"
"v_mov_b32 v95, 0\n"
"v_mov_b32 v96, 0\n"
"v_mov_b32 v97, 0\n"
"v_mov_b32 v98, 0\n"
"v_mov_b32 v99, 0\n"
"v_mov_b32 v100, 0\n"
"v_mov_b32 v101, 0\n"
"v_mov_b32 v102, 0\n"
"v_mov_b32 v103, 0\n"
"v_mov_b32 v116, 0\n"
"s_branch .LphA20_%=\n"
".p2align 6\n"
".Ltop19_%=:\n"
"s_barrier\n"
"buffer_load_dwordx4 v[0:3], v125, s[60:63], s94 offen sc1\n"
"s_nop 4\n"
"buffer_load_dwordx4 v[4:7], v125, s[60:63], s95 offen sc1\n"
".LphA20_%=:\n"
"v_mov_b32 v78, v24\n"
"s_waitcnt vmcnt(2)\n"
"ds_write_b128 v125, v[104:107] offset:0\n"
"ds_write_b128 v125, v[108:111] offset:1024\n"
"s_waitcnt vmcnt(1)\n"
"v_smfmac_f32_16x16x64_bf16 v[80:83], v[0:3], a[0:7], v124\n"
"ds_write_b128 v125, v[0:3] offset:19456\n"
"v_smfmac_f32_16x16x64_bf16 v[84:87], v[0:3], a[8:15], v124\n"
"v_max3_u32 v116, v0, v2, v116\n"
"v_smfmac_f32_16x16x64_bf16 v[88:91], v[0:3], a[128:135], v124\n"
"v_smfmac_f32_16x16x64_bf16 v[92:95], v[0:3], a[136:143], v124\n"
"s_waitcnt vmcnt(0)\n"
"ds_write_b128 v125, v[4:7] offset:20480\n"
"v_smfmac_f32_16x16x64_bf16 v[80:83], v[4:7], a[16:23], v124\n"
"v_max3_u32 v116, v4, v6, v116\n"
"v_smfmac_f32_16x16x64_bf16 v[84:87], v[4:7], a[24:31], v124\n"
"v_cmp_eq_u32 vcc, -1, v116\n"
"v_smfmac_f32_16x16x64_bf16 v[88:91], v[4:7], a[144:151], v124\n"
"s_cbranch_vccnz .Lretry21_%=\n"
"v_smfmac_f32_16x16x64_bf16 v[92:95], v[4:7], a[152:159], v124\n"
"s_waitcnt lgkmcnt(0)\n"
"s_barrier\n"
"ds_read_b128 v[8:11], v125 offset:21504\n"
"v_smfmac_f32_16x16x64_bf16 v[96:99], v[0:3], v[128:135], v124\n"
"s_memtime s[8:9]\n"
"ds_read_b128 v[12:15], v125 offset:22528\n"
"v_smfmac_f32_16x16x64_bf16 v[100:103], v[0:3], v[136:143], v124\n"
"ds_read_b128 v[16:19], v125 offset:23552\n"
"v_smfmac_f32_16x16x64_bf16 v[96:99], v[4:7], v[144:151], v124\n"
"ds_read_b128 v[20:23], v125 offset:24576\n"
"v_smfmac_f32_16x16x64_bf16 v[100:103], v[4:7], v[152:159], v124\n"
"s_waitcnt lgkmcnt(3)\n"
"ds_read_b128 v[24:27], v125 offset:25600\n"
"v_smfmac_f32_16x16x64_bf16 v[80:83], v[8:11], a[32:39], v124\n"
"ds_read_b128 v[28:31], v125 offset:26624\n"
"v_smfmac_f32_16x16x64_bf16 v[84:87], v[8:11], a[40:47], v124\n"
"s_waitcnt lgkmcnt(3)\n"
"ds_read_b128 v[32:35], v125 offset:0\n"
"v_smfmac_f32_16x16x64_bf16 v[80:83], v[12:15], a[48:55], v124\n"
"buffer_store_dwordx4 v[112:115], v125, s[60:63], s14 offen offset:0 sc1\n"
"v_smfmac_f32_16x16x64_bf16 v[84:87], v[12:15], a[56:63], v124\n"
"s_waitcnt lgkmcnt(2)\n"
"ds_read_b128 v[36:39], v125 offset:1024\n"
"v_smfmac_f32_16x16x64_bf16 v[80:83], v[16:19], a[64:71], v124\n"
"ds_read_b128 v[40:43], v125 offset:2048\n"
"v_smfmac_f32_16x16x64_bf16 v[84:87], v[16:19], a[72:79], v124\n"
"s_waitcnt lgkmcnt(3)\n"
"ds_read_b128 v[44:47], v125 offset:3072\n"
"v_smfmac_f32_16x16x64_bf16 v[80:83], v[20:23], a[80:87], v124\n"
"ds_read_b128 v[48:51], v125 offset:4096\n"
"v_mov_b32 v116, 0\n"
"v_smfmac_f32_16x16x64_bf16 v[84:87], v[20:23], a[88:95], v124\n"
"ds_read_b128 v[52:55], v125 offset:5120\n"
"v_mov_b32 v117, 0\n"
"v_smfmac_f32_16x16x64_bf16 v[80:83], v[24:27], a[96:103], v124\n"
"ds_read_b128 v[56:59], v125 offset:6144\n"
"v_mov_b32 v118, 0\n"
"v_smfmac_f32_16x16x64_bf16 v[84:87], v[24:27], a[104:111], v124\n"
"ds_read_b128 v[60:63], v125 offset:7168\n"
"v_mov_b32 v119, 0\n"
"v_smfmac_f32_16x16x64_bf16 v[80:83], v[28:31], a[112:119], v124\n"
"v_smfmac_f32_16x16x64_bf16 v[84:87], v[28:31], a[120:127], v124\n"
"v_smfmac_f32_16x16x64_bf16 v[88:91], v[8:11], a[160:167], v124\n"
"s_mov_b64 vcc, s[66:67]\n"
"v_smfmac_f32_16x16x64_bf16 v[92:95], v[8:11], a[168:175], v124\n"
"v_add_f32 v80, v80, v81\n"
"v_smfmac_f32_16x16x64_bf16 v[88:91], v[12:15], a[176:183], v124\n"
"v_add_f32 v82, v82, v83\n"
"v_smfmac_f32_16x16x64_bf16 v[92:95], v[12:15], a[184:191], v124\n"
"v_add_f32 v84, v84, v85\n"
"v_smfmac_f32_16x16x64_bf16 v[88:91], v[16:19], a[192:199], v124\n"
"v_add_f32 v86, v86, v87\n"
"v_smfmac_f32_16x16x64_bf16 v[92:95], v[16:19], a[200:207], v124\n"
"v_cvt_pk_bf16_f32 v80, v80, v84\n"
"v_cvt_pk_bf16_f32 v82, v82, v86\n"
"v_smfmac_f32_16x16x64_bf16 v[88:91], v[20:23], a[208:215], v124\n"
"ds_write2_b32 v67, v80, v82 offset0:0 offset1:4\n"
"v_smfmac_f32_16x16x64_bf16 v[92:95], v[20:23], a[216:223], v124\n"
"v_smfmac_f32_16x16x64_bf16 v[88:91], v[24:27], a[224:231], v124\n"
"buffer_store_dwordx2 v[112:113], v78, s[60:63], s75 offen sc1\n"
"v_smfmac_f32_16x16x64_bf16 v[92:95], v[24:27], a[232:239], v124\n"
"v_smfmac_f32_16x16x64_bf16 v[88:91], v[28:31], a[240:247], v124\n"
"s_waitcnt lgkmcnt(0)\n"
"s_barrier\n"
"ds_read_b128 v[80:83], v75 offset:0\n"
"v_smfmac_f32_16x16x64_bf16 v[92:95], v[28:31], a[248:255], v124\n"
"ds_read_b128 v[84:87], v75 offset:1600\n"
"v_smfmac_f32_16x16x64_bf16 v[96:99], v[8:11], v[160:167], v124\n"
"ds_read_b128 v[0:3], v75 offset:3200\n"
"v_smfmac_f32_16x16x64_bf16 v[100:103], v[8:11], v[168:175], v124\n"
"ds_read_b128 v[4:7], v75 offset:4800\n"
"v_add_f32 v88, v88, v89\n"
"v_smfmac_f32_16x16x64_bf16 v[96:99], v[12:15], v[176:183], v124\n"
"v_add_f32 v90, v90, v91\n"
"v_smfmac_f32_16x16x64_bf16 v[100:103], v[12:15], v[184:191], v124\n"
"v_add_f32 v92, v92, v93\n"
"v_smfmac_f32_16x16x64_bf16 v[96:99], v[16:19], v[192:199], v124\n"
"v_add_f32 v94, v94, v95\n"
"s_waitcnt lgkmcnt(3)\n"
"v_mfma_f32_16x16x32_bf16 v[104:107], v[32:35], v[80:83], 0\n"
"v_cvt_pk_bf16_f32 v88, v88, v92\n"
"v_cvt_pk_bf16_f32 v90, v90, v94\n"
"v_mfma_f32_16x16x32_bf16 v[108:111], v[36:39], v[80:83], 0\n"
"s_waitcnt lgkmcnt(2)\n"
"ds_write2_b32 v67, v88, v90 offset0:32 offset1:36\n"
"v_mfma_f32_16x16x32_bf16 v[104:107], v[40:43], v[84:87], v[104:107]\n"
"v_mfma_f32_16x16x32_bf16 v[108:111], v[44:47], v[84:87], v[108:111]\n"
"s_waitcnt lgkmcnt(1)\n"
"v_mfma_f32_16x16x32_bf16 v[104:107], v[48:51], v[0:3], v[104:107]\n"
"v_mfma_f32_16x16x32_bf16 v[108:111], v[52:55], v[0:3], v[108:111]\n"
"s_waitcnt lgkmcnt(0)\n"
"s_barrier\n"
"ds_read_b128 v[88:91], v79 offset:0\n"
"v_mfma_f32_16x16x32_bf16 v[104:107], v[56:59], v[4:7], v[104:107]\n"
"ds_read_b128 v[92:95], v79 offset:1600\n"
"v_mfma_f32_16x16x32_bf16 v[108:111], v[60:63], v[4:7], v[108:111]\n"
"ds_read_b128 v[8:11], v79 offset:3200\n"
"v_smfmac_f32_16x16x64_bf16 v[100:103], v[16:19], v[200:207], v124\n"
"ds_read_b128 v[12:15], v79 offset:4800\n"
"v_smfmac_f32_16x16x64_bf16 v[96:99], v[20:23], v[208:215], v124\n"
"ds_read_b128 v[32:35], v127 offset:0\n"
"v_max3_f32 v66, v104, v105, v106\n"
"v_smfmac_f32_16x16x64_bf16 v[100:103], v[20:23], v[216:223], v124\n"
"s_waitcnt lgkmcnt(4)\n"
"ds_read_b128 v[36:39], v127 offset:1024\n"
"v_mov_b32 v16, 0\n"
"v_mfma_f32_16x16x32_bf16 v[112:115], v[88:91], v[80:83], 0\n"
"v_max3_f32 v66, v66, v107, v108\n"
"v_max3_f32 v66, v66, v109, v110\n"
"v_smfmac_f32_16x16x64_bf16 v[96:99], v[24:27], v[224:231], v124\n"
"s_waitcnt lgkmcnt(4)\n"
"ds_read_b128 v[40:43], v127 offset:2048\n"
"v_max_f32 v70, v66, v111\n"
"v_mfma_f32_16x16x32_bf16 v[112:115], v[92:95], v[84:87], v[112:115]\n"
"v_max_f32 v66, v66, v111\n"
"v_cndmask_b32 v104, v104, v108, vcc\n"
"v_smfmac_f32_16x16x64_bf16 v[100:103], v[24:27], v[232:239], v124\n"
"s_waitcnt lgkmcnt(3)\n"
"ds_read_b128 v[44:47], v127 offset:3072\n"
"v_cndmask_b32 v105, v105, v109, vcc\n"
"v_mfma_f32_16x16x32_bf16 v[112:115], v[8:11], v[0:3], v[112:115]\n"
"v_permlane32_swap_b32 v66, v70\n"
"v_smfmac_f32_16x16x64_bf16 v[96:99], v[28:31], v[240:247], v124\n"
"ds_read_b128 v[48:51], v127 offset:4096\n"
"v_max_f32 v66, v66, v70\n"
"v_mfma_f32_16x16x32_bf16 v[112:115], v[12:15], v[4:7], v[112:115]\n"
"v_cndmask_b32 v106, v106, v110, vcc\n"
"v_cndmask_b32 v107, v107, v111, vcc\n"
"v_smfmac_f32_16x16x64_bf16 v[100:103], v[28:31], v[248:255], v124\n"
"ds_read_b128 v[52:55], v127 offset:5120\n"
"v_mov_b32 v17, 0\n"
"buffer_load_dword v29, v125, s[60:63], s12 offen sc1\n"
"s_nop 1\n"
"v_max3_f32 v66, v66, v112, v113\n"
"v_max3_f32 v70, v66, v114, v115\n"
"v_max3_f32 v66, v66, v114, v115\n"
"v_mov_b32 v18, 0\n"
"v_mov_b32 v19, 0\n"
"v_permlane16_swap_b32 v66, v70\n"
"v_max_f32 v66, v66, v70\n"
"v_mul_f32 v66, s68, v66\n"
"v_fma_f32 v104, v104, s68, -v66\n"
"v_fma_f32 v105, v105, s68, -v66\n"
"v_exp_f32 v104, v104\n"
"v_fma_f32 v112, v112, s68, -v66\n"
"v_exp_f32 v105, v105\n"
"v_fma_f32 v106, v106, s68, -v66\n"
"v_exp_f32 v112, v112\n"
"v_fma_f32 v107, v107, s68, -v66\n"
"v_exp_f32 v106, v106\n"
"v_fma_f32 v113, v113, s68, -v66\n"
"v_exp_f32 v107, v107\n"
"v_exp_f32 v113, v113\n"
"v_cvt_pk_bf16_f32 v20, v104, v105\n"
"v_cvt_pk_bf16_f32 v21, v112, 0\n"
"v_cvt_pk_bf16_f32 v22, v106, v107\n"
"v_cvt_pk_bf16_f32 v23, v113, 0\n"
"v_cvt_pk_bf16_f32 v66, v96, v97\n"
"v_cvt_pk_bf16_f32 v70, v98, v99\n"
"v_mfma_f32_16x16x32_bf16 v[24:27], v[20:23], v[120:123], 0\n"
"v_cvt_pk_bf16_f32 v74, v100, v101\n"
"v_cvt_pk_bf16_f32 v78, v102, v103\n"
"v_smfmac_f32_16x16x64_bf16 v[116:119], v[20:23], v[64:71], v124 abid:1\n"
"ds_read_b128 v[56:59], v127 offset:6144\n"
"v_mov_b32 v28, 0\n"
"v_smfmac_f32_16x16x64_bf16 v[16:19], v[20:23], v[72:79], v124 abid:1\n"
"buffer_load_dwordx2 v[30:31], off, s[60:63], s71 sc1\n"
"ds_read_b128 v[60:63], v127 offset:7168\n"
"v_add_f32 v24, v24, v25\n"
"v_add_f32 v26, v26, v27\n"
"v_rcp_f32 v24, v24\n"
"v_rcp_f32 v26, v26\n"
"v_add_f32 v116, v116, v117\n"
"v_add_f32 v118, v118, v119\n"
"v_mul_f32 v116, v116, v24\n"
"v_mul_f32 v118, v118, v26\n"
"v_add_f32 v16, v16, v17\n"
"v_add_f32 v18, v18, v19\n"
"v_mul_f32 v16, v16, v24\n"
"v_mul_f32 v18, v18, v26\n"
"v_cvt_pk_bf16_f32 v116, v116, v16\n"
"v_cvt_pk_bf16_f32 v118, v118, v18\n"
"ds_write2_b32 v71, v116, v118 offset0:0 offset1:8\n"
"v_mfma_f32_4x4x1_16b_f32 v[96:99], v28, v28, 0\n"
"ds_read_b128 v[80:83], v127 offset:8192\n"
"v_mfma_f32_4x4x1_16b_f32 v[100:103], v28, v28, 0\n"
"ds_read_b128 v[84:87], v127 offset:9216\n"
"v_mfma_f32_4x4x1_16b_f32 v[104:107], v28, v28, 0\n"
"ds_read_b128 v[88:91], v127 offset:10240\n"
"v_mfma_f32_4x4x1_16b_f32 v[108:111], v28, v28, 0\n"
"s_waitcnt lgkmcnt(3)\n"
"s_barrier\n"
"ds_read_b128 v[16:19], v125 offset:8192\n"
"ds_read_b128 v[20:23], v125 offset:9216\n"
"ds_read_b128 v[92:95], v127 offset:11264\n"
"s_waitcnt lgkmcnt(2)\n"
"v_smfmac_f32_16x16x64_bf16 v[96:99], v[16:19], v[32:39], v124\n"
"ds_read_b128 v[0:3], v127 offset:12288\n"
"v_smfmac_f32_16x16x64_bf16 v[100:103], v[16:19], v[40:47], v124\n"
"ds_read_b128 v[4:7], v127 offset:13312\n"
"v_smfmac_f32_16x16x64_bf16 v[104:107], v[16:19], v[48:55], v124\n"
"ds_read_b128 v[8:11], v127 offset:14336\n"
"v_smfmac_f32_16x16x64_bf16 v[108:111], v[16:19], v[56:63], v124\n"
"s_waitcnt lgkmcnt(4)\n"
"ds_read_b128 v[12:15], v127 offset:15360\n"
"v_smfmac_f32_16x16x64_bf16 v[96:99], v[20:23], v[80:87], v124\n"
"s_waitcnt lgkmcnt(4)\n"
"s_add_u32 s14, s96, s79\n"
"v_smfmac_f32_16x16x64_bf16 v[100:103], v[20:23], v[88:95], v124\n"
"s_waitcnt lgkmcnt(2)\n"
"v_smfmac_f32_16x16x64_bf16 v[104:107], v[20:23], v[0:7], v124\n"
"s_waitcnt lgkmcnt(0)\n"
"v_smfmac_f32_16x16x64_bf16 v[108:111], v[20:23], v[8:15], v124\n"
"v_add_f32 v96, v96, v97\n"
"v_add_f32 v98, v98, v99\n"
"v_add_f32 v100, v100, v101\n"
"v_add_f32 v102, v102, v103\n"
"v_cvt_pk_bf16_f32 v116, v96, v100\n"
"v_cvt_pk_bf16_f32 v118, v98, v102\n"
"v_add_f32 v104, v104, v105\n"
"v_add_f32 v106, v106, v107\n"
"v_add_f32 v108, v108, v109\n"
"v_add_f32 v110, v110, v111\n"
"v_cvt_pk_bf16_f32 v117, v104, v108\n"
"v_cvt_pk_bf16_f32 v119, v106, v110\n"
"buffer_store_dwordx4 v[116:119], v126, s[60:63], s96 offen sc1\n"
"s_waitcnt vmcnt(2)\n"
".Ltsw26_%=:\n"
"v_readlane_b32 s74, v29, s69\n"
"v_max_u32_dpp v29, v29, v29 row_shr:1 row_mask:0xf bank_mask:0xf\n"
"ds_read_b128 v[16:19], v125 offset:16640\n"
"s_nop 0\n"
"v_max_u32_dpp v29, v29, v29 row_shr:2 row_mask:0xf bank_mask:0xf\n"
"ds_read_b32 v24, v125 offset:17664\n"
"s_nop 0\n"
"v_max_u32_dpp v29, v29, v29 row_shr:4 row_mask:0xf bank_mask:0xf\n"
"s_memtime s[64:65]\n"
"s_nop 0\n"
"v_max_u32_dpp v29, v29, v29 row_shr:8 row_mask:0xf bank_mask:0xf\n"
"s_add_u32 s75, s8, s30\n"
"s_sub_u32 s75, s75, s74\n"
"v_max_u32_dpp v29, v29, v29 row_bcast:15 row_mask:0xf bank_mask:0xf\n"
"v_mov_b32 v25, 0\n"
"s_nop 0\n"
"v_max_u32_dpp v29, v29, v29 row_bcast:31 row_mask:0xf bank_mask:0xf\n"
"s_nop 0\n"
"v_readlane_b32 s70, v29, 63\n"
"s_nop 0\n"
"s_cmp_eq_u32 s70, -1\n"
"s_cbranch_scc1 .Ltsa27_%=\n"
"s_add_u32 s70, s70, s75\n"
"s_waitcnt lgkmcnt(0)\n"
"s_sub_i32 s64, s70, s64\n"
"s_getpc_b64 s[72:73]\n"
".Lpc29_%=:\n"
"s_max_i32 s64, s64, 0\n"
"s_min_i32 s64, s64, 2047\n"
"s_lshr_b32 s64, s64, 1\n"
"s_andn2_b32 s64, s64, 3\n"
"s_sub_u32 s64, .Lsled28_%=-.Lpc29_%=, s64\n"
"s_add_u32 s64, s72, s64\n"
"s_addc_u32 s65, s73, 0\n"
"s_setpc_b64 s[64:65]\n"
"s_nop 1\n"
"s_nop 1\n"
"s_nop 1\n"
"s_nop 1\n"
"s_nop 1\n"
"s_nop 1\n"
"s_nop 1\n"
"s_nop 1\n"
"s_nop 1\n"
"s_nop 1\n"
"s_nop 1\n"
"s_nop 1\n"
"s_nop 1\n"
"s_nop 1\n"
"s_nop 1\n"
"s_nop 1\n"
"s_nop 1\n"
"s_nop 1\n"
"s_nop 1\n"
"s_nop 1\n"
"s_nop 1\n"
"s_nop 1\n"
"s_nop 1\n"
"s_nop 1\n"
"s_nop 1\n"
"s_nop 1\n"
"s_nop 1\n"
"s_nop 1\n"
"s_nop 1\n"
"s_nop 1\n"
"s_nop 1\n"
"s_nop 1\n"
"s_nop 1\n"
"s_nop 1\n"
"s_nop 1\n"
"s_nop 1\n"
"s_nop 1\n"
"s_nop 1\n"
"s_nop 1\n"
"s_nop 1\n"
"s_nop 1\n"
"s_nop 1\n"
"s_nop 1\n"
"s_nop 1\n"
"s_nop 1\n"
"s_nop 1\n"
"s_nop 1\n"
"s_nop 1\n"
"s_nop 1\n"
"s_nop 1\n"
"s_nop 1\n"
"s_nop 1\n"
"s_nop 1\n"
"s_nop 1\n"
"s_nop 1\n"
"s_nop 1\n"
"s_nop 1\n"
"s_nop 1\n"
"s_nop 1\n"
"s_nop 1\n"
"s_nop 1\n"
"s_nop 1\n"
"s_nop 1\n"
"s_nop 1\n"
"s_nop 1\n"
"s_nop 1\n"
"s_nop 1\n"
"s_nop 1\n"
"s_nop 1\n"
"s_nop 1\n"
"s_nop 1\n"
"s_nop 1\n"
"s_nop 1\n"
"s_nop 1\n"
"s_nop 1\n"
"s_nop 1\n"
"s_nop 1\n"
"s_nop 1\n"
"s_nop 1\n"
"s_nop 1\n"
"s_nop 1\n"
"s_nop 1\n"
"s_nop 1\n"
"s_nop 1\n"
"s_nop 1\n"
"s_nop 1\n"
"s_nop 1\n"
"s_nop 1\n"
"s_nop 1\n"
"s_nop 1\n"
"s_nop 1\n"
"s_nop 1\n"
"s_nop 1\n"
"s_nop 1\n"
"s_nop 1\n"
"s_nop 1\n"
"s_nop 1\n"
"s_nop 1\n"
"s_nop 1\n"
"s_nop 1\n"
"s_nop 1\n"
"s_nop 1\n"
"s_nop 1\n"
"s_nop 1\n"
"s_nop 1\n"
"s_nop 1\n"
"s_nop 1\n"
"s_nop 1\n"
"s_nop 1\n"
"s_nop 1\n"
"s_nop 1\n"
"s_nop 1\n"
"s_nop 1\n"
"s_nop 1\n"
"s_nop 1\n"
"s_nop 1\n"
"s_nop 1\n"
"s_nop 1\n"
"s_nop 1\n"
"s_nop 1\n"
"s_nop 1\n"
"s_nop 1\n"
"s_nop 1\n"
"s_nop 1\n"
"s_nop 1\n"
"s_nop 1\n"
"s_nop 1\n"
"s_nop 1\n"
"s_nop 1\n"
"s_nop 1\n"
"s_nop 1\n"
"s_nop 1\n"
"s_nop 1\n"
"s_nop 1\n"
"s_nop 1\n"
"s_nop 1\n"
"s_nop 1\n"
"s_nop 1\n"
"s_nop 1\n"
"s_nop 1\n"
"s_nop 1\n"
"s_nop 1\n"
"s_nop 1\n"
"s_nop 1\n"
"s_nop 1\n"
"s_nop 1\n"
"s_nop 1\n"
"s_nop 1\n"
"s_nop 1\n"
"s_nop 1\n"
"s_nop 1\n"
"s_nop 1\n"
"s_nop 1\n"
"s_nop 1\n"
"s_nop 1\n"
"s_nop 1\n"
"s_nop 1\n"
"s_nop 1\n"
"s_nop 1\n"
"s_nop 1\n"
"s_nop 1\n"
"s_nop 1\n"
"s_nop 1\n"
"s_nop 1\n"
"s_nop 1\n"
"s_nop 1\n"
"s_nop 1\n"
"s_nop 1\n"
"s_nop 1\n"
"s_nop 1\n"
"s_nop 1\n"
"s_nop 1\n"
"s_nop 1\n"
"s_nop 1\n"
"s_nop 1\n"
"s_nop 1\n"
"s_nop 1\n"
"s_nop 1\n"
"s_nop 1\n"
"s_nop 1\n"
"s_nop 1\n"
"s_nop 1\n"
"s_nop 1\n"
"s_nop 1\n"
"s_nop 1\n"
"s_nop 1\n"
"s_nop 1\n"
"s_nop 1\n"
"s_nop 1\n"
"s_nop 1\n"
"s_nop 1\n"
"s_nop 1\n"
"s_nop 1\n"
"s_nop 1\n"
"s_nop 1\n"
"s_nop 1\n"
"s_nop 1\n"
"s_nop 1\n"
"s_nop 1\n"
"s_nop 1\n"
"s_nop 1\n"
"s_nop 1\n"
"s_nop 1\n"
"s_nop 1\n"
"s_nop 1\n"
"s_nop 1\n"
"s_nop 1\n"
"s_nop 1\n"
"s_nop 1\n"
"s_nop 1\n"
"s_nop 1\n"
"s_nop 1\n"
"s_nop 1\n"
"s_nop 1\n"
"s_nop 1\n"
"s_nop 1\n"
"s_nop 1\n"
"s_nop 1\n"
"s_nop 1\n"
"s_nop 1\n"
"s_nop 1\n"
"s_nop 1\n"
"s_nop 1\n"
"s_nop 1\n"
"s_nop 1\n"
"s_nop 1\n"
"s_nop 1\n"
"s_nop 1\n"
"s_nop 1\n"
"s_nop 1\n"
"s_nop 1\n"
"s_nop 1\n"
"s_nop 1\n"
"s_nop 1\n"
"s_nop 1\n"
"s_nop 1\n"
"s_nop 1\n"
"s_nop 1\n"
"s_nop 1\n"
"s_nop 1\n"
"s_nop 1\n"
"s_nop 1\n"
"s_nop 1\n"
"s_nop 1\n"
"s_nop 1\n"
"s_nop 1\n"
"s_nop 1\n"
"s_nop 1\n"
"s_nop 1\n"
"s_nop 1\n"
"s_nop 1\n"
"s_nop 1\n"
"s_nop 1\n"
"s_nop 1\n"
"s_nop 1\n"
"s_nop 1\n"
".Lsled28_%=:\n"
"buffer_load_dwordx4 v[0:3], v125, s[60:63], s14 offen offset:0 sc1\n"
"buffer_load_dwordx4 v[4:7], v125, s[60:63], s14 offen offset:1024 sc1\n"
"buffer_load_dwordx4 v[8:11], v125, s[60:63], s14 offen offset:2048 sc1\n"
"buffer_load_dwordx4 v[12:15], v125, s[60:63], s14 offen offset:3072 sc1\n"
"s_waitcnt vmcnt(5)\n"
"v_readfirstlane_b32 s64, v30\n"
"v_readfirstlane_b32 s65, v31\n"
"s_nop 0\n"
"s_sub_u32 s65, s65, s35\n"
"s_cmp_le_u32 s65, 64\n"
"s_cselect_b32 s65, s65, 0\n"
"s_cmp_eq_u32 s64, s34\n"
"s_cselect_b32 s65, s65, 0\n"
"s_add_u32 s76, s76, 1\n"
"s_cmp_gt_u32 s65, s76\n"
"s_cbranch_scc1 .Lkvok30_%=\n"
"s_add_u32 s74, s76, 1\n"
"s_mov_b32 s78, 16384\n"
".Lsw31_%=:\n"
"buffer_load_dwordx2 v[30:31], off, s[60:63], s71 sc1\n"
"s_waitcnt vmcnt(0)\n"
"v_readfirstlane_b32 s64, v30\n"
"v_readfirstlane_b32 s65, v31\n"
"s_nop 0\n"
"s_sub_u32 s65, s65, s35\n"
"s_cmp_le_u32 s65, 64\n"
"s_cselect_b32 s65, s65, 0\n"
"s_cmp_eq_u32 s64, s34\n"
"s_cselect_b32 s65, s65, 0\n"
"s_cmp_ge_u32 s65, s74\n"
"s_cbranch_scc1 .Lswok32_%=\n"
"s_sub_u32 s78, s78, 1\n"
"s_cbranch_scc1 .Lfail5_%=\n"
"s_sleep 2\n"
"s_branch .Lsw31_%=\n"
".Lswok32_%=:\n"
".Lkvok30_%=:\n"
"s_and_b32 s64, s76, 7\n"
"s_lshl_b32 s65, s76, 12\n"
"s_xor_b32 s65, s65, s77\n"
"s_and_b32 s65, s65, 8192\n"
"s_lshl_b32 s64, s64, 20\n"
"s_add_u32 s64, s64, s65\n"
"s_add_u32 s64, s64, s97\n"
"s_add_u32 s65, s64, 16384\n"
"s_mov_b32 s72, s64\n"
"s_mov_b32 s73, s65\n"
"s_and_b32 s64, s76, 15\n"
"s_lshl_b32 s64, s64, 16\n"
"s_add_u32 s101, s64, s77\n"
"s_lshr_b32 s64, s64, 5\n"
"s_and_b32 s64, s64, 0x4000\n"
"s_xor_b32 s64, s64, s33\n"
"s_add_u32 s64, s101, s64\n"
"s_lshl_b32 s65, s39, 15\n"
"s_add_u32 s64, s64, s65\n"
"s_xor_b32 s94, s64, 0\n"
"s_xor_b32 s95, s94, 1024\n"
"s_xor_b32 s100, s64, 2048\n"
"v_xor_b32 v126, 0x4000, v126\n"
"s_xor_b32 s79, s79, 0x4000\n"
"s_barrier\n"
"v_mov_b32 v80, 0\n"
"v_mov_b32 v81, 0\n"
"v_mov_b32 v82, 0\n"
"v_mov_b32 v83, 0\n"
"v_mov_b32 v84, 0\n"
"v_mov_b32 v85, 0\n"
"v_mov_b32 v86, 0\n"
"v_mov_b32 v87, 0\n"
"v_mov_b32 v88, 0\n"
"v_mov_b32 v89, 0\n"
"v_mov_b32 v90, 0\n"
"v_mov_b32 v91, 0\n"
"v_mov_b32 v92, 0\n"
"v_mov_b32 v93, 0\n"
"v_mov_b32 v94, 0\n"
"v_mov_b32 v95, 0\n"
"v_mov_b32 v96, 0\n"
"v_mov_b32 v97, 0\n"
"v_mov_b32 v98, 0\n"
"v_mov_b32 v99, 0\n"
"v_mov_b32 v100, 0\n"
"v_mov_b32 v101, 0\n"
"v_mov_b32 v102, 0\n"
"v_mov_b32 v103, 0\n"
"v_mov_b32 v116, 0\n"
"v_mov_b32 v112, -1\n"
"v_mov_b32 v113, -1\n"
"v_mov_b32 v114, -1\n"
"v_mov_b32 v115, -1\n"
"s_mov_b32 s78, 16384\n"
".Lrwait24_%=:\n"
"s_waitcnt vmcnt(3)\n"
"v_mfma_f32_16x16x32_bf16 v[20:23], v[16:19], v[0:3], 0\n"
"v_max3_u32 v25, v0, v2, v25\n"
"s_waitcnt vmcnt(2)\n"
"v_mfma_f32_16x16x32_bf16 v[20:23], v[16:19], v[4:7], v[20:23]\n"
"v_max3_u32 v25, v4, v6, v25\n"
"s_waitcnt vmcnt(1)\n"
"v_mfma_f32_16x16x32_bf16 v[20:23], v[16:19], v[8:11], v[20:23]\n"
"v_max3_u32 v25, v8, v10, v25\n"
"s_waitcnt vmcnt(0)\n"
"v_mfma_f32_16x16x32_bf16 v[20:23], v[16:19], v[12:15], v[20:23]\n"
"v_max3_u32 v25, v12, v14, v25\n"
"v_cmp_eq_u32 vcc, -1, v25\n"
"s_nop 0\n"
"s_cmp_eq_u64 vcc, 0\n"
"s_cselect_b32 s64, s101, 0x6000000\n"
"s_nop 2\n"
"v_add_f32_dpp v20, v20, v20 row_ror:8 row_mask:0xf bank_mask:0xf\n"
"v_add_f32_dpp v21, v21, v21 row_ror:8 row_mask:0xf bank_mask:0xf\n"
"v_add_f32_dpp v22, v22, v22 row_ror:8 row_mask:0xf bank_mask:0xf\n"
"v_add_f32_dpp v23, v23, v23 row_ror:8 row_mask:0xf bank_mask:0xf\n"
"v_cvt_pk_bf16_f32 v26, v20, v21\n"
"v_cvt_pk_bf16_f32 v27, v22, v23\n"
"buffer_store_dwordx2 v[26:27], v24, s[60:63], s64 offen sc1\n"
"s_cbranch_scc0 .Lrre25_%=\n"
"s_add_u32 s75, s76, 2\n"
"s_and_b32 s75, s75, 15\n"
"s_lshl_b32 s75, s75, 16\n"
"s_add_u32 s75, s75, s77\n"
"buffer_load_dwordx4 v[104:107], v125, s[60:63], s72 offen offset:0 sc1\n"
"buffer_load_dwordx4 v[108:111], v125, s[60:63], s72 offen offset:1024 sc1\n"
"v_lshrrev_b32 v118, 1, v125\n"
"buffer_load_dwordx2 v[64:65], v118, s[60:63], s73 offen offset:0 sc1\n"
"buffer_load_dwordx2 v[68:69], v118, s[60:63], s73 offen offset:512 sc1\n"
"buffer_load_dwordx2 v[72:73], v118, s[60:63], s73 offen offset:1024 sc1\n"
"buffer_load_dwordx2 v[76:77], v118, s[60:63], s73 offen offset:1536 sc1\n"
"s_xor_b32 s96, s96, 524288\n"
"s_mov_b32 s78, 16384\n"
"s_add_u32 s12, s12, 16384\n"
"s_add_u32 s13, s13, 16384\n"
"s_xor_b32 s71, s71, 128\n"
"s_cmp_lt_u32 s76, 63\n"
"s_waitcnt lgkmcnt(0)\n"
"s_cbranch_scc1 .Ltop19_%=\n"
"s_endpgm\n"
".Ltsa27_%=:\n"
"s_sub_u32 s78, s78, 1\n"
"s_cbranch_scc1 .Lfail5_%=\n"
"buffer_load_dword v29, v125, s[60:63], s12 offen sc1\n"
"s_waitcnt vmcnt(0)\n"
"s_branch .Ltsw26_%=\n"
".Lrre25_%=:\n"
"s_nop 7\n"
"s_sub_u32 s78, s78, 1\n"
"s_cbranch_scc1 .Lfail5_%=\n"
"buffer_load_dwordx4 v[0:3], v125, s[60:63], s14 offen offset:0 sc1\n"
"buffer_load_dwordx4 v[4:7], v125, s[60:63], s14 offen offset:1024 sc1\n"
"buffer_load_dwordx4 v[8:11], v125, s[60:63], s14 offen offset:2048 sc1\n"
"buffer_load_dwordx4 v[12:15], v125, s[60:63], s14 offen offset:3072 sc1\n"
"v_mov_b32 v25, 0\n"
"s_waitcnt vmcnt(0)\n"
"s_branch .Lrwait24_%=\n"
".Lent16_%=:\n"
"s_lshl_b32 s64, s39, 9\n"
"s_xor_b32 s65, s64, 256\n"
"buffer_load_dwordx4 v[0:3], v118, s[56:59], s65 offen\n"
"s_xor_b32 s65, s64, 384\n"
"buffer_load_dwordx4 v[4:7], v118, s[56:59], s65 offen\n"
"s_waitcnt lgkmcnt(0)\n"
"s_branch .LphA34_%=\n"
".Lretry35_%=:\n"
"s_sub_u32 s78, s78, 1\n"
"s_cbranch_scc1 .Lfail5_%=\n"
"s_waitcnt lgkmcnt(0)\n"
"buffer_load_dwordx4 v[0:3], v125, s[60:63], s94 offen sc1\n"
"buffer_load_dwordx4 v[4:7], v125, s[60:63], s95 offen sc1\n"
"s_nop 4\n"
"v_mov_b32 v80, 0\n"
"v_mov_b32 v81, 0\n"
"v_mov_b32 v82, 0\n"
"v_mov_b32 v83, 0\n"
"v_mov_b32 v84, 0\n"
"v_mov_b32 v85, 0\n"
"v_mov_b32 v86, 0\n"
"v_mov_b32 v87, 0\n"
"v_mov_b32 v88, 0\n"
"v_mov_b32 v89, 0\n"
"v_mov_b32 v90, 0\n"
"v_mov_b32 v91, 0\n"
"v_mov_b32 v92, 0\n"
"v_mov_b32 v93, 0\n"
"v_mov_b32 v94, 0\n"
"v_mov_b32 v95, 0\n"
"v_mov_b32 v96, 0\n"
"v_mov_b32 v97, 0\n"
"v_mov_b32 v98, 0\n"
"v_mov_b32 v99, 0\n"
"v_mov_b32 v100, 0\n"
"v_mov_b32 v101, 0\n"
"v_mov_b32 v102, 0\n"
"v_mov_b32 v103, 0\n"
"v_mov_b32 v116, 0\n"
"s_branch .LphA34_%=\n"
".p2align 6\n"
".Ltop33_%=:\n"
"s_barrier\n"
"buffer_load_dwordx4 v[0:3], v125, s[60:63], s94 offen sc1\n"
"s_nop 4\n"
"buffer_load_dwordx4 v[4:7], v125, s[60:63], s95 offen sc1\n"
".LphA34_%=:\n"
"s_waitcnt vmcnt(2)\n"
"ds_write_b128 v125, v[104:107] offset:2048\n"
"ds_write_b128 v125, v[108:111] offset:3072\n"
"s_waitcnt vmcnt(1)\n"
"v_smfmac_f32_16x16x64_bf16 v[80:83], v[0:3], a[32:39], v124\n"
"ds_write_b128 v125, v[0:3] offset:21504\n"
"v_smfmac_f32_16x16x64_bf16 v[84:87], v[0:3], a[40:47], v124\n"
"v_max3_u32 v116, v0, v2, v116\n"
"v_smfmac_f32_16x16x64_bf16 v[88:91], v[0:3], a[160:167], v124\n"
"v_smfmac_f32_16x16x64_bf16 v[92:95], v[0:3], a[168:175], v124\n"
"s_waitcnt vmcnt(0)\n"
"ds_write_b128 v125, v[4:7] offset:22528\n"
"v_smfmac_f32_16x16x64_bf16 v[80:83], v[4:7], a[48:55], v124\n"
"v_max3_u32 v116, v4, v6, v116\n"
"v_smfmac_f32_16x16x64_bf16 v[84:87], v[4:7], a[56:63], v124\n"
"v_cmp_eq_u32 vcc, -1, v116\n"
"v_smfmac_f32_16x16x64_bf16 v[88:91], v[4:7], a[176:183], v124\n"
"s_cbranch_vccnz .Lretry35_%=\n"
"v_smfmac_f32_16x16x64_bf16 v[92:95], v[4:7], a[184:191], v124\n"
"s_waitcnt lgkmcnt(0)\n"
"s_barrier\n"
"ds_read_b128 v[8:11], v125 offset:23552\n"
"v_smfmac_f32_16x16x64_bf16 v[96:99], v[0:3], v[160:167], v124\n"
"ds_read_b128 v[12:15], v125 offset:24576\n"
"v_smfmac_f32_16x16x64_bf16 v[100:103], v[0:3], v[168:175], v124\n"
"ds_read_b128 v[16:19], v125 offset:25600\n"
"v_smfmac_f32_16x16x64_bf16 v[96:99], v[4:7], v[176:183], v124\n"
"ds_read_b128 v[20:23], v125 offset:26624\n"
"v_smfmac_f32_16x16x64_bf16 v[100:103], v[4:7], v[184:191], v124\n"
"s_waitcnt lgkmcnt(3)\n"
"ds_read_b128 v[24:27], v125 offset:19456\n"
"v_smfmac_f32_16x16x64_bf16 v[80:83], v[8:11], a[64:71], v124\n"
"ds_read_b128 v[28:31], v125 offset:20480\n"
"v_smfmac_f32_16x16x64_bf16 v[84:87], v[8:11], a[72:79], v124\n"
"s_waitcnt lgkmcnt(3)\n"
"ds_read_b128 v[32:35], v125 offset:0\n"
"v_smfmac_f32_16x16x64_bf16 v[80:83], v[12:15], a[80:87], v124\n"
"ds_read_b128 v[36:39], v125 offset:1024\n"
"v_smfmac_f32_16x16x64_bf16 v[84:87], v[12:15], a[88:95], v124\n"
"s_waitcnt lgkmcnt(3)\n"
"ds_read_b128 v[40:43], v125 offset:2048\n"
"v_smfmac_f32_16x16x64_bf16 v[80:83], v[16:19], a[96:103], v124\n"
"ds_read_b128 v[44:47], v125 offset:3072\n"
"v_smfmac_f32_16x16x64_bf16 v[84:87], v[16:19], a[104:111], v124\n"
"s_waitcnt lgkmcnt(4)\n"
"ds_read_b128 v[48:51], v125 offset:4096\n"
"v_smfmac_f32_16x16x64_bf16 v[80:83], v[20:23], a[112:119], v124\n"
"buffer_store_dwordx4 v[112:115], v125, s[60:63], s14 offen offset:1024 sc1\n"
"v_mov_b32 v116, 0\n"
"v_smfmac_f32_16x16x64_bf16 v[84:87], v[20:23], a[120:127], v124\n"
"ds_read_b128 v[52:55], v125 offset:5120\n"
"v_mov_b32 v117, 0\n"
"v_smfmac_f32_16x16x64_bf16 v[80:83], v[24:27], a[0:7], v124\n"
"ds_read_b128 v[56:59], v125 offset:6144\n"
"v_mov_b32 v118, 0\n"
"v_smfmac_f32_16x16x64_bf16 v[84:87], v[24:27], a[8:15], v124\n"
"ds_read_b128 v[60:63], v125 offset:7168\n"
"v_mov_b32 v119, 0\n"
"v_smfmac_f32_16x16x64_bf16 v[80:83], v[28:31], a[16:23], v124\n"
"v_smfmac_f32_16x16x64_bf16 v[84:87], v[28:31], a[24:31], v124\n"
"v_smfmac_f32_16x16x64_bf16 v[88:91], v[8:11], a[192:199], v124\n"
"s_mov_b64 vcc, s[66:67]\n"
"v_smfmac_f32_16x16x64_bf16 v[92:95], v[8:11], a[200:207], v124\n"
"v_add_f32 v80, v80, v81\n"
"v_smfmac_f32_16x16x64_bf16 v[88:91], v[12:15], a[208:215], v124\n"
"v_add_f32 v82, v82, v83\n"
"v_smfmac_f32_16x16x64_bf16 v[92:95], v[12:15], a[216:223], v124\n"
"v_add_f32 v84, v84, v85\n"
"v_smfmac_f32_16x16x64_bf16 v[88:91], v[16:19], a[224:231], v124\n"
"v_add_f32 v86, v86, v87\n"
"v_smfmac_f32_16x16x64_bf16 v[92:95], v[16:19], a[232:239], v124\n"
"v_cvt_pk_bf16_f32 v80, v80, v84\n"
"v_cvt_pk_bf16_f32 v82, v82, v86\n"
"v_smfmac_f32_16x16x64_bf16 v[88:91], v[20:23], a[240:247], v124\n"
"ds_write2_b32 v67, v80, v82 offset0:0 offset1:4\n"
"v_smfmac_f32_16x16x64_bf16 v[92:95], v[20:23], a[248:255], v124\n"
"v_smfmac_f32_16x16x64_bf16 v[88:91], v[24:27], a[128:135], v124\n"
"v_smfmac_f32_16x16x64_bf16 v[92:95], v[24:27], a[136:143], v124\n"
"v_smfmac_f32_16x16x64_bf16 v[88:91], v[28:31], a[144:151], v124\n"
"s_waitcnt lgkmcnt(0)\n"
"s_barrier\n"
"ds_read_b128 v[80:83], v75 offset:0\n"
"v_smfmac_f32_16x16x64_bf16 v[92:95], v[28:31], a[152:159], v124\n"
"ds_read_b128 v[84:87], v75 offset:1600\n"
"v_smfmac_f32_16x16x64_bf16 v[96:99], v[8:11], v[192:199], v124\n"
"ds_read_b128 v[0:3], v75 offset:3200\n"
"v_smfmac_f32_16x16x64_bf16 v[100:103], v[8:11], v[200:207], v124\n"
"ds_read_b128 v[4:7], v75 offset:4800\n"
"v_add_f32 v88, v88, v89\n"
"v_smfmac_f32_16x16x64_bf16 v[96:99], v[12:15], v[208:215], v124\n"
"v_add_f32 v90, v90, v91\n"
"v_smfmac_f32_16x16x64_bf16 v[100:103], v[12:15], v[216:223], v124\n"
"v_add_f32 v92, v92, v93\n"
"v_smfmac_f32_16x16x64_bf16 v[96:99], v[16:19], v[224:231], v124\n"
"v_add_f32 v94, v94, v95\n"
"s_waitcnt lgkmcnt(3)\n"
"v_mfma_f32_16x16x32_bf16 v[104:107], v[32:35], v[80:83], 0\n"
"v_cvt_pk_bf16_f32 v88, v88, v92\n"
"v_cvt_pk_bf16_f32 v90, v90, v94\n"
"v_mfma_f32_16x16x32_bf16 v[108:111], v[36:39], v[80:83], 0\n"
"s_waitcnt lgkmcnt(2)\n"
"ds_write2_b32 v67, v88, v90 offset0:32 offset1:36\n"
"v_mfma_f32_16x16x32_bf16 v[104:107], v[40:43], v[84:87], v[104:107]\n"
"v_mfma_f32_16x16x32_bf16 v[108:111], v[44:47], v[84:87], v[108:111]\n"
"s_waitcnt lgkmcnt(1)\n"
"v_mfma_f32_16x16x32_bf16 v[104:107], v[48:51], v[0:3], v[104:107]\n"
"v_mfma_f32_16x16x32_bf16 v[108:111], v[52:55], v[0:3], v[108:111]\n"
"s_waitcnt lgkmcnt(0)\n"
"s_barrier\n"
"ds_read_b128 v[88:91], v79 offset:0\n"
"v_mfma_f32_16x16x32_bf16 v[104:107], v[56:59], v[4:7], v[104:107]\n"
"ds_read_b128 v[92:95], v79 offset:1600\n"
"v_mfma_f32_16x16x32_bf16 v[108:111], v[60:63], v[4:7], v[108:111]\n"
"ds_read_b128 v[8:11], v79 offset:3200\n"
"v_smfmac_f32_16x16x64_bf16 v[100:103], v[16:19], v[232:239], v124\n"
"ds_read_b128 v[12:15], v79 offset:4800\n"
"v_smfmac_f32_16x16x64_bf16 v[96:99], v[20:23], v[240:247], v124\n"
"ds_read_b128 v[32:35], v127 offset:0\n"
"v_max3_f32 v66, v104, v105, v106\n"
"v_smfmac_f32_16x16x64_bf16 v[100:103], v[20:23], v[248:255], v124\n"
"s_waitcnt lgkmcnt(4)\n"
"ds_read_b128 v[36:39], v127 offset:1024\n"
"v_mov_b32 v16, 0\n"
"v_mfma_f32_16x16x32_bf16 v[112:115], v[88:91], v[80:83], 0\n"
"v_max3_f32 v66, v66, v107, v108\n"
"v_max3_f32 v66, v66, v109, v110\n"
"v_smfmac_f32_16x16x64_bf16 v[96:99], v[24:27], v[128:135], v124\n"
"s_waitcnt lgkmcnt(4)\n"
"ds_read_b128 v[40:43], v127 offset:2048\n"
"v_max_f32 v70, v66, v111\n"
"v_mfma_f32_16x16x32_bf16 v[112:115], v[92:95], v[84:87], v[112:115]\n"
"v_max_f32 v66, v66, v111\n"
"v_cndmask_b32 v104, v104, v108, vcc\n"
"v_smfmac_f32_16x16x64_bf16 v[100:103], v[24:27], v[136:143], v124\n"
"s_waitcnt lgkmcnt(3)\n"
"ds_read_b128 v[44:47], v127 offset:3072\n"
"v_cndmask_b32 v105, v105, v109, vcc\n"
"v_mfma_f32_16x16x32_bf16 v[112:115], v[8:11], v[0:3], v[112:115]\n"
"v_permlane32_swap_b32 v66, v70\n"
"v_smfmac_f32_16x16x64_bf16 v[96:99], v[28:31], v[144:151], v124\n"
"ds_read_b128 v[48:51], v127 offset:4096\n"
"v_max_f32 v66, v66, v70\n"
"v_mfma_f32_16x16x32_bf16 v[112:115], v[12:15], v[4:7], v[112:115]\n"
"v_cndmask_b32 v106, v106, v110, vcc\n"
"v_cndmask_b32 v107, v107, v111, vcc\n"
"v_smfmac_f32_16x16x64_bf16 v[100:103], v[28:31], v[152:159], v124\n"
"ds_read_b128 v[52:55], v127 offset:5120\n"
"v_mov_b32 v17, 0\n"
"buffer_load_dword v29, v125, s[60:63], s12 offen sc1\n"
"s_nop 1\n"
"v_max3_f32 v66, v66, v112, v113\n"
"v_max3_f32 v70, v66, v114, v115\n"
"v_max3_f32 v66, v66, v114, v115\n"
"v_mov_b32 v18, 0\n"
"v_mov_b32 v19, 0\n"
"v_permlane16_swap_b32 v66, v70\n"
"v_max_f32 v66, v66, v70\n"
"v_mul_f32 v66, s68, v66\n"
"v_fma_f32 v104, v104, s68, -v66\n"
"v_fma_f32 v105, v105, s68, -v66\n"
"v_exp_f32 v104, v104\n"
"v_fma_f32 v112, v112, s68, -v66\n"
"v_exp_f32 v105, v105\n"
"v_fma_f32 v106, v106, s68, -v66\n"
"v_exp_f32 v112, v112\n"
"v_fma_f32 v107, v107, s68, -v66\n"
"v_exp_f32 v106, v106\n"
"v_fma_f32 v113, v113, s68, -v66\n"
"v_exp_f32 v107, v107\n"
"v_exp_f32 v113, v113\n"
"v_cvt_pk_bf16_f32 v20, v104, v105\n"
"v_cvt_pk_bf16_f32 v21, v112, 0\n"
"v_cvt_pk_bf16_f32 v22, v106, v107\n"
"v_cvt_pk_bf16_f32 v23, v113, 0\n"
"v_cvt_pk_bf16_f32 v66, v96, v97\n"
"v_cvt_pk_bf16_f32 v70, v98, v99\n"
"v_mfma_f32_16x16x32_bf16 v[24:27], v[20:23], v[120:123], 0\n"
"v_cvt_pk_bf16_f32 v74, v100, v101\n"
"v_cvt_pk_bf16_f32 v78, v102, v103\n"
"v_smfmac_f32_16x16x64_bf16 v[116:119], v[20:23], v[64:71], v124 abid:1\n"
"ds_read_b128 v[56:59], v127 offset:6144\n"
"v_mov_b32 v28, 0\n"
"v_smfmac_f32_16x16x64_bf16 v[16:19], v[20:23], v[72:79], v124 abid:1\n"
"buffer_load_dwordx2 v[30:31], off, s[60:63], s71 sc1\n"
"ds_read_b128 v[60:63], v127 offset:7168\n"
"v_add_f32 v24, v24, v25\n"
"v_add_f32 v26, v26, v27\n"
"v_rcp_f32 v24, v24\n"
"v_rcp_f32 v26, v26\n"
"v_add_f32 v116, v116, v117\n"
"v_add_f32 v118, v118, v119\n"
"v_mul_f32 v116, v116, v24\n"
"v_mul_f32 v118, v118, v26\n"
"v_add_f32 v16, v16, v17\n"
"v_add_f32 v18, v18, v19\n"
"v_mul_f32 v16, v16, v24\n"
"v_mul_f32 v18, v18, v26\n"
"v_cvt_pk_bf16_f32 v116, v116, v16\n"
"v_cvt_pk_bf16_f32 v118, v118, v18\n"
"ds_write2_b32 v71, v116, v118 offset0:0 offset1:8\n"
"v_mfma_f32_4x4x1_16b_f32 v[96:99], v28, v28, 0\n"
"ds_read_b128 v[80:83], v127 offset:8192\n"
"v_mfma_f32_4x4x1_16b_f32 v[100:103], v28, v28, 0\n"
"ds_read_b128 v[84:87], v127 offset:9216\n"
"v_mfma_f32_4x4x1_16b_f32 v[104:107], v28, v28, 0\n"
"ds_read_b128 v[88:91], v127 offset:10240\n"
"v_mfma_f32_4x4x1_16b_f32 v[108:111], v28, v28, 0\n"
"s_waitcnt lgkmcnt(3)\n"
"s_barrier\n"
"ds_read_b128 v[16:19], v125 offset:8192\n"
"ds_read_b128 v[20:23], v125 offset:9216\n"
"ds_read_b128 v[92:95], v127 offset:11264\n"
"s_waitcnt lgkmcnt(2)\n"
"v_smfmac_f32_16x16x64_bf16 v[96:99], v[16:19], v[32:39], v124\n"
"ds_read_b128 v[0:3], v127 offset:12288\n"
"v_smfmac_f32_16x16x64_bf16 v[100:103], v[16:19], v[40:47], v124\n"
"ds_read_b128 v[4:7], v127 offset:13312\n"
"v_smfmac_f32_16x16x64_bf16 v[104:107], v[16:19], v[48:55], v124\n"
"ds_read_b128 v[8:11], v127 offset:14336\n"
"v_smfmac_f32_16x16x64_bf16 v[108:111], v[16:19], v[56:63], v124\n"
"s_waitcnt lgkmcnt(4)\n"
"ds_read_b128 v[12:15], v127 offset:15360\n"
"v_smfmac_f32_16x16x64_bf16 v[96:99], v[20:23], v[80:87], v124\n"
"s_waitcnt lgkmcnt(4)\n"
"s_add_u32 s14, s96, s79\n"
"v_smfmac_f32_16x16x64_bf16 v[100:103], v[20:23], v[88:95], v124\n"
"s_waitcnt lgkmcnt(2)\n"
"v_smfmac_f32_16x16x64_bf16 v[104:107], v[20:23], v[0:7], v124\n"
"s_waitcnt lgkmcnt(0)\n"
"v_smfmac_f32_16x16x64_bf16 v[108:111], v[20:23], v[8:15], v124\n"
"v_add_f32 v96, v96, v97\n"
"v_add_f32 v98, v98, v99\n"
"v_add_f32 v100, v100, v101\n"
"v_add_f32 v102, v102, v103\n"
"v_cvt_pk_bf16_f32 v116, v96, v100\n"
"v_cvt_pk_bf16_f32 v118, v98, v102\n"
"v_add_f32 v104, v104, v105\n"
"v_add_f32 v106, v106, v107\n"
"v_add_f32 v108, v108, v109\n"
"v_add_f32 v110, v110, v111\n"
"v_cvt_pk_bf16_f32 v117, v104, v108\n"
"v_cvt_pk_bf16_f32 v119, v106, v110\n"
"buffer_store_dwordx4 v[116:119], v126, s[60:63], s96 offen sc1\n"
"s_waitcnt vmcnt(2)\n"
".Ltsw38_%=:\n"
"v_readlane_b32 s74, v29, s69\n"
"v_max_u32_dpp v29, v29, v29 row_shr:1 row_mask:0xf bank_mask:0xf\n"
"s_nop 1\n"
"v_max_u32_dpp v29, v29, v29 row_shr:2 row_mask:0xf bank_mask:0xf\n"
"s_nop 1\n"
"v_max_u32_dpp v29, v29, v29 row_shr:4 row_mask:0xf bank_mask:0xf\n"
"s_nop 1\n"
"v_max_u32_dpp v29, v29, v29 row_shr:8 row_mask:0xf bank_mask:0xf\n"
"s_nop 1\n"
"v_max_u32_dpp v29, v29, v29 row_bcast:15 row_mask:0xf bank_mask:0xf\n"
"s_nop 1\n"
"v_max_u32_dpp v29, v29, v29 row_bcast:31 row_mask:0xf bank_mask:0xf\n"
"s_nop 0\n"
"v_readlane_b32 s70, v29, 63\n"
"s_nop 0\n"
"s_cmp_eq_u32 s70, -1\n"
"s_cbranch_scc1 .Ltsa39_%=\n"
"v_mov_b32 v80, 0\n"
"v_mov_b32 v81, 0\n"
"v_mov_b32 v82, 0\n"
"v_mov_b32 v83, 0\n"
"v_mov_b32 v84, 0\n"
"v_mov_b32 v85, 0\n"
"v_mov_b32 v86, 0\n"
"v_mov_b32 v87, 0\n"
"v_mov_b32 v88, 0\n"
"v_mov_b32 v89, 0\n"
"v_mov_b32 v90, 0\n"
"v_mov_b32 v91, 0\n"
"v_mov_b32 v92, 0\n"
"v_mov_b32 v93, 0\n"
"v_mov_b32 v94, 0\n"
"v_mov_b32 v95, 0\n"
"v_mov_b32 v96, 0\n"
"v_mov_b32 v97, 0\n"
"v_mov_b32 v98, 0\n"
"v_mov_b32 v99, 0\n"
"v_mov_b32 v100, 0\n"
"v_mov_b32 v101, 0\n"
"v_mov_b32 v102, 0\n"
"v_mov_b32 v103, 0\n"
"v_mov_b32 v116, 0\n"
"v_mov_b32 v112, -1\n"
"v_mov_b32 v113, -1\n"
"v_mov_b32 v114, -1\n"
"v_mov_b32 v115, -1\n"
"s_waitcnt vmcnt(1)\n"
"v_readfirstlane_b32 s64, v30\n"
"v_readfirstlane_b32 s65, v31\n"
"s_nop 0\n"
"s_sub_u32 s65, s65, s35\n"
"s_cmp_le_u32 s65, 64\n"
"s_cselect_b32 s65, s65, 0\n"
"s_cmp_eq_u32 s64, s34\n"
"s_cselect_b32 s65, s65, 0\n"
"s_add_u32 s76, s76, 1\n"
"s_cmp_gt_u32 s65, s76\n"
"s_cbranch_scc1 .Lkvok40_%=\n"
"s_add_u32 s74, s76, 1\n"
"s_mov_b32 s78, 16384\n"
".Lsw41_%=:\n"
"buffer_load_dwordx2 v[30:31], off, s[60:63], s71 sc1\n"
"s_waitcnt vmcnt(0)\n"
"v_readfirstlane_b32 s64, v30\n"
"v_readfirstlane_b32 s65, v31\n"
"s_nop 0\n"
"s_sub_u32 s65, s65, s35\n"
"s_cmp_le_u32 s65, 64\n"
"s_cselect_b32 s65, s65, 0\n"
"s_cmp_eq_u32 s64, s34\n"
"s_cselect_b32 s65, s65, 0\n"
"s_cmp_ge_u32 s65, s74\n"
"s_cbranch_scc1 .Lswok42_%=\n"
"s_sub_u32 s78, s78, 1\n"
"s_cbranch_scc1 .Lfail5_%=\n"
"s_sleep 2\n"
"s_branch .Lsw41_%=\n"
".Lswok42_%=:\n"
".Lkvok40_%=:\n"
"s_and_b32 s64, s76, 7\n"
"s_lshl_b32 s65, s76, 12\n"
"s_xor_b32 s65, s65, s77\n"
"s_and_b32 s65, s65, 8192\n"
"s_lshl_b32 s64, s64, 20\n"
"s_add_u32 s64, s64, s65\n"
"s_add_u32 s64, s64, s97\n"
"s_add_u32 s65, s64, 16384\n"
"s_barrier\n"
"buffer_load_dwordx4 v[104:107], v125, s[60:63], s64 offen offset:0 sc1\n"
"buffer_load_dwordx4 v[108:111], v125, s[60:63], s64 offen offset:1024 sc1\n"
"v_lshrrev_b32 v118, 1, v125\n"
"buffer_load_dwordx2 v[64:65], v118, s[60:63], s65 offen offset:0 sc1\n"
"buffer_load_dwordx2 v[68:69], v118, s[60:63], s65 offen offset:512 sc1\n"
"buffer_load_dwordx2 v[72:73], v118, s[60:63], s65 offen offset:1024 sc1\n"
"buffer_load_dwordx2 v[76:77], v118, s[60:63], s65 offen offset:1536 sc1\n"
"s_and_b32 s64, s76, 15\n"
"s_lshl_b32 s64, s64, 16\n"
"s_add_u32 s101, s64, s77\n"
"s_lshr_b32 s64, s64, 5\n"
"s_and_b32 s64, s64, 0x4000\n"
"s_xor_b32 s64, s64, s33\n"
"s_add_u32 s64, s101, s64\n"
"s_lshl_b32 s65, s39, 15\n"
"s_add_u32 s64, s64, s65\n"
"s_xor_b32 s94, s64, 2048\n"
"s_xor_b32 s95, s94, 1024\n"
"s_xor_b32 s100, s64, 32768\n"
"v_xor_b32 v126, 0x4000, v126\n"
"s_xor_b32 s79, s79, 0x4000\n"
"s_cmp_le_u32 s76, 1\n"
"s_cselect_b32 s64, 250, 0\n"
"s_add_u32 s70, s70, s64\n"
"s_add_u32 s70, s70, s31\n"
"s_memtime s[8:9]\n"
"s_memrealtime s[16:17]\n"
"s_nop 2\n"
"s_memrealtime s[18:19]\n"
"s_nop 2\n"
"s_memrealtime s[20:21]\n"
"s_nop 2\n"
"s_memrealtime s[22:23]\n"
"s_nop 2\n"
"s_memrealtime s[24:25]\n"
"s_nop 2\n"
"s_memrealtime s[26:27]\n"
"s_nop 2\n"
"s_memrealtime s[28:29]\n"
"s_nop 2\n"
"s_waitcnt lgkmcnt(0)\n"
"s_mul_i32 s64, s16, 24\n"
"s_mov_b32 s17, 0\n"
"s_cmp_eq_u32 s18, s16\n"
"s_addc_u32 s17, s17, 0\n"
"s_cmp_eq_u32 s20, s16\n"
"s_addc_u32 s17, s17, 0\n"
"s_cmp_eq_u32 s22, s16\n"
"s_addc_u32 s17, s17, 0\n"
"s_cmp_eq_u32 s24, s16\n"
"s_addc_u32 s17, s17, 0\n"
"s_cmp_eq_u32 s26, s16\n"
"s_addc_u32 s17, s17, 0\n"
"s_cmp_eq_u32 s28, s16\n"
"s_addc_u32 s17, s17, 0\n"
"s_lshl_b32 s17, s17, 4\n"
"s_sub_u32 s64, s64, s17\n"
"s_sub_u32 s70, s70, s64\n"
"s_sub_u32 s64, s8, s64\n"
"s_sub_u32 s65, s64, s15\n"
"s_mov_b32 s15, s64\n"
"s_add_u32 s64, s65, 256\n"
"s_cmp_lt_u32 s64, 512\n"
"s_cselect_b32 s65, s65, 0\n"
"s_mul_i32 s65, s65, s70\n"
"s_mul_i32 s65, s65, 18\n"
"s_ashr_i32 s65, s65, 16\n"
"s_add_u32 s70, s70, s65\n"
"s_add_u32 s70, s70, s8\n"
"s_xor_b32 s96, s96, 524288\n"
"s_mov_b32 s78, 16384\n"
"s_add_u32 s12, s12, 16384\n"
"s_add_u32 s13, s13, 16384\n"
"s_xor_b32 s71, s71, 128\n"
"s_memtime s[64:65]\n"
"s_waitcnt lgkmcnt(0)\n"
"s_sub_i32 s64, s70, s64\n"
"s_getpc_b64 s[72:73]\n"
".Lpc44_%=:\n"
"s_max_i32 s64, s64, 0\n"
"s_min_i32 s64, s64, 2047\n"
"s_lshr_b32 s64, s64, 1\n"
"s_andn2_b32 s64, s64, 3\n"
"s_sub_u32 s64, .Lsled43_%=-.Lpc44_%=, s64\n"
"s_add_u32 s64, s72, s64\n"
"s_addc_u32 s65, s73, 0\n"
"s_setpc_b64 s[64:65]\n"
"s_nop 1\n"
"s_nop 1\n"
"s_nop 1\n"
"s_nop 1\n"
"s_nop 1\n"
"s_nop 1\n"
"s_nop 1\n"
"s_nop 1\n"
"s_nop 1\n"
"s_nop 1\n"
"s_nop 1\n"
"s_nop 1\n"
"s_nop 1\n"
"s_nop 1\n"
"s_nop 1\n"
"s_nop 1\n"
"s_nop 1\n"
"s_nop 1\n"
"s_nop 1\n"
"s_nop 1\n"
"s_nop 1\n"
"s_nop 1\n"
"s_nop 1\n"
"s_nop 1\n"
"s_nop 1\n"
"s_nop 1\n"
"s_nop 1\n"
"s_nop 1\n"
"s_nop 1\n"
"s_nop 1\n"
"s_nop 1\n"
"s_nop 1\n"
"s_nop 1\n"
"s_nop 1\n"
"s_nop 1\n"
"s_nop 1\n"
"s_nop 1\n"
"s_nop 1\n"
"s_nop 1\n"
"s_nop 1\n"
"s_nop 1\n"
"s_nop 1\n"
"s_nop 1\n"
"s_nop 1\n"
"s_nop 1\n"
"s_nop 1\n"
"s_nop 1\n"
"s_nop 1\n"
"s_nop 1\n"
"s_nop 1\n"
"s_nop 1\n"
"s_nop 1\n"
"s_nop 1\n"
"s_nop 1\n"
"s_nop 1\n"
"s_nop 1\n"
"s_nop 1\n"
"s_nop 1\n"
"s_nop 1\n"
"s_nop 1\n"
"s_nop 1\n"
"s_nop 1\n"
"s_nop 1\n"
"s_nop 1\n"
"s_nop 1\n"
"s_nop 1\n"
"s_nop 1\n"
"s_nop 1\n"
"s_nop 1\n"
"s_nop 1\n"
"s_nop 1\n"
"s_nop 1\n"
"s_nop 1\n"
"s_nop 1\n"
"s_nop 1\n"
"s_nop 1\n"
"s_nop 1\n"
"s_nop 1\n"
"s_nop 1\n"
"s_nop 1\n"
"s_nop 1\n"
"s_nop 1\n"
"s_nop 1\n"
"s_nop 1\n"
"s_nop 1\n"
"s_nop 1\n"
"s_nop 1\n"
"s_nop 1\n"
"s_nop 1\n"
"s_nop 1\n"
"s_nop 1\n"
"s_nop 1\n"
"s_nop 1\n"
"s_nop 1\n"
"s_nop 1\n"
"s_nop 1\n"
"s_nop 1\n"
"s_nop 1\n"
"s_nop 1\n"
"s_nop 1\n"
"s_nop 1\n"
"s_nop 1\n"
"s_nop 1\n"
"s_nop 1\n"
"s_nop 1\n"
"s_nop 1\n"
"s_nop 1\n"
"s_nop 1\n"
"s_nop 1\n"
"s_nop 1\n"
"s_nop 1\n"
"s_nop 1\n"
"s_nop 1\n"
"s_nop 1\n"
"s_nop 1\n"
"s_nop 1\n"
"s_nop 1\n"
"s_nop 1\n"
"s_nop 1\n"
"s_nop 1\n"
"s_nop 1\n"
"s_nop 1\n"
"s_nop 1\n"
"s_nop 1\n"
"s_nop 1\n"
"s_nop 1\n"
"s_nop 1\n"
"s_nop 1\n"
"s_nop 1\n"
"s_nop 1\n"
"s_nop 1\n"
"s_nop 1\n"
"s_nop 1\n"
"s_nop 1\n"
"s_nop 1\n"
"s_nop 1\n"
"s_nop 1\n"
"s_nop 1\n"
"s_nop 1\n"
"s_nop 1\n"
"s_nop 1\n"
"s_nop 1\n"
"s_nop 1\n"
"s_nop 1\n"
"s_nop 1\n"
"s_nop 1\n"
"s_nop 1\n"
"s_nop 1\n"
"s_nop 1\n"
"s_nop 1\n"
"s_nop 1\n"
"s_nop 1\n"
"s_nop 1\n"
"s_nop 1\n"
"s_nop 1\n"
"s_nop 1\n"
"s_nop 1\n"
"s_nop 1\n"
"s_nop 1\n"
"s_nop 1\n"
"s_nop 1\n"
"s_nop 1\n"
"s_nop 1\n"
"s_nop 1\n"
"s_nop 1\n"
"s_nop 1\n"
"s_nop 1\n"
"s_nop 1\n"
"s_nop 1\n"
"s_nop 1\n"
"s_nop 1\n"
"s_nop 1\n"
"s_nop 1\n"
"s_nop 1\n"
"s_nop 1\n"
"s_nop 1\n"
"s_nop 1\n"
"s_nop 1\n"
"s_nop 1\n"
"s_nop 1\n"
"s_nop 1\n"
"s_nop 1\n"
"s_nop 1\n"
"s_nop 1\n"
"s_nop 1\n"
"s_nop 1\n"
"s_nop 1\n"
"s_nop 1\n"
"s_nop 1\n"
"s_nop 1\n"
"s_nop 1\n"
"s_nop 1\n"
"s_nop 1\n"
"s_nop 1\n"
"s_nop 1\n"
"s_nop 1\n"
"s_nop 1\n"
"s_nop 1\n"
"s_nop 1\n"
"s_nop 1\n"
"s_nop 1\n"
"s_nop 1\n"
"s_nop 1\n"
"s_nop 1\n"
"s_nop 1\n"
"s_nop 1\n"
"s_nop 1\n"
"s_nop 1\n"
"s_nop 1\n"
"s_nop 1\n"
"s_nop 1\n"
"s_nop 1\n"
"s_nop 1\n"
"s_nop 1\n"
"s_nop 1\n"
"s_nop 1\n"
"s_nop 1\n"
"s_nop 1\n"
"s_nop 1\n"
"s_nop 1\n"
"s_nop 1\n"
"s_nop 1\n"
"s_nop 1\n"
"s_nop 1\n"
"s_nop 1\n"
"s_nop 1\n"
"s_nop 1\n"
"s_nop 1\n"
"s_nop 1\n"
"s_nop 1\n"
"s_nop 1\n"
"s_nop 1\n"
"s_nop 1\n"
"s_nop 1\n"
"s_nop 1\n"
"s_nop 1\n"
"s_nop 1\n"
"s_nop 1\n"
"s_nop 1\n"
"s_nop 1\n"
"s_nop 1\n"
"s_nop 1\n"
"s_nop 1\n"
"s_nop 1\n"
"s_nop 1\n"
"s_nop 1\n"
"s_nop 1\n"
"s_nop 1\n"
"s_nop 1\n"
"s_nop 1\n"
"s_nop 1\n"
"s_nop 1\n"
"s_nop 1\n"
"s_nop 1\n"
"s_nop 1\n"
"s_nop 1\n"
".Lsled43_%=:\n"
"s_cmp_lt_u32 s76, 63\n"
"s_waitcnt lgkmcnt(0)\n"
"s_cbranch_scc1 .Ltop33_%=\n"
"s_endpgm\n"
".Ltsa39_%=:\n"
"s_sub_u32 s78, s78, 1\n"
"s_cbranch_scc1 .Lfail5_%=\n"
"buffer_load_dword v29, v125, s[60:63], s12 offen sc1\n"
"s_waitcnt vmcnt(0)\n"
"s_branch .Ltsw38_%=\n"
".Lent17_%=:\n"
"s_lshl_b32 s13, s38, 8\n"
"s_add_u32 s13, s13, 3162112\n"
"s_xor_b32 s10, s39, 1\n"
"s_mov_b32 s11, 0\n"
"s_lshl_b32 s64, s39, 9\n"
"s_xor_b32 s65, s64, 512\n"
"buffer_load_dwordx4 v[0:3], v118, s[56:59], s65 offen\n"
"s_xor_b32 s65, s64, 640\n"
"buffer_load_dwordx4 v[4:7], v118, s[56:59], s65 offen\n"
"s_waitcnt lgkmcnt(0)\n"
"s_branch .LphA46_%=\n"
".Lretry47_%=:\n"
"s_sub_u32 s78, s78, 1\n"
"s_cbranch_scc1 .Lfail5_%=\n"
"s_waitcnt lgkmcnt(0)\n"
"buffer_load_dwordx4 v[0:3], v125, s[60:63], s94 offen sc1\n"
"buffer_load_dwordx4 v[4:7], v125, s[60:63], s95 offen sc1\n"
"s_nop 4\n"
"v_mov_b32 v80, 0\n"
"v_mov_b32 v81, 0\n"
"v_mov_b32 v82, 0\n"
"v_mov_b32 v83, 0\n"
"v_mov_b32 v84, 0\n"
"v_mov_b32 v85, 0\n"
"v_mov_b32 v86, 0\n"
"v_mov_b32 v87, 0\n"
"v_mov_b32 v88, 0\n"
"v_mov_b32 v89, 0\n"
"v_mov_b32 v90, 0\n"
"v_mov_b32 v91, 0\n"
"v_mov_b32 v92, 0\n"
"v_mov_b32 v93, 0\n"
"v_mov_b32 v94, 0\n"
"v_mov_b32 v95, 0\n"
"v_mov_b32 v96, 0\n"
"v_mov_b32 v97, 0\n"
"v_mov_b32 v98, 0\n"
"v_mov_b32 v99, 0\n"
"v_mov_b32 v100, 0\n"
"v_mov_b32 v101, 0\n"
"v_mov_b32 v102, 0\n"
"v_mov_b32 v103, 0\n"
"v_mov_b32 v116, 0\n"
"s_branch .LphA46_%=\n"
".p2align 6\n"
".Ltop45_%=:\n"
"s_barrier\n"
"buffer_load_dwordx4 v[0:3], v125, s[60:63], s94 offen sc1\n"
"s_nop 4\n"
"buffer_load_dwordx4 v[4:7], v125, s[60:63], s95 offen sc1\n"
".LphA46_%=:\n"
"s_waitcnt vmcnt(2)\n"
"ds_write_b128 v125, v[104:107] offset:4096\n"
"ds_write_b128 v125, v[108:111] offset:5120\n"
"v_min_u32 v78, 1, v125\n"
"v_or_b32 v78, s39, v78\n"
"v_lshlrev_b32 v78, 27, v78\n"
"s_add_u32 s64, s76, 1\n"
"v_mov_b32 v74, s64\n"
"s_waitcnt vmcnt(1)\n"
"v_smfmac_f32_16x16x64_bf16 v[80:83], v[0:3], a[64:71], v124\n"
"ds_write_b128 v125, v[0:3] offset:23552\n"
"v_smfmac_f32_16x16x64_bf16 v[84:87], v[0:3], a[72:79], v124\n"
"v_max3_u32 v116, v0, v2, v116\n"
"v_smfmac_f32_16x16x64_bf16 v[88:91], v[0:3], a[192:199], v124\n"
"v_smfmac_f32_16x16x64_bf16 v[92:95], v[0:3], a[200:207], v124\n"
"s_waitcnt vmcnt(0)\n"
"ds_write_b128 v125, v[4:7] offset:24576\n"
"v_smfmac_f32_16x16x64_bf16 v[80:83], v[4:7], a[80:87], v124\n"
"v_max3_u32 v116, v4, v6, v116\n"
"v_smfmac_f32_16x16x64_bf16 v[84:87], v[4:7], a[88:95], v124\n"
"v_cmp_eq_u32 vcc, -1, v116\n"
"v_smfmac_f32_16x16x64_bf16 v[88:91], v[4:7], a[208:215], v124\n"
"s_cbranch_vccnz .Lretry47_%=\n"
"v_smfmac_f32_16x16x64_bf16 v[92:95], v[4:7], a[216:223], v124\n"
"s_waitcnt lgkmcnt(0)\n"
"s_barrier\n"
"ds_read_b128 v[8:11], v125 offset:25600\n"
"v_smfmac_f32_16x16x64_bf16 v[96:99], v[0:3], v[192:199], v124\n"
"ds_read_b128 v[12:15], v125 offset:26624\n"
"v_smfmac_f32_16x16x64_bf16 v[100:103], v[0:3], v[200:207], v124\n"
"ds_read_b128 v[16:19], v125 offset:19456\n"
"v_smfmac_f32_16x16x64_bf16 v[96:99], v[4:7], v[208:215], v124\n"
"ds_read_b128 v[20:23], v125 offset:20480\n"
"v_smfmac_f32_16x16x64_bf16 v[100:103], v[4:7], v[216:223], v124\n"
"s_waitcnt lgkmcnt(3)\n"
"ds_read_b128 v[24:27], v125 offset:21504\n"
"v_smfmac_f32_16x16x64_bf16 v[80:83], v[8:11], a[96:103], v124\n"
"ds_read_b128 v[28:31], v125 offset:22528\n"
"v_smfmac_f32_16x16x64_bf16 v[84:87], v[8:11], a[104:111], v124\n"
"s_waitcnt lgkmcnt(3)\n"
"buffer_store_dword v74, v78, s[60:63], s13 offen sc1\n"
"v_smfmac_f32_16x16x64_bf16 v[80:83], v[12:15], a[112:119], v124\n"
"ds_read_b128 v[32:35], v125 offset:0\n"
"v_smfmac_f32_16x16x64_bf16 v[84:87], v[12:15], a[120:127], v124\n"
"s_waitcnt lgkmcnt(2)\n"
"ds_read_b128 v[36:39], v125 offset:1024\n"
"v_smfmac_f32_16x16x64_bf16 v[80:83], v[16:19], a[0:7], v124\n"
"ds_read_b128 v[40:43], v125 offset:2048\n"
"v_smfmac_f32_16x16x64_bf16 v[84:87], v[16:19], a[8:15], v124\n"
"s_waitcnt lgkmcnt(3)\n"
"ds_read_b128 v[44:47], v125 offset:3072\n"
"v_smfmac_f32_16x16x64_bf16 v[80:83], v[20:23], a[16:23], v124\n"
"ds_read_b128 v[48:51], v125 offset:4096\n"
"v_mov_b32 v116, 0\n"
"v_smfmac_f32_16x16x64_bf16 v[84:87], v[20:23], a[24:31], v124\n"
"ds_read_b128 v[52:55], v125 offset:5120\n"
"v_mov_b32 v117, 0\n"
"v_smfmac_f32_16x16x64_bf16 v[80:83], v[24:27], a[32:39], v124\n"
"ds_read_b128 v[56:59], v125 offset:6144\n"
"v_mov_b32 v118, 0\n"
"v_smfmac_f32_16x16x64_bf16 v[84:87], v[24:27], a[40:47], v124\n"
"ds_read_b128 v[60:63], v125 offset:7168\n"
"v_mov_b32 v119, 0\n"
"v_smfmac_f32_16x16x64_bf16 v[80:83], v[28:31], a[48:55], v124\n"
"v_smfmac_f32_16x16x64_bf16 v[84:87], v[28:31], a[56:63], v124\n"
"v_smfmac_f32_16x16x64_bf16 v[88:91], v[8:11], a[224:231], v124\n"
"s_mov_b64 vcc, s[66:67]\n"
"buffer_store_dwordx4 v[112:115], v125, s[60:63], s14 offen offset:2048 sc1\n"
"v_smfmac_f32_16x16x64_bf16 v[92:95], v[8:11], a[232:239], v124\n"
"v_add_f32 v80, v80, v81\n"
"v_smfmac_f32_16x16x64_bf16 v[88:91], v[12:15], a[240:247], v124\n"
"v_add_f32 v82, v82, v83\n"
"v_smfmac_f32_16x16x64_bf16 v[92:95], v[12:15], a[248:255], v124\n"
"v_add_f32 v84, v84, v85\n"
"v_smfmac_f32_16x16x64_bf16 v[88:91], v[16:19], a[128:135], v124\n"
"v_add_f32 v86, v86, v87\n"
"v_smfmac_f32_16x16x64_bf16 v[92:95], v[16:19], a[136:143], v124\n"
"v_cvt_pk_bf16_f32 v80, v80, v84\n"
"v_cvt_pk_bf16_f32 v82, v82, v86\n"
"v_smfmac_f32_16x16x64_bf16 v[88:91], v[20:23], a[144:151], v124\n"
"ds_write2_b32 v67, v80, v82 offset0:0 offset1:4\n"
"v_smfmac_f32_16x16x64_bf16 v[92:95], v[20:23], a[152:159], v124\n"
"v_smfmac_f32_16x16x64_bf16 v[88:91], v[24:27], a[160:167], v124\n"
"v_smfmac_f32_16x16x64_bf16 v[92:95], v[24:27], a[168:175], v124\n"
"v_smfmac_f32_16x16x64_bf16 v[88:91], v[28:31], a[176:183], v124\n"
"s_waitcnt lgkmcnt(0)\n"
"s_barrier\n"
"ds_read_b128 v[80:83], v75 offset:0\n"
"v_smfmac_f32_16x16x64_bf16 v[92:95], v[28:31], a[184:191], v124\n"
"ds_read_b128 v[84:87], v75 offset:1600\n"
"v_smfmac_f32_16x16x64_bf16 v[96:99], v[8:11], v[224:231], v124\n"
"ds_read_b128 v[0:3], v75 offset:3200\n"
"v_smfmac_f32_16x16x64_bf16 v[100:103], v[8:11], v[232:239], v124\n"
"ds_read_b128 v[4:7], v75 offset:4800\n"
"v_add_f32 v88, v88, v89\n"
"v_smfmac_f32_16x16x64_bf16 v[96:99], v[12:15], v[240:247], v124\n"
"v_add_f32 v90, v90, v91\n"
"v_smfmac_f32_16x16x64_bf16 v[100:103], v[12:15], v[248:255], v124\n"
"v_add_f32 v92, v92, v93\n"
"v_smfmac_f32_16x16x64_bf16 v[96:99], v[16:19], v[128:135], v124\n"
"v_add_f32 v94, v94, v95\n"
"s_waitcnt lgkmcnt(3)\n"
"v_mfma_f32_16x16x32_bf16 v[104:107], v[32:35], v[80:83], 0\n"
"v_cvt_pk_bf16_f32 v88, v88, v92\n"
"v_cvt_pk_bf16_f32 v90, v90, v94\n"
"v_mfma_f32_16x16x32_bf16 v[108:111], v[36:39], v[80:83], 0\n"
"s_waitcnt lgkmcnt(2)\n"
"ds_write2_b32 v67, v88, v90 offset0:32 offset1:36\n"
"v_mfma_f32_16x16x32_bf16 v[104:107], v[40:43], v[84:87], v[104:107]\n"
"v_mfma_f32_16x16x32_bf16 v[108:111], v[44:47], v[84:87], v[108:111]\n"
"s_waitcnt lgkmcnt(1)\n"
"v_mfma_f32_16x16x32_bf16 v[104:107], v[48:51], v[0:3], v[104:107]\n"
"v_mfma_f32_16x16x32_bf16 v[108:111], v[52:55], v[0:3], v[108:111]\n"
"s_waitcnt lgkmcnt(0)\n"
"s_barrier\n"
"ds_read_b128 v[88:91], v79 offset:0\n"
"v_mfma_f32_16x16x32_bf16 v[104:107], v[56:59], v[4:7], v[104:107]\n"
"ds_read_b128 v[92:95], v79 offset:1600\n"
"v_mfma_f32_16x16x32_bf16 v[108:111], v[60:63], v[4:7], v[108:111]\n"
"ds_read_b128 v[8:11], v79 offset:3200\n"
"v_smfmac_f32_16x16x64_bf16 v[100:103], v[16:19], v[136:143], v124\n"
"ds_read_b128 v[12:15], v79 offset:4800\n"
"v_smfmac_f32_16x16x64_bf16 v[96:99], v[20:23], v[144:151], v124\n"
"ds_read_b128 v[32:35], v127 offset:0\n"
"v_max3_f32 v66, v104, v105, v106\n"
"v_smfmac_f32_16x16x64_bf16 v[100:103], v[20:23], v[152:159], v124\n"
"s_waitcnt lgkmcnt(4)\n"
"ds_read_b128 v[36:39], v127 offset:1024\n"
"v_mov_b32 v16, 0\n"
"v_mfma_f32_16x16x32_bf16 v[112:115], v[88:91], v[80:83], 0\n"
"v_max3_f32 v66, v66, v107, v108\n"
"v_max3_f32 v66, v66, v109, v110\n"
"v_smfmac_f32_16x16x64_bf16 v[96:99], v[24:27], v[160:167], v124\n"
"s_waitcnt lgkmcnt(4)\n"
"ds_read_b128 v[40:43], v127 offset:2048\n"
"v_max_f32 v70, v66, v111\n"
"v_mfma_f32_16x16x32_bf16 v[112:115], v[92:95], v[84:87], v[112:115]\n"
"v_max_f32 v66, v66, v111\n"
"v_cndmask_b32 v104, v104, v108, vcc\n"
"v_smfmac_f32_16x16x64_bf16 v[100:103], v[24:27], v[168:175], v124\n"
"s_waitcnt lgkmcnt(3)\n"
"ds_read_b128 v[44:47], v127 offset:3072\n"
"v_cndmask_b32 v105, v105, v109, vcc\n"
"v_mfma_f32_16x16x32_bf16 v[112:115], v[8:11], v[0:3], v[112:115]\n"
"v_permlane32_swap_b32 v66, v70\n"
"v_smfmac_f32_16x16x64_bf16 v[96:99], v[28:31], v[176:183], v124\n"
"ds_read_b128 v[48:51], v127 offset:4096\n"
"v_max_f32 v66, v66, v70\n"
"v_mfma_f32_16x16x32_bf16 v[112:115], v[12:15], v[4:7], v[112:115]\n"
"v_cndmask_b32 v106, v106, v110, vcc\n"
"v_cndmask_b32 v107, v107, v111, vcc\n"
"v_smfmac_f32_16x16x64_bf16 v[100:103], v[28:31], v[184:191], v124\n"
"ds_read_b128 v[52:55], v127 offset:5120\n"
"v_mov_b32 v17, 0\n"
"s_nop 2\n"
"v_max3_f32 v66, v66, v112, v113\n"
"v_max3_f32 v70, v66, v114, v115\n"
"v_max3_f32 v66, v66, v114, v115\n"
"v_mov_b32 v18, 0\n"
"v_mov_b32 v19, 0\n"
"v_permlane16_swap_b32 v66, v70\n"
"v_max_f32 v66, v66, v70\n"
"v_mul_f32 v66, s68, v66\n"
"v_fma_f32 v104, v104, s68, -v66\n"
"v_fma_f32 v105, v105, s68, -v66\n"
"v_exp_f32 v104, v104\n"
"v_fma_f32 v112, v112, s68, -v66\n"
"v_exp_f32 v105, v105\n"
"v_fma_f32 v106, v106, s68, -v66\n"
"v_exp_f32 v112, v112\n"
"v_fma_f32 v107, v107, s68, -v66\n"
"v_exp_f32 v106, v106\n"
"v_fma_f32 v113, v113, s68, -v66\n"
"v_exp_f32 v107, v107\n"
"v_exp_f32 v113, v113\n"
"v_cvt_pk_bf16_f32 v20, v104, v105\n"
"v_cvt_pk_bf16_f32 v21, v112, 0\n"
"v_cvt_pk_bf16_f32 v22, v106, v107\n"
"v_cvt_pk_bf16_f32 v23, v113, 0\n"
"v_cvt_pk_bf16_f32 v66, v96, v97\n"
"v_cvt_pk_bf16_f32 v70, v98, v99\n"
"v_mfma_f32_16x16x32_bf16 v[24:27], v[20:23], v[120:123], 0\n"
"v_cvt_pk_bf16_f32 v74, v100, v101\n"
"v_cvt_pk_bf16_f32 v78, v102, v103\n"
"v_smfmac_f32_16x16x64_bf16 v[116:119], v[20:23], v[64:71], v124 abid:1\n"
"ds_read_b128 v[56:59], v127 offset:6144\n"
"v_mov_b32 v28, 0\n"
"v_smfmac_f32_16x16x64_bf16 v[16:19], v[20:23], v[72:79], v124 abid:1\n"
"buffer_load_dwordx2 v[30:31], off, s[60:63], s71 sc1\n"
"ds_read_b128 v[60:63], v127 offset:7168\n"
"v_add_f32 v24, v24, v25\n"
"v_add_f32 v26, v26, v27\n"
"v_rcp_f32 v24, v24\n"
"v_rcp_f32 v26, v26\n"
"v_add_f32 v116, v116, v117\n"
"v_add_f32 v118, v118, v119\n"
"v_mul_f32 v116, v116, v24\n"
"v_mul_f32 v118, v118, v26\n"
"v_add_f32 v16, v16, v17\n"
"v_add_f32 v18, v18, v19\n"
"v_mul_f32 v16, v16, v24\n"
"v_mul_f32 v18, v18, v26\n"
"v_cvt_pk_bf16_f32 v116, v116, v16\n"
"v_cvt_pk_bf16_f32 v118, v118, v18\n"
"ds_write2_b32 v71, v116, v118 offset0:0 offset1:8\n"
"v_mfma_f32_4x4x1_16b_f32 v[96:99], v28, v28, 0\n"
"ds_read_b128 v[80:83], v127 offset:8192\n"
"v_mfma_f32_4x4x1_16b_f32 v[100:103], v28, v28, 0\n"
"ds_read_b128 v[84:87], v127 offset:9216\n"
"v_mfma_f32_4x4x1_16b_f32 v[104:107], v28, v28, 0\n"
"ds_read_b128 v[88:91], v127 offset:10240\n"
"v_mfma_f32_4x4x1_16b_f32 v[108:111], v28, v28, 0\n"
"s_waitcnt lgkmcnt(3)\n"
"s_barrier\n"
"ds_read_b128 v[16:19], v125 offset:8192\n"
"ds_read_b128 v[20:23], v125 offset:9216\n"
"ds_read_b128 v[92:95], v127 offset:11264\n"
"s_cmp_eq_u32 s76, 62\n"
"s_cbranch_scc1 .Llr48_%=\n"
".Llrb49_%=:\n"
"s_waitcnt lgkmcnt(2)\n"
"v_smfmac_f32_16x16x64_bf16 v[96:99], v[16:19], v[32:39], v124\n"
"ds_read_b128 v[0:3], v127 offset:12288\n"
"v_smfmac_f32_16x16x64_bf16 v[100:103], v[16:19], v[40:47], v124\n"
"ds_read_b128 v[4:7], v127 offset:13312\n"
"v_smfmac_f32_16x16x64_bf16 v[104:107], v[16:19], v[48:55], v124\n"
"ds_read_b128 v[8:11], v127 offset:14336\n"
"v_smfmac_f32_16x16x64_bf16 v[108:111], v[16:19], v[56:63], v124\n"
"s_waitcnt lgkmcnt(4)\n"
"ds_read_b128 v[12:15], v127 offset:15360\n"
"v_smfmac_f32_16x16x64_bf16 v[96:99], v[20:23], v[80:87], v124\n"
"s_waitcnt lgkmcnt(4)\n"
"s_add_u32 s14, s96, s79\n"
"v_smfmac_f32_16x16x64_bf16 v[100:103], v[20:23], v[88:95], v124\n"
"s_waitcnt lgkmcnt(2)\n"
"v_smfmac_f32_16x16x64_bf16 v[104:107], v[20:23], v[0:7], v124\n"
"s_waitcnt lgkmcnt(0)\n"
"v_smfmac_f32_16x16x64_bf16 v[108:111], v[20:23], v[8:15], v124\n"
"v_add_f32 v96, v96, v97\n"
"v_add_f32 v98, v98, v99\n"
"v_add_f32 v100, v100, v101\n"
"v_add_f32 v102, v102, v103\n"
"v_cvt_pk_bf16_f32 v116, v96, v100\n"
"v_cvt_pk_bf16_f32 v118, v98, v102\n"
"v_add_f32 v104, v104, v105\n"
"v_add_f32 v106, v106, v107\n"
"v_add_f32 v108, v108, v109\n"
"v_add_f32 v110, v110, v111\n"
"v_cvt_pk_bf16_f32 v117, v104, v108\n"
"v_cvt_pk_bf16_f32 v119, v106, v110\n"
"buffer_store_dwordx4 v[116:119], v126, s[60:63], s96 offen sc1\n"
"v_mov_b32 v80, 0\n"
"v_mov_b32 v81, 0\n"
"v_mov_b32 v82, 0\n"
"v_mov_b32 v83, 0\n"
"v_mov_b32 v84, 0\n"
"v_mov_b32 v85, 0\n"
"v_mov_b32 v86, 0\n"
"v_mov_b32 v87, 0\n"
"v_mov_b32 v88, 0\n"
"v_mov_b32 v89, 0\n"
"v_mov_b32 v90, 0\n"
"v_mov_b32 v91, 0\n"
"v_mov_b32 v92, 0\n"
"v_mov_b32 v93, 0\n"
"v_mov_b32 v94, 0\n"
"v_mov_b32 v95, 0\n"
"v_mov_b32 v96, 0\n"
"v_mov_b32 v97, 0\n"
"v_mov_b32 v98, 0\n"
"v_mov_b32 v99, 0\n"
"v_mov_b32 v100, 0\n"
"v_mov_b32 v101, 0\n"
"v_mov_b32 v102, 0\n"
"v_mov_b32 v103, 0\n"
"v_mov_b32 v116, 0\n"
"v_mov_b32 v112, -1\n"
"v_mov_b32 v113, -1\n"
"v_mov_b32 v114, -1\n"
"v_mov_b32 v115, -1\n"
"s_waitcnt vmcnt(1)\n"
"v_readfirstlane_b32 s64, v30\n"
"v_readfirstlane_b32 s65, v31\n"
"s_nop 0\n"
"s_sub_u32 s65, s65, s35\n"
"s_cmp_le_u32 s65, 64\n"
"s_cselect_b32 s65, s65, 0\n"
"s_cmp_eq_u32 s64, s34\n"
"s_cselect_b32 s65, s65, 0\n"
"s_add_u32 s76, s76, 1\n"
"s_cmp_gt_u32 s65, s76\n"
"s_cbranch_scc1 .Lkvok50_%=\n"
"s_add_u32 s74, s76, 1\n"
"s_mov_b32 s78, 16384\n"
".Lsw51_%=:\n"
"buffer_load_dwordx2 v[30:31], off, s[60:63], s71 sc1\n"
"s_waitcnt vmcnt(0)\n"
"v_readfirstlane_b32 s64, v30\n"
"v_readfirstlane_b32 s65, v31\n"
"s_nop 0\n"
"s_sub_u32 s65, s65, s35\n"
"s_cmp_le_u32 s65, 64\n"
"s_cselect_b32 s65, s65, 0\n"
"s_cmp_eq_u32 s64, s34\n"
"s_cselect_b32 s65, s65, 0\n"
"s_cmp_ge_u32 s65, s74\n"
"s_cbranch_scc1 .Lswok52_%=\n"
"s_sub_u32 s78, s78, 1\n"
"s_cbranch_scc1 .Lfail5_%=\n"
"s_sleep 2\n"
"s_branch .Lsw51_%=\n"
".Lswok52_%=:\n"
".Lkvok50_%=:\n"
"s_and_b32 s64, s76, 7\n"
"s_lshl_b32 s65, s76, 12\n"
"s_xor_b32 s65, s65, s77\n"
"s_and_b32 s65, s65, 8192\n"
"s_lshl_b32 s64, s64, 20\n"
"s_add_u32 s64, s64, s65\n"
"s_add_u32 s64, s64, s97\n"
"s_add_u32 s65, s64, 16384\n"
"s_barrier\n"
"buffer_load_dwordx4 v[104:107], v125, s[60:63], s64 offen offset:0 sc1\n"
"buffer_load_dwordx4 v[108:111], v125, s[60:63], s64 offen offset:1024 sc1\n"
"v_lshrrev_b32 v118, 1, v125\n"
"buffer_load_dwordx2 v[64:65], v118, s[60:63], s65 offen offset:0 sc1\n"
"buffer_load_dwordx2 v[68:69], v118, s[60:63], s65 offen offset:512 sc1\n"
"buffer_load_dwordx2 v[72:73], v118, s[60:63], s65 offen offset:1024 sc1\n"
"buffer_load_dwordx2 v[76:77], v118, s[60:63], s65 offen offset:1536 sc1\n"
"s_and_b32 s64, s76, 15\n"
"s_lshl_b32 s64, s64, 16\n"
"s_add_u32 s101, s64, s77\n"
"s_lshr_b32 s64, s64, 5\n"
"s_and_b32 s64, s64, 0x4000\n"
"s_xor_b32 s64, s64, s33\n"
"s_add_u32 s64, s101, s64\n"
"s_lshl_b32 s65, s39, 15\n"
"s_add_u32 s64, s64, s65\n"
"s_xor_b32 s94, s64, 32768\n"
"s_xor_b32 s95, s94, 1024\n"
"s_xor_b32 s100, s64, 34816\n"
"v_xor_b32 v126, 0x4000, v126\n"
"s_xor_b32 s79, s79, 0x4000\n"
"s_xor_b32 s96, s96, 524288\n"
"s_mov_b32 s78, 16384\n"
"s_add_u32 s12, s12, 16384\n"
"s_add_u32 s13, s13, 0\n"
"s_xor_b32 s71, s71, 128\n"
"s_cmp_lt_u32 s76, 63\n"
"s_waitcnt lgkmcnt(0)\n"
"s_cbranch_scc1 .Ltop45_%=\n"
"s_endpgm\n"
".Llr48_%=:\n"
"v_mov_b32 v29, 64\n"
"s_mov_b64 exec, s[10:11]\n"
"buffer_store_dword v29, off, s[60:63], s13 sc1\n"
"s_mov_b64 exec, -1\n"
"s_branch .Llrb49_%=\n"
".Lent18_%=:\n"
"s_lshl_b32 s64, s39, 9\n"
"s_xor_b32 s65, s64, 768\n"
"buffer_load_dwordx4 v[0:3], v118, s[56:59], s65 offen\n"
"s_xor_b32 s65, s64, 896\n"
"buffer_load_dwordx4 v[4:7], v118, s[56:59], s65 offen\n"
"s_waitcnt lgkmcnt(0)\n"
"s_branch .LphA54_%=\n"
".Lretry55_%=:\n"
"s_sub_u32 s78, s78, 1\n"
"s_cbranch_scc1 .Lfail5_%=\n"
"s_waitcnt lgkmcnt(0)\n"
"buffer_load_dwordx4 v[0:3], v125, s[60:63], s94 offen sc1\n"
"buffer_load_dwordx4 v[4:7], v125, s[60:63], s95 offen sc1\n"
"s_nop 4\n"
"v_mov_b32 v80, 0\n"
"v_mov_b32 v81, 0\n"
"v_mov_b32 v82, 0\n"
"v_mov_b32 v83, 0\n"
"v_mov_b32 v84, 0\n"
"v_mov_b32 v85, 0\n"
"v_mov_b32 v86, 0\n"
"v_mov_b32 v87, 0\n"
"v_mov_b32 v88, 0\n"
"v_mov_b32 v89, 0\n"
"v_mov_b32 v90, 0\n"
"v_mov_b32 v91, 0\n"
"v_mov_b32 v92, 0\n"
"v_mov_b32 v93, 0\n"
"v_mov_b32 v94, 0\n"
"v_mov_b32 v95, 0\n"
"v_mov_b32 v96, 0\n"
"v_mov_b32 v97, 0\n"
"v_mov_b32 v98, 0\n"
"v_mov_b32 v99, 0\n"
"v_mov_b32 v100, 0\n"
"v_mov_b32 v101, 0\n"
"v_mov_b32 v102, 0\n"
"v_mov_b32 v103, 0\n"
"v_mov_b32 v116, 0\n"
"s_branch .LphA54_%=\n"
".p2align 6\n"
".Ltop53_%=:\n"
"s_barrier\n"
"buffer_load_dwordx4 v[0:3], v125, s[60:63], s94 offen sc1\n"
"s_nop 4\n"
"buffer_load_dwordx4 v[4:7], v125, s[60:63], s95 offen sc1\n"
".LphA54_%=:\n"
"s_memtime s[72:73]\n"
"s_memrealtime s[16:17]\n"
"s_nop 2\n"
"s_memrealtime s[18:19]\n"
"s_nop 2\n"
"s_memrealtime s[20:21]\n"
"s_nop 2\n"
"s_memrealtime s[22:23]\n"
"s_nop 2\n"
"s_memrealtime s[24:25]\n"
"s_nop 2\n"
"s_memrealtime s[26:27]\n"
"s_nop 2\n"
"s_memrealtime s[28:29]\n"
"s_nop 2\n"
"s_waitcnt lgkmcnt(0)\n"
"s_mul_i32 s74, s16, 24\n"
"s_mov_b32 s17, 0\n"
"s_cmp_eq_u32 s18, s16\n"
"s_addc_u32 s17, s17, 0\n"
"s_cmp_eq_u32 s20, s16\n"
"s_addc_u32 s17, s17, 0\n"
"s_cmp_eq_u32 s22, s16\n"
"s_addc_u32 s17, s17, 0\n"
"s_cmp_eq_u32 s24, s16\n"
"s_addc_u32 s17, s17, 0\n"
"s_cmp_eq_u32 s26, s16\n"
"s_addc_u32 s17, s17, 0\n"
"s_cmp_eq_u32 s28, s16\n"
"s_addc_u32 s17, s17, 0\n"
"s_lshl_b32 s17, s17, 4\n"
"s_sub_u32 s74, s74, s17\n"
"s_sub_u32 s74, s74, s72\n"
"s_mul_i32 s75, s15, 8\n"
"s_ashr_i32 s75, s75, 5\n"
"s_add_u32 s75, s75, s74\n"
"s_waitcnt vmcnt(2)\n"
"ds_write_b128 v125, v[104:107] offset:6144\n"
"ds_write_b128 v125, v[108:111] offset:7168\n"
"v_min_u32 v78, 1, v125\n"
"v_lshlrev_b32 v78, 27, v78\n"
"s_waitcnt vmcnt(1)\n"
"v_smfmac_f32_16x16x64_bf16 v[80:83], v[0:3], a[96:103], v124\n"
"ds_write_b128 v125, v[0:3] offset:25600\n"
"v_smfmac_f32_16x16x64_bf16 v[84:87], v[0:3], a[104:111], v124\n"
"v_max3_u32 v116, v0, v2, v116\n"
"v_smfmac_f32_16x16x64_bf16 v[88:91], v[0:3], a[224:231], v124\n"
"v_smfmac_f32_16x16x64_bf16 v[92:95], v[0:3], a[232:239], v124\n"
"s_waitcnt vmcnt(0)\n"
"ds_write_b128 v125, v[4:7] offset:26624\n"
"v_smfmac_f32_16x16x64_bf16 v[80:83], v[4:7], a[112:119], v124\n"
"v_max3_u32 v116, v4, v6, v116\n"
"v_smfmac_f32_16x16x64_bf16 v[84:87], v[4:7], a[120:127], v124\n"
"v_cmp_eq_u32 vcc, -1, v116\n"
"v_smfmac_f32_16x16x64_bf16 v[88:91], v[4:7], a[240:247], v124\n"
"s_cbranch_vccnz .Lretry55_%=\n"
"v_smfmac_f32_16x16x64_bf16 v[92:95], v[4:7], a[248:255], v124\n"
"s_waitcnt lgkmcnt(0)\n"
"s_barrier\n"
"ds_read_b128 v[8:11], v125 offset:19456\n"
"v_smfmac_f32_16x16x64_bf16 v[96:99], v[0:3], v[224:231], v124\n"
"s_memtime s[8:9]\n"
"ds_read_b128 v[12:15], v125 offset:20480\n"
"v_smfmac_f32_16x16x64_bf16 v[100:103], v[0:3], v[232:239], v124\n"
"ds_read_b128 v[16:19], v125 offset:21504\n"
"v_smfmac_f32_16x16x64_bf16 v[96:99], v[4:7], v[240:247], v124\n"
"ds_read_b128 v[20:23], v125 offset:22528\n"
"v_smfmac_f32_16x16x64_bf16 v[100:103], v[4:7], v[248:255], v124\n"
"s_waitcnt lgkmcnt(3)\n"
"ds_read_b128 v[24:27], v125 offset:23552\n"
"v_smfmac_f32_16x16x64_bf16 v[80:83], v[8:11], a[0:7], v124\n"
"ds_read_b128 v[28:31], v125 offset:24576\n"
"v_smfmac_f32_16x16x64_bf16 v[84:87], v[8:11], a[8:15], v124\n"
"s_waitcnt lgkmcnt(3)\n"
"s_add_u32 s64, s8, s75\n"
"s_min_u32 s64, s64, -2\n"
"v_mov_b32 v74, s64\n"
"v_smfmac_f32_16x16x64_bf16 v[80:83], v[12:15], a[16:23], v124\n"
"buffer_store_dword v74, v78, s[60:63], s13 offen sc1\n"
"v_smfmac_f32_16x16x64_bf16 v[84:87], v[12:15], a[24:31], v124\n"
"s_waitcnt lgkmcnt(1)\n"
"ds_read_b128 v[32:35], v125 offset:0\n"
"v_smfmac_f32_16x16x64_bf16 v[80:83], v[16:19], a[32:39], v124\n"
"ds_read_b128 v[36:39], v125 offset:1024\n"
"v_smfmac_f32_16x16x64_bf16 v[84:87], v[16:19], a[40:47], v124\n"
"s_waitcnt lgkmcnt(2)\n"
"ds_read_b128 v[40:43], v125 offset:2048\n"
"v_smfmac_f32_16x16x64_bf16 v[80:83], v[20:23], a[48:55], v124\n"
"ds_read_b128 v[44:47], v125 offset:3072\n"
"v_mov_b32 v116, 0\n"
"v_smfmac_f32_16x16x64_bf16 v[84:87], v[20:23], a[56:63], v124\n"
"ds_read_b128 v[48:51], v125 offset:4096\n"
"v_mov_b32 v117, 0\n"
"v_smfmac_f32_16x16x64_bf16 v[80:83], v[24:27], a[64:71], v124\n"
"ds_read_b128 v[52:55], v125 offset:5120\n"
"v_mov_b32 v118, 0\n"
"v_smfmac_f32_16x16x64_bf16 v[84:87], v[24:27], a[72:79], v124\n"
"ds_read_b128 v[56:59], v125 offset:6144\n"
"v_mov_b32 v119, 0\n"
"v_smfmac_f32_16x16x64_bf16 v[80:83], v[28:31], a[80:87], v124\n"
"ds_read_b128 v[60:63], v125 offset:7168\n"
"v_smfmac_f32_16x16x64_bf16 v[84:87], v[28:31], a[88:95], v124\n"
"v_smfmac_f32_16x16x64_bf16 v[88:91], v[8:11], a[128:135], v124\n"
"s_mov_b64 vcc, s[66:67]\n"
"v_smfmac_f32_16x16x64_bf16 v[92:95], v[8:11], a[136:143], v124\n"
"v_add_f32 v80, v80, v81\n"
"v_smfmac_f32_16x16x64_bf16 v[88:91], v[12:15], a[144:151], v124\n"
"v_add_f32 v82, v82, v83\n"
"v_smfmac_f32_16x16x64_bf16 v[92:95], v[12:15], a[152:159], v124\n"
"buffer_store_dwordx4 v[112:115], v125, s[60:63], s14 offen offset:3072 sc1\n"
"v_add_f32 v84, v84, v85\n"
"v_smfmac_f32_16x16x64_bf16 v[88:91], v[16:19], a[160:167], v124\n"
"v_add_f32 v86, v86, v87\n"
"v_smfmac_f32_16x16x64_bf16 v[92:95], v[16:19], a[168:175], v124\n"
"v_cvt_pk_bf16_f32 v80, v80, v84\n"
"v_cvt_pk_bf16_f32 v82, v82, v86\n"
"v_smfmac_f32_16x16x64_bf16 v[88:91], v[20:23], a[176:183], v124\n"
"ds_write2_b32 v67, v80, v82 offset0:0 offset1:4\n"
"v_smfmac_f32_16x16x64_bf16 v[92:95], v[20:23], a[184:191], v124\n"
"v_smfmac_f32_16x16x64_bf16 v[88:91], v[24:27], a[192:199], v124\n"
"v_smfmac_f32_16x16x64_bf16 v[92:95], v[24:27], a[200:207], v124\n"
"v_smfmac_f32_16x16x64_bf16 v[88:91], v[28:31], a[208:215], v124\n"
"s_waitcnt lgkmcnt(0)\n"
"s_barrier\n"
"ds_read_b128 v[80:83], v75 offset:0\n"
"v_smfmac_f32_16x16x64_bf16 v[92:95], v[28:31], a[216:223], v124\n"
"ds_read_b128 v[84:87], v75 offset:1600\n"
"v_smfmac_f32_16x16x64_bf16 v[96:99], v[8:11], v[128:135], v124\n"
"ds_read_b128 v[0:3], v75 offset:3200\n"
"v_smfmac_f32_16x16x64_bf16 v[100:103], v[8:11], v[136:143], v124\n"
"ds_read_b128 v[4:7], v75 offset:4800\n"
"v_add_f32 v88, v88, v89\n"
"v_smfmac_f32_16x16x64_bf16 v[96:99], v[12:15], v[144:151], v124\n"
"v_add_f32 v90, v90, v91\n"
"v_smfmac_f32_16x16x64_bf16 v[100:103], v[12:15], v[152:159], v124\n"
"v_add_f32 v92, v92, v93\n"
"v_smfmac_f32_16x16x64_bf16 v[96:99], v[16:19], v[160:167], v124\n"
"v_add_f32 v94, v94, v95\n"
"s_waitcnt lgkmcnt(3)\n"
"v_mfma_f32_16x16x32_bf16 v[104:107], v[32:35], v[80:83], 0\n"
"v_cvt_pk_bf16_f32 v88, v88, v92\n"
"v_cvt_pk_bf16_f32 v90, v90, v94\n"
"v_mfma_f32_16x16x32_bf16 v[108:111], v[36:39], v[80:83], 0\n"
"s_waitcnt lgkmcnt(2)\n"
"ds_write2_b32 v67, v88, v90 offset0:32 offset1:36\n"
"v_mfma_f32_16x16x32_bf16 v[104:107], v[40:43], v[84:87], v[104:107]\n"
"v_mfma_f32_16x16x32_bf16 v[108:111], v[44:47], v[84:87], v[108:111]\n"
"s_waitcnt lgkmcnt(1)\n"
"v_mfma_f32_16x16x32_bf16 v[104:107], v[48:51], v[0:3], v[104:107]\n"
"v_mfma_f32_16x16x32_bf16 v[108:111], v[52:55], v[0:3], v[108:111]\n"
"s_waitcnt lgkmcnt(0)\n"
"s_barrier\n"
"ds_read_b128 v[88:91], v79 offset:0\n"
"v_mfma_f32_16x16x32_bf16 v[104:107], v[56:59], v[4:7], v[104:107]\n"
"ds_read_b128 v[92:95], v79 offset:1600\n"
"v_mfma_f32_16x16x32_bf16 v[108:111], v[60:63], v[4:7], v[108:111]\n"
"ds_read_b128 v[8:11], v79 offset:3200\n"
"v_smfmac_f32_16x16x64_bf16 v[100:103], v[16:19], v[168:175], v124\n"
"ds_read_b128 v[12:15], v79 offset:4800\n"
"v_smfmac_f32_16x16x64_bf16 v[96:99], v[20:23], v[176:183], v124\n"
"ds_read_b128 v[32:35], v127 offset:0\n"
"v_max3_f32 v66, v104, v105, v106\n"
"v_smfmac_f32_16x16x64_bf16 v[100:103], v[20:23], v[184:191], v124\n"
"s_waitcnt lgkmcnt(4)\n"
"ds_read_b128 v[36:39], v127 offset:1024\n"
"v_mov_b32 v16, 0\n"
"v_mfma_f32_16x16x32_bf16 v[112:115], v[88:91], v[80:83], 0\n"
"v_max3_f32 v66, v66, v107, v108\n"
"v_max3_f32 v66, v66, v109, v110\n"
"v_smfmac_f32_16x16x64_bf16 v[96:99], v[24:27], v[192:199], v124\n"
"s_waitcnt lgkmcnt(4)\n"
"ds_read_b128 v[40:43], v127 offset:2048\n"
"v_max_f32 v70, v66, v111\n"
"v_mfma_f32_16x16x32_bf16 v[112:115], v[92:95], v[84:87], v[112:115]\n"
"v_max_f32 v66, v66, v111\n"
"v_cndmask_b32 v104, v104, v108, vcc\n"
"v_smfmac_f32_16x16x64_bf16 v[100:103], v[24:27], v[200:207], v124\n"
"s_waitcnt lgkmcnt(3)\n"
"ds_read_b128 v[44:47], v127 offset:3072\n"
"v_cndmask_b32 v105, v105, v109, vcc\n"
"v_mfma_f32_16x16x32_bf16 v[112:115], v[8:11], v[0:3], v[112:115]\n"
"v_permlane32_swap_b32 v66, v70\n"
"v_smfmac_f32_16x16x64_bf16 v[96:99], v[28:31], v[208:215], v124\n"
"ds_read_b128 v[48:51], v127 offset:4096\n"
"v_max_f32 v66, v66, v70\n"
"v_mfma_f32_16x16x32_bf16 v[112:115], v[12:15], v[4:7], v[112:115]\n"
"v_cndmask_b32 v106, v106, v110, vcc\n"
"v_cndmask_b32 v107, v107, v111, vcc\n"
"v_smfmac_f32_16x16x64_bf16 v[100:103], v[28:31], v[216:223], v124\n"
"ds_read_b128 v[52:55], v127 offset:5120\n"
"v_mov_b32 v17, 0\n"
"s_nop 2\n"
"v_max3_f32 v66, v66, v112, v113\n"
"v_max3_f32 v70, v66, v114, v115\n"
"v_max3_f32 v66, v66, v114, v115\n"
"v_mov_b32 v18, 0\n"
"v_mov_b32 v19, 0\n"
"v_permlane16_swap_b32 v66, v70\n"
"v_max_f32 v66, v66, v70\n"
"v_mul_f32 v66, s68, v66\n"
"v_fma_f32 v104, v104, s68, -v66\n"
"v_fma_f32 v105, v105, s68, -v66\n"
"v_exp_f32 v104, v104\n"
"v_fma_f32 v112, v112, s68, -v66\n"
"v_exp_f32 v105, v105\n"
"v_fma_f32 v106, v106, s68, -v66\n"
"v_exp_f32 v112, v112\n"
"v_fma_f32 v107, v107, s68, -v66\n"
"v_exp_f32 v106, v106\n"
"v_fma_f32 v113, v113, s68, -v66\n"
"v_exp_f32 v107, v107\n"
"v_exp_f32 v113, v113\n"
"v_cvt_pk_bf16_f32 v20, v104, v105\n"
"v_cvt_pk_bf16_f32 v21, v112, 0\n"
"v_cvt_pk_bf16_f32 v22, v106, v107\n"
"v_cvt_pk_bf16_f32 v23, v113, 0\n"
"v_cvt_pk_bf16_f32 v66, v96, v97\n"
"v_cvt_pk_bf16_f32 v70, v98, v99\n"
"v_mfma_f32_16x16x32_bf16 v[24:27], v[20:23], v[120:123], 0\n"
"v_cvt_pk_bf16_f32 v74, v100, v101\n"
"v_cvt_pk_bf16_f32 v78, v102, v103\n"
"v_smfmac_f32_16x16x64_bf16 v[116:119], v[20:23], v[64:71], v124 abid:1\n"
"ds_read_b128 v[56:59], v127 offset:6144\n"
"v_mov_b32 v28, 0\n"
"v_smfmac_f32_16x16x64_bf16 v[16:19], v[20:23], v[72:79], v124 abid:1\n"
"buffer_load_dwordx2 v[30:31], off, s[60:63], s71 sc1\n"
"ds_read_b128 v[60:63], v127 offset:7168\n"
"v_add_f32 v24, v24, v25\n"
"v_add_f32 v26, v26, v27\n"
"v_rcp_f32 v24, v24\n"
"v_rcp_f32 v26, v26\n"
"v_add_f32 v116, v116, v117\n"
"v_add_f32 v118, v118, v119\n"
"v_mul_f32 v116, v116, v24\n"
"v_mul_f32 v118, v118, v26\n"
"v_add_f32 v16, v16, v17\n"
"v_add_f32 v18, v18, v19\n"
"v_mul_f32 v16, v16, v24\n"
"v_mul_f32 v18, v18, v26\n"
"v_cvt_pk_bf16_f32 v116, v116, v16\n"
"v_cvt_pk_bf16_f32 v118, v118, v18\n"
"ds_write2_b32 v71, v116, v118 offset0:0 offset1:8\n"
"v_mfma_f32_4x4x1_16b_f32 v[96:99], v28, v28, 0\n"
"ds_read_b128 v[80:83], v127 offset:8192\n"
"v_mfma_f32_4x4x1_16b_f32 v[100:103], v28, v28, 0\n"
"ds_read_b128 v[84:87], v127 offset:9216\n"
"v_mfma_f32_4x4x1_16b_f32 v[104:107], v28, v28, 0\n"
"ds_read_b128 v[88:91], v127 offset:10240\n"
"v_mfma_f32_4x4x1_16b_f32 v[108:111], v28, v28, 0\n"
"s_waitcnt lgkmcnt(3)\n"
"s_barrier\n"
"ds_read_b128 v[16:19], v125 offset:8192\n"
"ds_read_b128 v[20:23], v125 offset:9216\n"
"ds_read_b128 v[92:95], v127 offset:11264\n"
"s_waitcnt lgkmcnt(2)\n"
"v_smfmac_f32_16x16x64_bf16 v[96:99], v[16:19], v[32:39], v124\n"
"ds_read_b128 v[0:3], v127 offset:12288\n"
"v_smfmac_f32_16x16x64_bf16 v[100:103], v[16:19], v[40:47], v124\n"
"ds_read_b128 v[4:7], v127 offset:13312\n"
"v_smfmac_f32_16x16x64_bf16 v[104:107], v[16:19], v[48:55], v124\n"
"ds_read_b128 v[8:11], v127 offset:14336\n"
"v_smfmac_f32_16x16x64_bf16 v[108:111], v[16:19], v[56:63], v124\n"
"s_waitcnt lgkmcnt(4)\n"
"ds_read_b128 v[12:15], v127 offset:15360\n"
"v_smfmac_f32_16x16x64_bf16 v[96:99], v[20:23], v[80:87], v124\n"
"s_waitcnt lgkmcnt(4)\n"
"s_add_u32 s14, s96, s79\n"
"v_smfmac_f32_16x16x64_bf16 v[100:103], v[20:23], v[88:95], v124\n"
"s_waitcnt lgkmcnt(2)\n"
"v_smfmac_f32_16x16x64_bf16 v[104:107], v[20:23], v[0:7], v124\n"
"s_waitcnt lgkmcnt(0)\n"
"v_smfmac_f32_16x16x64_bf16 v[108:111], v[20:23], v[8:15], v124\n"
"v_add_f32 v96, v96, v97\n"
"v_add_f32 v98, v98, v99\n"
"v_add_f32 v100, v100, v101\n"
"v_add_f32 v102, v102, v103\n"
"v_cvt_pk_bf16_f32 v116, v96, v100\n"
"v_cvt_pk_bf16_f32 v118, v98, v102\n"
"v_add_f32 v104, v104, v105\n"
"v_add_f32 v106, v106, v107\n"
"v_add_f32 v108, v108, v109\n"
"v_add_f32 v110, v110, v111\n"
"v_cvt_pk_bf16_f32 v117, v104, v108\n"
"v_cvt_pk_bf16_f32 v119, v106, v110\n"
"buffer_store_dwordx4 v[116:119], v126, s[60:63], s96 offen sc1\n"
"s_memtime s[72:73]\n"
"s_memrealtime s[16:17]\n"
"s_nop 2\n"
"s_memrealtime s[18:19]\n"
"s_nop 2\n"
"s_memrealtime s[20:21]\n"
"s_nop 2\n"
"s_memrealtime s[22:23]\n"
"s_nop 2\n"
"s_memrealtime s[24:25]\n"
"s_nop 2\n"
"s_memrealtime s[26:27]\n"
"s_nop 2\n"
"s_memrealtime s[28:29]\n"
"s_nop 2\n"
"s_waitcnt lgkmcnt(0)\n"
"s_mul_i32 s64, s16, 24\n"
"s_mov_b32 s17, 0\n"
"s_cmp_eq_u32 s18, s16\n"
"s_addc_u32 s17, s17, 0\n"
"s_cmp_eq_u32 s20, s16\n"
"s_addc_u32 s17, s17, 0\n"
"s_cmp_eq_u32 s22, s16\n"
"s_addc_u32 s17, s17, 0\n"
"s_cmp_eq_u32 s24, s16\n"
"s_addc_u32 s17, s17, 0\n"
"s_cmp_eq_u32 s26, s16\n"
"s_addc_u32 s17, s17, 0\n"
"s_cmp_eq_u32 s28, s16\n"
"s_addc_u32 s17, s17, 0\n"
"s_lshl_b32 s17, s17, 4\n"
"s_sub_u32 s64, s64, s17\n"
"s_sub_u32 s64, s64, s72\n"
"s_sub_u32 s64, s64, s74\n"
"s_ashr_i32 s65, s15, 2\n"
"s_sub_u32 s15, s15, s65\n"
"s_add_u32 s65, s65, 64\n"
"s_min_i32 s64, s64, s65\n"
"s_add_u32 s15, s15, s64\n"
"s_waitcnt vmcnt(1)\n"
"v_readfirstlane_b32 s64, v30\n"
"v_readfirstlane_b32 s65, v31\n"
"s_nop 0\n"
"s_sub_u32 s65, s65, s35\n"
"s_cmp_le_u32 s65, 64\n"
"s_cselect_b32 s65, s65, 0\n"
"s_cmp_eq_u32 s64, s34\n"
"s_cselect_b32 s65, s65, 0\n"
"s_add_u32 s76, s76, 1\n"
"s_cmp_gt_u32 s65, s76\n"
"s_cbranch_scc1 .Lkvok58_%=\n"
"s_add_u32 s74, s76, 1\n"
"s_mov_b32 s78, 16384\n"
".Lsw59_%=:\n"
"buffer_load_dwordx2 v[30:31], off, s[60:63], s71 sc1\n"
"s_waitcnt vmcnt(0)\n"
"v_readfirstlane_b32 s64, v30\n"
"v_readfirstlane_b32 s65, v31\n"
"s_nop 0\n"
"s_sub_u32 s65, s65, s35\n"
"s_cmp_le_u32 s65, 64\n"
"s_cselect_b32 s65, s65, 0\n"
"s_cmp_eq_u32 s64, s34\n"
"s_cselect_b32 s65, s65, 0\n"
"s_cmp_ge_u32 s65, s74\n"
"s_cbranch_scc1 .Lswok60_%=\n"
"s_sub_u32 s78, s78, 1\n"
"s_cbranch_scc1 .Lfail5_%=\n"
"s_sleep 2\n"
"s_branch .Lsw59_%=\n"
".Lswok60_%=:\n"
".Lkvok58_%=:\n"
"s_and_b32 s64, s76, 7\n"
"s_lshl_b32 s65, s76, 12\n"
"s_xor_b32 s65, s65, s77\n"
"s_and_b32 s65, s65, 8192\n"
"s_lshl_b32 s64, s64, 20\n"
"s_add_u32 s64, s64, s65\n"
"s_add_u32 s64, s64, s97\n"
"s_add_u32 s65, s64, 16384\n"
"s_barrier\n"
"buffer_load_dwordx4 v[104:107], v125, s[60:63], s64 offen offset:0 sc1\n"
"buffer_load_dwordx4 v[108:111], v125, s[60:63], s64 offen offset:1024 sc1\n"
"v_lshrrev_b32 v118, 1, v125\n"
"buffer_load_dwordx2 v[64:65], v118, s[60:63], s65 offen offset:0 sc1\n"
"buffer_load_dwordx2 v[68:69], v118, s[60:63], s65 offen offset:512 sc1\n"
"buffer_load_dwordx2 v[72:73], v118, s[60:63], s65 offen offset:1024 sc1\n"
"buffer_load_dwordx2 v[76:77], v118, s[60:63], s65 offen offset:1536 sc1\n"
"s_and_b32 s64, s76, 15\n"
"s_lshl_b32 s64, s64, 16\n"
"s_add_u32 s101, s64, s77\n"
"s_lshr_b32 s64, s64, 5\n"
"s_and_b32 s64, s64, 0x4000\n"
"s_xor_b32 s64, s64, s33\n"
"s_add_u32 s64, s101, s64\n"
"s_lshl_b32 s65, s39, 15\n"
"s_add_u32 s64, s64, s65\n"
"s_xor_b32 s94, s64, 34816\n"
"s_xor_b32 s95, s94, 1024\n"
"s_xor_b32 s100, s64, 0\n"
"v_xor_b32 v126, 0x4000, v126\n"
"s_xor_b32 s79, s79, 0x4000\n"
"v_mov_b32 v80, 0\n"
"v_mov_b32 v81, 0\n"
"v_mov_b32 v82, 0\n"
"v_mov_b32 v83, 0\n"
"v_mov_b32 v84, 0\n"
"v_mov_b32 v85, 0\n"
"v_mov_b32 v86, 0\n"
"v_mov_b32 v87, 0\n"
"v_mov_b32 v88, 0\n"
"v_mov_b32 v89, 0\n"
"v_mov_b32 v90, 0\n"
"v_mov_b32 v91, 0\n"
"v_mov_b32 v92, 0\n"
"v_mov_b32 v93, 0\n"
"v_mov_b32 v94, 0\n"
"v_mov_b32 v95, 0\n"
"v_mov_b32 v96, 0\n"
"v_mov_b32 v97, 0\n"
"v_mov_b32 v98, 0\n"
"v_mov_b32 v99, 0\n"
"v_mov_b32 v100, 0\n"
"v_mov_b32 v101, 0\n"
"v_mov_b32 v102, 0\n"
"v_mov_b32 v103, 0\n"
"v_mov_b32 v116, 0\n"
"v_mov_b32 v112, -1\n"
"v_mov_b32 v113, -1\n"
"v_mov_b32 v114, -1\n"
"v_mov_b32 v115, -1\n"
"s_xor_b32 s96, s96, 524288\n"
"s_mov_b32 s78, 16384\n"
"s_add_u32 s12, s12, 16384\n"
"s_add_u32 s13, s13, 16384\n"
"s_xor_b32 s71, s71, 128\n"
"s_cmp_lt_u32 s76, 63\n"
"s_waitcnt lgkmcnt(0)\n"
"s_cbranch_scc1 .Ltop53_%=\n"
"s_endpgm\n"
".Lh13_%=:\n"
".Lsu9_%=:\n"
"s_and_b32 s38, s65, 15\n"
"s_lshl_b32 s38, s38, 2\n"
"s_add_u32 s38, s38, s64\n"
"s_getpc_b64 s[64:65]\n"
".Lwc61_%=:\n"
"s_sub_u32 s64, s64, .Lwc61_%=-.Lh02_%=\n"
"s_subb_u32 s65, s65, 0\n"
"s_lshl_b32 s66, s37, 13\n"
"v_lshlrev_b32 v116, 7, v119\n"
"v_add_u32 v116, s66, v116\n"
"s_mov_b32 s66, .Lh13_%=-.Lh02_%=\n"
"v_min_u32 v116, s66, v116\n"
"global_load_dword a255, v116, s[64:65]\n"
"s_mov_b32 s52, s8\n"
"s_and_b32 s53, s9, 0xffff\n"
"s_mov_b32 s54, 0x200000\n"
"s_mov_b32 s55, 0x20000\n"
"v_lshlrev_b32 v107, 4, v119\n"
"s_mov_b32 s21, 0\n"
"v_mov_b32 v108, -1\n"
"v_mov_b32 v109, -1\n"
"v_mov_b32 v110, -1\n"
"v_mov_b32 v111, -1\n"
"s_lshl_b32 s66, s38, 2\n"
"s_add_u32 s66, s66, s37\n"
"s_lshl_b32 s67, s66, 11\n"
"buffer_store_dwordx4 v[108:111], v107, s[60:63], s67 offen offset:0 sc1\n"
"buffer_store_dwordx4 v[108:111], v107, s[60:63], s67 offen offset:1024 sc1\n"
"s_lshl_b32 s67, s38, 14\n"
"s_lshl_b32 s68, s37, 13\n"
"s_add_u32 s67, s67, s68\n"
"s_add_u32 s67, s67, 2097152\n"
"s_lshl_b32 s68, s38, 10\n"
"s_add_u32 s68, s68, 1114112\n"
"s_cmp_lt_u32 s37, 2\n"
"s_cselect_b32 s67, s67, s68\n"
"buffer_store_dwordx4 v[108:111], v107, s[60:63], s67 offen sc1\n"
"v_mov_b32 v1, 0\n"
"s_lshl_b32 s66, s38, 8\n"
"s_add_u32 s66, s66, 3162112\n"
"s_mov_b64 exec, 1\n"
"buffer_store_dword v1, off, s[60:63], s66 sc1\n"
"s_mov_b64 exec, -1\n"
"s_waitcnt vmcnt(0)\n"
"s_barrier\n"
"s_cmp_lg_u32 s37, 0\n"
"s_cbranch_scc1 .Lsdone65_%=\n"
"v_lshlrev_b32 v0, 8, v119\n"
"s_bfe_u32 s22, s38, 0x20002\n"
"s_bfe_u32 s23, s38, 0x10001\n"
"s_and_b32 s24, s38, 1\n"
"s_lshr_b32 s67, s38, 2\n"
"s_lshl_b32 s67, s67, 15\n"
"s_lshl_b32 s64, s23, 19\n"
"s_add_u32 s67, s67, s64\n"
"s_lshl_b32 s64, s24, 7\n"
"s_add_u32 s67, s67, s64\n"
"s_add_u32 s22, s22, 1\n"
"s_and_b32 s22, s22, 2\n"
"s_mov_b32 s65, 8192\n"
"s_mov_b64 exec, 0xffff\n"
"s_mov_b32 s68, -1\n"
"s_mov_b32 s69, -1\n"
"s_mov_b32 s64, 2\n"
".Lprobe66_%=:\n"
"s_memtime s[70:71]\n"
"buffer_store_dword v108, v0, s[60:63], s67 offen sc1\n"
"s_waitcnt vmcnt(0)\n"
"s_memtime s[72:73]\n"
"s_xor_b32 s67, s67, s65\n"
"s_waitcnt lgkmcnt(0)\n"
"s_sub_u32 s70, s72, s70\n"
"s_min_u32 s70, s68, s70\n"
"s_mov_b32 s68, s69\n"
"s_mov_b32 s69, s70\n"
"s_sub_u32 s64, s64, 1\n"
"s_cmp_lg_u32 s64, 0\n"
"s_cbranch_scc1 .Lprobe66_%=\n"
"s_cmp_lt_u32 s69, s68\n"
"s_cselect_b32 s66, 1, 0\n"
"s_mov_b32 s21, s66\n"
"s_lshl_b32 s66, s66, 13\n"
"s_add_u32 s67, s67, s66\n"
"s_cmp_eq_u32 s22, 0\n"
"s_cselect_b32 s65, 4, 1\n"
"s_lshl_b32 s65, s65, 12\n"
"s_mov_b32 s68, -1\n"
"s_mov_b32 s69, -1\n"
"s_mov_b32 s64, 6\n"
".Lprobe67_%=:\n"
"s_memtime s[70:71]\n"
"buffer_store_dword v108, v0, s[60:63], s67 offen sc1\n"
"s_waitcnt vmcnt(0)\n"
"s_memtime s[72:73]\n"
"s_xor_b32 s67, s67, s65\n"
"s_waitcnt lgkmcnt(0)\n"
"s_sub_u32 s70, s72, s70\n"
"s_min_u32 s70, s68, s70\n"
"s_mov_b32 s68, s69\n"
"s_mov_b32 s69, s70\n"
"s_sub_u32 s64, s64, 1\n"
"s_cmp_lg_u32 s64, 0\n"
"s_cbranch_scc1 .Lprobe67_%=\n"
"s_cmp_lt_u32 s69, s68\n"
"s_cselect_b32 s66, 1, 0\n"
"s_cmp_eq_u32 s22, 0\n"
"s_cselect_b32 s24, s24, 0\n"
"s_xor_b32 s66, s66, s24\n"
"s_xor_b32 s66, s66, s23\n"
"s_lshl_b32 s66, s66, 1\n"
"s_or_b32 s21, s21, s66\n"
"s_mov_b64 exec, -1\n"
"v_readfirstlane_b32 s75, v118\n"
"s_nop 0\n"
"s_add_u32 s75, s75, 1\n"
"s_mul_i32 s34, s75, 0x9e3779b1\n"
"s_xor_b32 s35, s34, 0x5bd1e995\n"
"s_xor_b32 s64, s21, s35\n"
"v_mov_b32 v33, s64\n"
"v_mov_b32 v32, s34\n"
"s_lshl_b32 s64, s38, 3\n"
"s_add_u32 s64, s64, 3146752\n"
"s_mov_b64 exec, 1\n"
"buffer_store_dwordx2 v[32:33], off, s[60:63], s64 sc1\n"
"s_mov_b64 exec, -1\n"
".Lsdone65_%=:\n"
"s_endpgm\n"
".Lfar6_%=:\n"
"s_bitcmp1_b32 s36, 0\n"
"s_cbranch_scc1 .Lhi7_%=\n"
"s_and_b32 s38, s65, 15\n"
"s_lshr_b32 s39, s65, 4\n"
"s_lshl_b32 s38, s38, 0\n"
"s_lshl_b32 s64, s64, 3\n"
"s_add_u32 s38, s38, s64\n"
"s_mov_b32 s40, s12\n"
"s_and_b32 s41, s13, 0xffff\n"
"s_mov_b32 s42, 0x400000\n"
"s_mov_b32 s43, 0x20000\n"
"s_mov_b32 s44, s14\n"
"s_and_b32 s45, s15, 0xffff\n"
"s_mov_b32 s46, 0x400000\n"
"s_mov_b32 s47, 0x20000\n"
"s_mov_b32 s48, s16\n"
"s_and_b32 s49, s17, 0xffff\n"
"s_mov_b32 s50, 0x400000\n"
"s_mov_b32 s51, 0x20000\n"
"s_mov_b32 s56, s10\n"
"s_and_b32 s57, s11, 0xffff\n"
"s_mov_b32 s58, 0x2000\n"
"s_mov_b32 s59, 0x20000\n"
"s_mov_b32 s52, s8\n"
"s_and_b32 s53, s9, 0xffff\n"
"s_mov_b32 s54, 0x200000\n"
"s_mov_b32 s55, 0x20000\n"
"v_lshrrev_b32 v104, 3, v119\n"
"v_and_b32 v105, 7, v119\n"
"v_lshlrev_b32 v106, 10, v104\n"
"v_lshl_add_u32 v106, v105, 4, v106\n"
"s_lshl_b32 s64, s39, 2\n"
"s_add_u32 s64, s64, s37\n"
"s_lshl_b32 s64, s64, 13\n"
"buffer_load_dwordx4 v[72:75], v106, s[52:55], s64 offen offset:0\n"
"buffer_load_dwordx4 v[76:79], v106, s[52:55], s64 offen offset:128\n"
"buffer_load_dwordx4 v[80:83], v106, s[52:55], s64 offen offset:256\n"
"buffer_load_dwordx4 v[84:87], v106, s[52:55], s64 offen offset:384\n"
"buffer_load_dwordx4 v[88:91], v106, s[52:55], s64 offen offset:512\n"
"buffer_load_dwordx4 v[92:95], v106, s[52:55], s64 offen offset:640\n"
"buffer_load_dwordx4 v[96:99], v106, s[52:55], s64 offen offset:768\n"
"buffer_load_dwordx4 v[100:103], v106, s[52:55], s64 offen offset:896\n"
".Lq069_%=:\n"
"s_and_b32 s101, s39, 0\n"
"s_lshl_b32 s100, s101, 13\n"
"s_lshl_b32 s70, s101, 14\n"
"s_lshl_b32 s101, s101, 21\n"
"s_lshl_b32 s67, s38, 8\n"
"v_lshrrev_b32 v108, 4, v119\n"
"v_and_b32 v109, 1, v108\n"
"s_lshl_b32 s73, s37, 1\n"
"v_or_b32 v109, s73, v109\n"
"v_lshlrev_b32 v109, 1, v109\n"
"v_and_b32 v110, 15, v119\n"
"v_xor_b32 v109, v109, v110\n"
"v_lshlrev_b32 v109, 4, v109\n"
"s_lshl_b32 s73, s37, 2\n"
"v_add_u32 v108, s73, v108\n"
"v_mul_u32_u24 v108, 8192, v108\n"
"v_add3_u32 v108, v108, v109, s67\n"
"s_lshl_b32 s73, s37, 12\n"
"s_add_u32 s73, s73, 65536\n"
"v_add_u32 v109, 130048, v108\n"
"v_add_u32 v110, 260096, v108\n"
"v_add_u32 v111, 390144, v108\n"
"s_add_u32 m0, s73, 0\n"
"s_xor_b32 s72, s101, 0\n"
"buffer_load_dwordx4 v108, s[44:47], s72 offen offset:0 lds\n"
"buffer_load_dwordx4 v109, s[44:47], s72 offen offset:1024 lds\n"
"buffer_load_dwordx4 v110, s[44:47], s72 offen offset:2048 lds\n"
"buffer_load_dwordx4 v111, s[44:47], s72 offen offset:3072 lds\n"
"s_add_u32 m0, s73, 16384\n"
"s_xor_b32 s72, s101, 524288\n"
"buffer_load_dwordx4 v108, s[44:47], s72 offen offset:0 lds\n"
"buffer_load_dwordx4 v109, s[44:47], s72 offen offset:1024 lds\n"
"buffer_load_dwordx4 v110, s[44:47], s72 offen offset:2048 lds\n"
"buffer_load_dwordx4 v111, s[44:47], s72 offen offset:3072 lds\n"
"v_bfe_u32 v112, v119, 4, 1\n"
"v_bfe_u32 v113, v119, 3, 1\n"
"v_lshl_add_u32 v112, v113, 1, v112\n"
"v_bfe_u32 v113, v119, 2, 1\n"
"v_lshl_add_u32 v114, v112, 1, v113\n"
"v_lshlrev_b32 v112, 12, v112\n"
"v_lshl_add_u32 v112, v113, 8, v112\n"
"v_bfe_u32 v113, v119, 5, 1\n"
"v_lshl_add_u32 v112, v113, 10, v112\n"
"v_and_b32 v113, 3, v119\n"
"v_lshl_add_u32 v112, v113, 3, v112\n"
"s_lshl_b32 s72, s37, 1\n"
"v_xor_b32 v114, s72, v114\n"
"v_lshl_add_u32 v112, v114, 5, v112\n"
"v_add_u32 v112, 65536, v112\n"
"v_xor_b32 v113, 32, v112\n"
"v_add_u32 v114, 0x10000, v112\n"
"v_add_u32 v115, 0x10000, v113\n"
"s_waitcnt vmcnt(8)\n"
"v_readfirstlane_b32 s75, v118\n"
"s_nop 0\n"
"s_add_u32 s75, s75, 1\n"
"s_mul_i32 s34, s75, 0x9e3779b1\n"
"s_xor_b32 s35, s34, 0x5bd1e995\n"
"v_lshlrev_b32 v0, 16, v72\n"
"v_and_b32 v1, 0xffff0000, v72\n"
"v_lshlrev_b32 v2, 16, v73\n"
"v_and_b32 v3, 0xffff0000, v73\n"
"v_lshlrev_b32 v4, 16, v74\n"
"v_and_b32 v5, 0xffff0000, v74\n"
"v_lshlrev_b32 v6, 16, v75\n"
"v_and_b32 v7, 0xffff0000, v75\n"
"v_lshlrev_b32 v8, 16, v76\n"
"v_and_b32 v9, 0xffff0000, v76\n"
"v_lshlrev_b32 v10, 16, v77\n"
"v_and_b32 v11, 0xffff0000, v77\n"
"v_lshlrev_b32 v12, 16, v78\n"
"v_and_b32 v13, 0xffff0000, v78\n"
"v_lshlrev_b32 v14, 16, v79\n"
"v_and_b32 v15, 0xffff0000, v79\n"
"s_add_u32 m0, s73, 32768\n"
"s_xor_b32 s72, s101, 1048576\n"
"buffer_load_dwordx4 v108, s[44:47], s72 offen offset:0 lds\n"
"v_lshlrev_b32 v16, 16, v80\n"
"v_and_b32 v17, 0xffff0000, v80\n"
"v_lshlrev_b32 v18, 16, v81\n"
"v_and_b32 v19, 0xffff0000, v81\n"
"v_lshlrev_b32 v20, 16, v82\n"
"v_and_b32 v21, 0xffff0000, v82\n"
"v_lshlrev_b32 v22, 16, v83\n"
"v_and_b32 v23, 0xffff0000, v83\n"
"v_lshlrev_b32 v24, 16, v84\n"
"v_and_b32 v25, 0xffff0000, v84\n"
"v_lshlrev_b32 v26, 16, v85\n"
"v_and_b32 v27, 0xffff0000, v85\n"
"v_lshlrev_b32 v28, 16, v86\n"
"v_and_b32 v29, 0xffff0000, v86\n"
"v_lshlrev_b32 v30, 16, v87\n"
"v_and_b32 v31, 0xffff0000, v87\n"
"buffer_load_dwordx4 v109, s[44:47], s72 offen offset:1024 lds\n"
"v_lshlrev_b32 v32, 16, v88\n"
"v_and_b32 v33, 0xffff0000, v88\n"
"v_lshlrev_b32 v34, 16, v89\n"
"v_and_b32 v35, 0xffff0000, v89\n"
"v_lshlrev_b32 v36, 16, v90\n"
"v_and_b32 v37, 0xffff0000, v90\n"
"v_lshlrev_b32 v38, 16, v91\n"
"v_and_b32 v39, 0xffff0000, v91\n"
"v_lshlrev_b32 v40, 16, v92\n"
"v_and_b32 v41, 0xffff0000, v92\n"
"v_lshlrev_b32 v42, 16, v93\n"
"v_and_b32 v43, 0xffff0000, v93\n"
"v_lshlrev_b32 v44, 16, v94\n"
"v_and_b32 v45, 0xffff0000, v94\n"
"v_lshlrev_b32 v46, 16, v95\n"
"v_and_b32 v47, 0xffff0000, v95\n"
"buffer_load_dwordx4 v110, s[44:47], s72 offen offset:2048 lds\n"
"v_lshlrev_b32 v48, 16, v96\n"
"v_and_b32 v49, 0xffff0000, v96\n"
"v_lshlrev_b32 v50, 16, v97\n"
"v_and_b32 v51, 0xffff0000, v97\n"
"v_lshlrev_b32 v52, 16, v98\n"
"v_and_b32 v53, 0xffff0000, v98\n"
"v_lshlrev_b32 v54, 16, v99\n"
"v_and_b32 v55, 0xffff0000, v99\n"
"v_lshlrev_b32 v56, 16, v100\n"
"v_and_b32 v57, 0xffff0000, v100\n"
"v_lshlrev_b32 v58, 16, v101\n"
"v_and_b32 v59, 0xffff0000, v101\n"
"v_lshlrev_b32 v60, 16, v102\n"
"v_and_b32 v61, 0xffff0000, v102\n"
"v_lshlrev_b32 v62, 16, v103\n"
"v_and_b32 v63, 0xffff0000, v103\n"
"buffer_load_dwordx4 v111, s[44:47], s72 offen offset:3072 lds\n"
"v_pk_add_f32 v[64:65], v[0:1], v[2:3]\n"
"v_pk_add_f32 v[66:67], v[4:5], v[6:7]\n"
"v_pk_add_f32 v[64:65], v[64:65], v[8:9]\n"
"v_pk_add_f32 v[66:67], v[66:67], v[10:11]\n"
"v_pk_add_f32 v[64:65], v[64:65], v[12:13]\n"
"v_pk_add_f32 v[66:67], v[66:67], v[14:15]\n"
"v_pk_add_f32 v[64:65], v[64:65], v[16:17]\n"
"v_pk_add_f32 v[66:67], v[66:67], v[18:19]\n"
"s_add_u32 m0, s73, 49152\n"
"s_xor_b32 s72, s101, 1572864\n"
"buffer_load_dwordx4 v108, s[44:47], s72 offen offset:0 lds\n"
"v_pk_add_f32 v[64:65], v[64:65], v[20:21]\n"
"v_pk_add_f32 v[66:67], v[66:67], v[22:23]\n"
"v_pk_add_f32 v[64:65], v[64:65], v[24:25]\n"
"v_pk_add_f32 v[66:67], v[66:67], v[26:27]\n"
"v_pk_add_f32 v[64:65], v[64:65], v[28:29]\n"
"v_pk_add_f32 v[66:67], v[66:67], v[30:31]\n"
"v_pk_add_f32 v[64:65], v[64:65], v[32:33]\n"
"v_pk_add_f32 v[66:67], v[66:67], v[34:35]\n"
"v_pk_add_f32 v[64:65], v[64:65], v[36:37]\n"
"v_pk_add_f32 v[66:67], v[66:67], v[38:39]\n"
"buffer_load_dwordx4 v109, s[44:47], s72 offen offset:1024 lds\n"
"v_pk_add_f32 v[64:65], v[64:65], v[40:41]\n"
"v_pk_add_f32 v[66:67], v[66:67], v[42:43]\n"
"v_pk_add_f32 v[64:65], v[64:65], v[44:45]\n"
"v_pk_add_f32 v[66:67], v[66:67], v[46:47]\n"
"v_pk_add_f32 v[64:65], v[64:65], v[48:49]\n"
"v_pk_add_f32 v[66:67], v[66:67], v[50:51]\n"
"v_pk_add_f32 v[64:65], v[64:65], v[52:53]\n"
"v_pk_add_f32 v[66:67], v[66:67], v[54:55]\n"
"v_pk_add_f32 v[64:65], v[64:65], v[56:57]\n"
"v_pk_add_f32 v[66:67], v[66:67], v[58:59]\n"
"buffer_load_dwordx4 v110, s[44:47], s72 offen offset:2048 lds\n"
"v_pk_add_f32 v[64:65], v[64:65], v[60:61]\n"
"v_pk_add_f32 v[66:67], v[66:67], v[62:63]\n"
"v_pk_add_f32 v[64:65], v[64:65], v[66:67]\n"
"v_add_f32 v64, v64, v65\n"
"s_nop 1\n"
"v_add_f32_dpp v64, v64, v64 quad_perm:[1,0,3,2] row_mask:0xf bank_mask:0xf\n"
"s_nop 1\n"
"v_add_f32_dpp v64, v64, v64 quad_perm:[2,3,0,1] row_mask:0xf bank_mask:0xf\n"
"s_nop 1\n"
"v_add_f32_dpp v64, v64, v64 row_half_mirror row_mask:0xf bank_mask:0xf\n"
"v_mul_f32 v64, 0x3b000000, v64\n"
"v_sub_f32 v64, 0, v64\n"
"v_mov_b32 v65, v64\n"
"v_pk_add_f32 v[0:1], v[0:1], v[64:65]\n"
"v_pk_add_f32 v[2:3], v[2:3], v[64:65]\n"
"v_pk_add_f32 v[4:5], v[4:5], v[64:65]\n"
"v_pk_add_f32 v[6:7], v[6:7], v[64:65]\n"
"v_pk_add_f32 v[8:9], v[8:9], v[64:65]\n"
"v_pk_add_f32 v[10:11], v[10:11], v[64:65]\n"
"v_pk_add_f32 v[12:13], v[12:13], v[64:65]\n"
"v_pk_add_f32 v[14:15], v[14:15], v[64:65]\n"
"v_pk_add_f32 v[16:17], v[16:17], v[64:65]\n"
"v_pk_add_f32 v[18:19], v[18:19], v[64:65]\n"
"buffer_load_dwordx4 v111, s[44:47], s72 offen offset:3072 lds\n"
"v_pk_add_f32 v[20:21], v[20:21], v[64:65]\n"
"v_pk_add_f32 v[22:23], v[22:23], v[64:65]\n"
"v_pk_add_f32 v[24:25], v[24:25], v[64:65]\n"
"v_pk_add_f32 v[26:27], v[26:27], v[64:65]\n"
"v_pk_add_f32 v[28:29], v[28:29], v[64:65]\n"
"v_pk_add_f32 v[30:31], v[30:31], v[64:65]\n"
"v_pk_add_f32 v[32:33], v[32:33], v[64:65]\n"
"v_pk_add_f32 v[34:35], v[34:35], v[64:65]\n"
"v_pk_add_f32 v[36:37], v[36:37], v[64:65]\n"
"v_pk_add_f32 v[38:39], v[38:39], v[64:65]\n"
"s_add_u32 m0, s73, 65536\n"
"s_xor_b32 s72, s101, 2097152\n"
"buffer_load_dwordx4 v108, s[44:47], s72 offen offset:0 lds\n"
"v_pk_add_f32 v[40:41], v[40:41], v[64:65]\n"
"v_pk_add_f32 v[42:43], v[42:43], v[64:65]\n"
"v_pk_add_f32 v[44:45], v[44:45], v[64:65]\n"
"v_pk_add_f32 v[46:47], v[46:47], v[64:65]\n"
"v_pk_add_f32 v[48:49], v[48:49], v[64:65]\n"
"v_pk_add_f32 v[50:51], v[50:51], v[64:65]\n"
"v_pk_add_f32 v[52:53], v[52:53], v[64:65]\n"
"v_pk_add_f32 v[54:55], v[54:55], v[64:65]\n"
"v_pk_add_f32 v[56:57], v[56:57], v[64:65]\n"
"v_pk_add_f32 v[58:59], v[58:59], v[64:65]\n"
"buffer_load_dwordx4 v109, s[44:47], s72 offen offset:1024 lds\n"
"v_pk_add_f32 v[60:61], v[60:61], v[64:65]\n"
"v_pk_add_f32 v[62:63], v[62:63], v[64:65]\n"
"v_pk_mul_f32 v[64:65], v[0:1], v[0:1]\n"
"v_pk_mul_f32 v[66:67], v[2:3], v[2:3]\n"
"v_pk_fma_f32 v[64:65], v[4:5], v[4:5], v[64:65]\n"
"v_pk_fma_f32 v[66:67], v[6:7], v[6:7], v[66:67]\n"
"v_pk_fma_f32 v[64:65], v[8:9], v[8:9], v[64:65]\n"
"v_pk_fma_f32 v[66:67], v[10:11], v[10:11], v[66:67]\n"
"v_pk_fma_f32 v[64:65], v[12:13], v[12:13], v[64:65]\n"
"v_pk_fma_f32 v[66:67], v[14:15], v[14:15], v[66:67]\n"
"v_pk_fma_f32 v[64:65], v[16:17], v[16:17], v[64:65]\n"
"v_pk_fma_f32 v[66:67], v[18:19], v[18:19], v[66:67]\n"
"buffer_load_dwordx4 v110, s[44:47], s72 offen offset:2048 lds\n"
"v_pk_fma_f32 v[64:65], v[20:21], v[20:21], v[64:65]\n"
"v_pk_fma_f32 v[66:67], v[22:23], v[22:23], v[66:67]\n"
"v_pk_fma_f32 v[64:65], v[24:25], v[24:25], v[64:65]\n"
"v_pk_fma_f32 v[66:67], v[26:27], v[26:27], v[66:67]\n"
"v_pk_fma_f32 v[64:65], v[28:29], v[28:29], v[64:65]\n"
"v_pk_fma_f32 v[66:67], v[30:31], v[30:31], v[66:67]\n"
"v_pk_fma_f32 v[64:65], v[32:33], v[32:33], v[64:65]\n"
"v_pk_fma_f32 v[66:67], v[34:35], v[34:35], v[66:67]\n"
"v_pk_fma_f32 v[64:65], v[36:37], v[36:37], v[64:65]\n"
"v_pk_fma_f32 v[66:67], v[38:39], v[38:39], v[66:67]\n"
"buffer_load_dwordx4 v111, s[44:47], s72 offen offset:3072 lds\n"
"v_pk_fma_f32 v[64:65], v[40:41], v[40:41], v[64:65]\n"
"v_pk_fma_f32 v[66:67], v[42:43], v[42:43], v[66:67]\n"
"v_pk_fma_f32 v[64:65], v[44:45], v[44:45], v[64:65]\n"
"v_pk_fma_f32 v[66:67], v[46:47], v[46:47], v[66:67]\n"
"v_pk_fma_f32 v[64:65], v[48:49], v[48:49], v[64:65]\n"
"v_pk_fma_f32 v[66:67], v[50:51], v[50:51], v[66:67]\n"
"v_pk_fma_f32 v[64:65], v[52:53], v[52:53], v[64:65]\n"
"v_pk_fma_f32 v[66:67], v[54:55], v[54:55], v[66:67]\n"
"v_pk_fma_f32 v[64:65], v[56:57], v[56:57], v[64:65]\n"
"v_pk_fma_f32 v[66:67], v[58:59], v[58:59], v[66:67]\n"
"s_add_u32 m0, s73, 81920\n"
"s_xor_b32 s72, s101, 2621440\n"
"buffer_load_dwordx4 v108, s[44:47], s72 offen offset:0 lds\n"
"v_pk_fma_f32 v[64:65], v[60:61], v[60:61], v[64:65]\n"
"v_pk_fma_f32 v[66:67], v[62:63], v[62:63], v[66:67]\n"
"v_pk_add_f32 v[64:65], v[64:65], v[66:67]\n"
"v_add_f32 v64, v64, v65\n"
"s_nop 1\n"
"v_add_f32_dpp v64, v64, v64 quad_perm:[1,0,3,2] row_mask:0xf bank_mask:0xf\n"
"s_nop 1\n"
"v_add_f32_dpp v64, v64, v64 quad_perm:[2,3,0,1] row_mask:0xf bank_mask:0xf\n"
"s_nop 1\n"
"v_add_f32_dpp v64, v64, v64 row_half_mirror row_mask:0xf bank_mask:0xf\n"
"v_mul_f32 v64, 0x3b000000, v64\n"
"v_add_f32 v64, 0x3727c5ac, v64\n"
"v_sqrt_f32 v64, v64\n"
"v_rcp_f32 v64, v64\n"
"s_nop 0\n"
"v_mov_b32 v65, v64\n"
"s_lshr_b32 s64, s37, 1\n"
"s_lshl_b32 s64, s64, 14\n"
"s_and_b32 s65, s37, 1\n"
"s_lshl_b32 s65, s65, 7\n"
"s_add_u32 s64, s64, s65\n"
"s_add_u32 s64, s64, s100\n"
"v_lshrrev_b32 v106, 1, v105\n"
"v_lshlrev_b32 v106, 9, v106\n"
"v_lshl_add_u32 v106, v104, 4, v106\n"
"v_and_b32 v66, 1, v105\n"
"v_lshl_add_u32 v106, v66, 2, v106\n"
"v_add_u32 v106, s64, v106\n"
"v_pk_mul_f32 v[0:1], v[0:1], v[64:65]\n"
"v_pk_mul_f32 v[2:3], v[2:3], v[64:65]\n"
"v_pk_mul_f32 v[4:5], v[4:5], v[64:65]\n"
"v_pk_mul_f32 v[6:7], v[6:7], v[64:65]\n"
"v_cvt_pk_bf16_f32 v72, v0, v1\n"
"v_cvt_pk_bf16_f32 v73, v2, v3\n"
"v_cvt_pk_bf16_f32 v74, v4, v5\n"
"v_cvt_pk_bf16_f32 v75, v6, v7\n"
"ds_write2_b32 v106, v72, v73 offset0:0 offset1:2\n"
"ds_write2_b32 v106, v74, v75 offset0:64 offset1:66\n"
"v_add_u32 v106, 2048, v106\n"
"buffer_load_dwordx4 v109, s[44:47], s72 offen offset:1024 lds\n"
"v_pk_mul_f32 v[8:9], v[8:9], v[64:65]\n"
"v_pk_mul_f32 v[10:11], v[10:11], v[64:65]\n"
"v_pk_mul_f32 v[12:13], v[12:13], v[64:65]\n"
"v_pk_mul_f32 v[14:15], v[14:15], v[64:65]\n"
"v_cvt_pk_bf16_f32 v76, v8, v9\n"
"v_cvt_pk_bf16_f32 v77, v10, v11\n"
"v_cvt_pk_bf16_f32 v78, v12, v13\n"
"v_cvt_pk_bf16_f32 v79, v14, v15\n"
"ds_write2_b32 v106, v76, v77 offset0:0 offset1:2\n"
"ds_write2_b32 v106, v78, v79 offset0:64 offset1:66\n"
"v_add_u32 v106, 2048, v106\n"
"buffer_load_dwordx4 v110, s[44:47], s72 offen offset:2048 lds\n"
"v_pk_mul_f32 v[16:17], v[16:17], v[64:65]\n"
"v_pk_mul_f32 v[18:19], v[18:19], v[64:65]\n"
"v_pk_mul_f32 v[20:21], v[20:21], v[64:65]\n"
"v_pk_mul_f32 v[22:23], v[22:23], v[64:65]\n"
"v_cvt_pk_bf16_f32 v80, v16, v17\n"
"v_cvt_pk_bf16_f32 v81, v18, v19\n"
"v_cvt_pk_bf16_f32 v82, v20, v21\n"
"v_cvt_pk_bf16_f32 v83, v22, v23\n"
"ds_write2_b32 v106, v80, v81 offset0:0 offset1:2\n"
"ds_write2_b32 v106, v82, v83 offset0:64 offset1:66\n"
"v_add_u32 v106, 2048, v106\n"
"buffer_load_dwordx4 v111, s[44:47], s72 offen offset:3072 lds\n"
"v_pk_mul_f32 v[24:25], v[24:25], v[64:65]\n"
"v_pk_mul_f32 v[26:27], v[26:27], v[64:65]\n"
"v_pk_mul_f32 v[28:29], v[28:29], v[64:65]\n"
"v_pk_mul_f32 v[30:31], v[30:31], v[64:65]\n"
"v_cvt_pk_bf16_f32 v84, v24, v25\n"
"v_cvt_pk_bf16_f32 v85, v26, v27\n"
"v_cvt_pk_bf16_f32 v86, v28, v29\n"
"v_cvt_pk_bf16_f32 v87, v30, v31\n"
"ds_write2_b32 v106, v84, v85 offset0:0 offset1:2\n"
"ds_write2_b32 v106, v86, v87 offset0:64 offset1:66\n"
"v_add_u32 v106, 2048, v106\n"
"v_subrev_u32 v106, s70, v106\n"
"v_pk_mul_f32 v[32:33], v[32:33], v[64:65]\n"
"v_pk_mul_f32 v[34:35], v[34:35], v[64:65]\n"
"v_pk_mul_f32 v[36:37], v[36:37], v[64:65]\n"
"v_pk_mul_f32 v[38:39], v[38:39], v[64:65]\n"
"v_cvt_pk_bf16_f32 v88, v32, v33\n"
"v_cvt_pk_bf16_f32 v89, v34, v35\n"
"v_cvt_pk_bf16_f32 v90, v36, v37\n"
"v_cvt_pk_bf16_f32 v91, v38, v39\n"
"ds_write2_b32 v106, v88, v89 offset0:0 offset1:2\n"
"ds_write2_b32 v106, v90, v91 offset0:64 offset1:66\n"
"v_add_u32 v106, 2048, v106\n"
"v_pk_mul_f32 v[40:41], v[40:41], v[64:65]\n"
"v_pk_mul_f32 v[42:43], v[42:43], v[64:65]\n"
"v_pk_mul_f32 v[44:45], v[44:45], v[64:65]\n"
"v_pk_mul_f32 v[46:47], v[46:47], v[64:65]\n"
"v_cvt_pk_bf16_f32 v92, v40, v41\n"
"v_cvt_pk_bf16_f32 v93, v42, v43\n"
"v_cvt_pk_bf16_f32 v94, v44, v45\n"
"v_cvt_pk_bf16_f32 v95, v46, v47\n"
"ds_write2_b32 v106, v92, v93 offset0:0 offset1:2\n"
"ds_write2_b32 v106, v94, v95 offset0:64 offset1:66\n"
"v_add_u32 v106, 2048, v106\n"
"v_pk_mul_f32 v[48:49], v[48:49], v[64:65]\n"
"v_pk_mul_f32 v[50:51], v[50:51], v[64:65]\n"
"v_pk_mul_f32 v[52:53], v[52:53], v[64:65]\n"
"v_pk_mul_f32 v[54:55], v[54:55], v[64:65]\n"
"v_cvt_pk_bf16_f32 v96, v48, v49\n"
"v_cvt_pk_bf16_f32 v97, v50, v51\n"
"v_cvt_pk_bf16_f32 v98, v52, v53\n"
"v_cvt_pk_bf16_f32 v99, v54, v55\n"
"ds_write2_b32 v106, v96, v97 offset0:0 offset1:2\n"
"ds_write2_b32 v106, v98, v99 offset0:64 offset1:66\n"
"v_add_u32 v106, 2048, v106\n"
"v_pk_mul_f32 v[56:57], v[56:57], v[64:65]\n"
"v_pk_mul_f32 v[58:59], v[58:59], v[64:65]\n"
"v_pk_mul_f32 v[60:61], v[60:61], v[64:65]\n"
"v_pk_mul_f32 v[62:63], v[62:63], v[64:65]\n"
"v_cvt_pk_bf16_f32 v100, v56, v57\n"
"v_cvt_pk_bf16_f32 v101, v58, v59\n"
"v_cvt_pk_bf16_f32 v102, v60, v61\n"
"v_cvt_pk_bf16_f32 v103, v62, v63\n"
"ds_write2_b32 v106, v100, v101 offset0:0 offset1:2\n"
"ds_write2_b32 v106, v102, v103 offset0:64 offset1:66\n"
"v_lshlrev_b32 v64, 4, v119\n"
"s_mov_b32 s9, 0\n"
"s_mov_b32 s66, 0xaaaaaaaa\n"
"s_mov_b32 s67, 0xaaaaaaaa\n"
"v_mov_b32 v0, 0x4444\n"
"v_mov_b32 v1, 0xeeee\n"
"v_cndmask_b32 v65, v0, v1, s[66:67]\n"
"s_mov_b32 s20, 0x3e0293ee\n"
"v_lshrrev_b32 v0, 4, v119\n"
"v_and_b32 v1, 15, v119\n"
"v_mov_b32 v66, v64\n"
"s_lshl_b32 s64, s37, 11\n"
"s_add_u32 s65, s64, 65536\n"
"v_add_u32 v67, s65, v64\n"
"v_add_u32 v68, 65536, v64\n"
"s_lshl_b32 s10, s38, 15\n"
"s_add_u32 s10, s10, s64\n"
"s_add_u32 s10, s10, 75497472\n"
"s_lshl_b32 s24, s38, 8\n"
"s_lshl_b32 s64, s39, 2\n"
"s_add_u32 s64, s64, s37\n"
"s_lshl_b32 s64, s64, 5\n"
"s_add_u32 s21, s24, s64\n"
"s_add_u32 s21, s21, 3149824\n"
"s_add_u32 s24, s24, 3162112\n"
"s_add_u32 s26, s21, 16\n"
"s_mov_b32 s22, s39\n"
"s_mov_b32 s28, 0\n"
"s_mov_b32 s29, 0\n"
"s_sub_u32 s8, s39, 2\n"
"s_lshl_b32 s64, s38, 2\n"
"s_add_u32 s64, s64, s37\n"
"s_lshl_b32 s76, s38, 4\n"
"s_add_u32 s76, s76, s37\n"
"s_lshl_b32 s76, s76, 3\n"
"s_andn2_b32 s64, s64, 1\n"
"s_lshr_b32 s74, s64, 3\n"
"s_lshl_b32 s74, s74, 13\n"
"s_lshl_b32 s79, s64, 10\n"
"s_add_u32 s79, s79, s74\n"
"s_add_u32 s79, s79, 8388608\n"
"s_and_b32 s64, s37, 1\n"
"s_lshl_b32 s64, s64, 19\n"
"s_add_u32 s79, s79, s64\n"
"v_bfe_u32 v2, v1, 1, 1\n"
"v_lshlrev_b32 v2, 3, v2\n"
"v_lshrrev_b32 v3, 2, v1\n"
"v_lshl_add_u32 v2, v3, 1, v2\n"
"v_and_b32 v3, 1, v1\n"
"v_add_u32 v2, v2, v3\n"
"v_lshl_add_u32 v2, v0, 4, v2\n"
"v_lshlrev_b32 v69, 4, v2\n"
"s_mul_i32 s64, s37, 576\n"
"s_add_u32 s64, s64, 139264\n"
"v_lshrrev_b32 v2, 2, v1\n"
"v_mul_u32_u24 v4, 144, v2\n"
"v_lshl_add_u32 v4, v0, 5, v4\n"
"v_and_b32 v5, 3, v1\n"
"v_lshl_add_u32 v4, v5, 2, v4\n"
"v_add_u32 v70, s64, v4\n"
"v_lshl_add_u32 v2, v2, 1, v3\n"
"v_mul_u32_u24 v4, 144, v0\n"
"v_lshl_add_u32 v4, v2, 4, v4\n"
"v_add_u32 v71, 139264, v4\n"
"v_lshlrev_b32 v0, 2, v0\n"
"s_and_b32 s64, s37, 1\n"
"s_lshl_b32 s64, s64, 4\n"
"v_add_u32 v1, s64, v1\n"
"v_add_u32 v2, 0, v0\n"
"v_cmp_le_u32 s[12:13], v2, v1\n"
"v_add_u32 v2, 16, v0\n"
"v_cmp_le_u32 s[30:31], v2, v1\n"
"v_mov_b32 v124, 0x3f803f80\n"
"v_add_u32 v2, 1, v0\n"
"v_cmp_le_u32 s[14:15], v2, v1\n"
"v_add_u32 v2, 17, v0\n"
"v_cmp_le_u32 s[32:33], v2, v1\n"
"v_mov_b32 v125, 0x3f803f80\n"
"v_add_u32 v2, 2, v0\n"
"v_cmp_le_u32 s[16:17], v2, v1\n"
"v_add_u32 v2, 18, v0\n"
"v_cmp_le_u32 s[52:53], v2, v1\n"
"v_mov_b32 v126, 0x3f803f80\n"
"v_add_u32 v2, 3, v0\n"
"v_cmp_le_u32 s[18:19], v2, v1\n"
"v_add_u32 v2, 19, v0\n"
"v_cmp_le_u32 s[54:55], v2, v1\n"
"v_mov_b32 v127, 0x3f803f80\n"
"s_waitcnt vmcnt(20) lgkmcnt(0)\n"
"s_barrier\n"
"ds_read_b64_tr_b16 a[128:129], v112 offset:0\n"
"ds_read_b64_tr_b16 a[130:131], v112 offset:512\n"
"ds_read_b64_tr_b16 a[132:133], v112 offset:2048\n"
"ds_read_b64_tr_b16 a[134:135], v112 offset:2560\n"
"ds_read_b64_tr_b16 a[136:137], v113 offset:0\n"
"ds_read_b64_tr_b16 a[138:139], v113 offset:512\n"
"ds_read_b64_tr_b16 a[140:141], v113 offset:2048\n"
"ds_read_b64_tr_b16 a[142:143], v113 offset:2560\n"
"ds_read_b128 v[48:51], v66 offset:0\n"
"ds_read_b128 v[52:55], v66 offset:16384\n"
"ds_read_b128 v[56:59], v66 offset:1024\n"
"ds_read_b128 v[60:63], v66 offset:17408\n"
"s_waitcnt vmcnt(16) lgkmcnt(0)\n"
"s_barrier\n"
"v_mfma_f32_16x16x32_bf16 v[16:19], a[128:131], v[48:51], 0\n"
"ds_read_b64_tr_b16 a[144:145], v112 offset:16384\n"
"ds_read_b64_tr_b16 a[146:147], v112 offset:16896\n"
"v_mfma_f32_16x16x32_bf16 v[24:27], a[128:131], v[52:55], 0\n"
"ds_read_b64_tr_b16 a[148:149], v112 offset:18432\n"
"v_mfma_f32_16x16x32_bf16 v[20:23], a[136:139], v[48:51], 0\n"
"ds_read_b64_tr_b16 a[150:151], v112 offset:18944\n"
"ds_read_b64_tr_b16 a[152:153], v113 offset:16384\n"
"v_mfma_f32_16x16x32_bf16 v[28:31], a[136:139], v[52:55], 0\n"
"ds_read_b64_tr_b16 a[154:155], v113 offset:16896\n"
"v_mfma_f32_16x16x32_bf16 v[16:19], a[132:135], v[56:59], v[16:19]\n"
"ds_read_b64_tr_b16 a[156:157], v113 offset:18432\n"
"ds_read_b64_tr_b16 a[158:159], v113 offset:18944\n"
"v_mfma_f32_16x16x32_bf16 v[24:27], a[132:135], v[60:63], v[24:27]\n"
"ds_read_b128 v[72:75], v66 offset:2048\n"
"v_mfma_f32_16x16x32_bf16 v[20:23], a[140:143], v[56:59], v[20:23]\n"
"ds_read_b128 v[76:79], v66 offset:18432\n"
"ds_read_b128 v[80:83], v66 offset:3072\n"
"v_mfma_f32_16x16x32_bf16 v[28:31], a[140:143], v[60:63], v[28:31]\n"
"ds_read_b128 v[84:87], v66 offset:19456\n"
"s_waitcnt vmcnt(12) lgkmcnt(0)\n"
"s_barrier\n"
"v_mfma_f32_16x16x32_bf16 v[16:19], a[144:147], v[72:75], v[16:19]\n"
"ds_read_b64_tr_b16 a[160:161], v112 offset:32768\n"
"ds_read_b64_tr_b16 a[162:163], v112 offset:33280\n"
"v_mfma_f32_16x16x32_bf16 v[24:27], a[144:147], v[76:79], v[24:27]\n"
"ds_read_b64_tr_b16 a[164:165], v112 offset:34816\n"
"v_mfma_f32_16x16x32_bf16 v[20:23], a[152:155], v[72:75], v[20:23]\n"
"ds_read_b64_tr_b16 a[166:167], v112 offset:35328\n"
"ds_read_b64_tr_b16 a[168:169], v113 offset:32768\n"
"v_mfma_f32_16x16x32_bf16 v[28:31], a[152:155], v[76:79], v[28:31]\n"
"ds_read_b64_tr_b16 a[170:171], v113 offset:33280\n"
"v_mfma_f32_16x16x32_bf16 v[16:19], a[148:151], v[80:83], v[16:19]\n"
"ds_read_b64_tr_b16 a[172:173], v113 offset:34816\n"
"ds_read_b64_tr_b16 a[174:175], v113 offset:35328\n"
"v_mfma_f32_16x16x32_bf16 v[24:27], a[148:151], v[84:87], v[24:27]\n"
"ds_read_b128 v[48:51], v66 offset:4096\n"
"v_mfma_f32_16x16x32_bf16 v[20:23], a[156:159], v[80:83], v[20:23]\n"
"ds_read_b128 v[52:55], v66 offset:20480\n"
"ds_read_b128 v[56:59], v66 offset:5120\n"
"v_mfma_f32_16x16x32_bf16 v[28:31], a[156:159], v[84:87], v[28:31]\n"
"ds_read_b128 v[60:63], v66 offset:21504\n"
"s_waitcnt vmcnt(8) lgkmcnt(0)\n"
"s_barrier\n"
"v_mfma_f32_16x16x32_bf16 v[16:19], a[160:163], v[48:51], v[16:19]\n"
"ds_read_b64_tr_b16 a[176:177], v112 offset:49152\n"
"ds_read_b64_tr_b16 a[178:179], v112 offset:49664\n"
"v_mfma_f32_16x16x32_bf16 v[24:27], a[160:163], v[52:55], v[24:27]\n"
"ds_read_b64_tr_b16 a[180:181], v112 offset:51200\n"
"s_add_u32 m0, s73, 0\n"
"s_xor_b32 s72, s101, 3145728\n"
"buffer_load_dwordx4 v108, s[44:47], s72 offen offset:0 lds\n"
"v_mfma_f32_16x16x32_bf16 v[20:23], a[168:171], v[48:51], v[20:23]\n"
"ds_read_b64_tr_b16 a[182:183], v112 offset:51712\n"
"ds_read_b64_tr_b16 a[184:185], v113 offset:49152\n"
"v_mfma_f32_16x16x32_bf16 v[28:31], a[168:171], v[52:55], v[28:31]\n"
"ds_read_b64_tr_b16 a[186:187], v113 offset:49664\n"
"buffer_load_dwordx4 v109, s[44:47], s72 offen offset:1024 lds\n"
"v_mfma_f32_16x16x32_bf16 v[16:19], a[164:167], v[56:59], v[16:19]\n"
"ds_read_b64_tr_b16 a[188:189], v113 offset:51200\n"
"ds_read_b64_tr_b16 a[190:191], v113 offset:51712\n"
"v_mfma_f32_16x16x32_bf16 v[24:27], a[164:167], v[60:63], v[24:27]\n"
"ds_read_b128 v[72:75], v66 offset:6144\n"
"buffer_load_dwordx4 v110, s[44:47], s72 offen offset:2048 lds\n"
"v_mfma_f32_16x16x32_bf16 v[20:23], a[172:175], v[56:59], v[20:23]\n"
"ds_read_b128 v[76:79], v66 offset:22528\n"
"ds_read_b128 v[80:83], v66 offset:7168\n"
"v_mfma_f32_16x16x32_bf16 v[28:31], a[172:175], v[60:63], v[28:31]\n"
"ds_read_b128 v[84:87], v66 offset:23552\n"
"buffer_load_dwordx4 v111, s[44:47], s72 offen offset:3072 lds\n"
"s_waitcnt vmcnt(8) lgkmcnt(0)\n"
"s_barrier\n"
"v_mfma_f32_16x16x32_bf16 v[16:19], a[176:179], v[72:75], v[16:19]\n"
"ds_read_b64_tr_b16 a[192:193], v114 offset:0\n"
"ds_read_b64_tr_b16 a[194:195], v114 offset:512\n"
"v_mfma_f32_16x16x32_bf16 v[24:27], a[176:179], v[76:79], v[24:27]\n"
"ds_read_b64_tr_b16 a[196:197], v114 offset:2048\n"
"s_add_u32 m0, s73, 16384\n"
"s_xor_b32 s72, s101, 3670016\n"
"buffer_load_dwordx4 v108, s[44:47], s72 offen offset:0 lds\n"
"v_mfma_f32_16x16x32_bf16 v[20:23], a[184:187], v[72:75], v[20:23]\n"
"ds_read_b64_tr_b16 a[198:199], v114 offset:2560\n"
"ds_read_b64_tr_b16 a[200:201], v115 offset:0\n"
"v_mfma_f32_16x16x32_bf16 v[28:31], a[184:187], v[76:79], v[28:31]\n"
"ds_read_b64_tr_b16 a[202:203], v115 offset:512\n"
"buffer_load_dwordx4 v109, s[44:47], s72 offen offset:1024 lds\n"
"v_mfma_f32_16x16x32_bf16 v[16:19], a[180:183], v[80:83], v[16:19]\n"
"ds_read_b64_tr_b16 a[204:205], v115 offset:2048\n"
"ds_read_b64_tr_b16 a[206:207], v115 offset:2560\n"
"v_mfma_f32_16x16x32_bf16 v[24:27], a[180:183], v[84:87], v[24:27]\n"
"ds_read_b128 v[48:51], v66 offset:8192\n"
"buffer_load_dwordx4 v110, s[44:47], s72 offen offset:2048 lds\n"
"v_mfma_f32_16x16x32_bf16 v[20:23], a[188:191], v[80:83], v[20:23]\n"
"ds_read_b128 v[52:55], v66 offset:24576\n"
"ds_read_b128 v[56:59], v66 offset:9216\n"
"v_mfma_f32_16x16x32_bf16 v[28:31], a[188:191], v[84:87], v[28:31]\n"
"ds_read_b128 v[60:63], v66 offset:25600\n"
"buffer_load_dwordx4 v111, s[44:47], s72 offen offset:3072 lds\n"
"s_waitcnt vmcnt(8) lgkmcnt(0)\n"
"s_barrier\n"
"v_mfma_f32_16x16x32_bf16 v[16:19], a[192:195], v[48:51], v[16:19]\n"
"ds_read_b64_tr_b16 a[208:209], v114 offset:16384\n"
"ds_read_b64_tr_b16 a[210:211], v114 offset:16896\n"
"v_mfma_f32_16x16x32_bf16 v[24:27], a[192:195], v[52:55], v[24:27]\n"
"ds_read_b64_tr_b16 a[212:213], v114 offset:18432\n"
"s_add_u32 m0, s73, 32768\n"
"s_xor_b32 s72, s101, 0\n"
"buffer_load_dwordx4 v108, s[48:51], s72 offen offset:0 lds\n"
"v_mfma_f32_16x16x32_bf16 v[20:23], a[200:203], v[48:51], v[20:23]\n"
"ds_read_b64_tr_b16 a[214:215], v114 offset:18944\n"
"ds_read_b64_tr_b16 a[216:217], v115 offset:16384\n"
"v_mfma_f32_16x16x32_bf16 v[28:31], a[200:203], v[52:55], v[28:31]\n"
"ds_read_b64_tr_b16 a[218:219], v115 offset:16896\n"
"buffer_load_dwordx4 v109, s[48:51], s72 offen offset:1024 lds\n"
"v_mfma_f32_16x16x32_bf16 v[16:19], a[196:199], v[56:59], v[16:19]\n"
"ds_read_b64_tr_b16 a[220:221], v115 offset:18432\n"
"ds_read_b64_tr_b16 a[222:223], v115 offset:18944\n"
"v_mfma_f32_16x16x32_bf16 v[24:27], a[196:199], v[60:63], v[24:27]\n"
"ds_read_b128 v[72:75], v66 offset:10240\n"
"buffer_load_dwordx4 v110, s[48:51], s72 offen offset:2048 lds\n"
"v_mfma_f32_16x16x32_bf16 v[20:23], a[204:207], v[56:59], v[20:23]\n"
"ds_read_b128 v[76:79], v66 offset:26624\n"
"ds_read_b128 v[80:83], v66 offset:11264\n"
"v_mfma_f32_16x16x32_bf16 v[28:31], a[204:207], v[60:63], v[28:31]\n"
"ds_read_b128 v[84:87], v66 offset:27648\n"
"buffer_load_dwordx4 v111, s[48:51], s72 offen offset:3072 lds\n"
"s_waitcnt vmcnt(8) lgkmcnt(0)\n"
"s_barrier\n"
"v_mfma_f32_16x16x32_bf16 v[16:19], a[208:211], v[72:75], v[16:19]\n"
"ds_read_b64_tr_b16 a[224:225], v112 offset:0\n"
"ds_read_b64_tr_b16 a[226:227], v112 offset:512\n"
"v_mfma_f32_16x16x32_bf16 v[24:27], a[208:211], v[76:79], v[24:27]\n"
"ds_read_b64_tr_b16 a[228:229], v112 offset:2048\n"
"s_add_u32 m0, s73, 49152\n"
"s_xor_b32 s72, s101, 524288\n"
"buffer_load_dwordx4 v108, s[48:51], s72 offen offset:0 lds\n"
"v_mfma_f32_16x16x32_bf16 v[20:23], a[216:219], v[72:75], v[20:23]\n"
"ds_read_b64_tr_b16 a[230:231], v112 offset:2560\n"
"ds_read_b64_tr_b16 a[232:233], v113 offset:0\n"
"v_mfma_f32_16x16x32_bf16 v[28:31], a[216:219], v[76:79], v[28:31]\n"
"ds_read_b64_tr_b16 a[234:235], v113 offset:512\n"
"buffer_load_dwordx4 v109, s[48:51], s72 offen offset:1024 lds\n"
"v_mfma_f32_16x16x32_bf16 v[16:19], a[212:215], v[80:83], v[16:19]\n"
"ds_read_b64_tr_b16 a[236:237], v113 offset:2048\n"
"ds_read_b64_tr_b16 a[238:239], v113 offset:2560\n"
"v_mfma_f32_16x16x32_bf16 v[24:27], a[212:215], v[84:87], v[24:27]\n"
"ds_read_b128 v[48:51], v66 offset:12288\n"
"buffer_load_dwordx4 v110, s[48:51], s72 offen offset:2048 lds\n"
"v_mfma_f32_16x16x32_bf16 v[20:23], a[220:223], v[80:83], v[20:23]\n"
"ds_read_b128 v[52:55], v66 offset:28672\n"
"ds_read_b128 v[56:59], v66 offset:13312\n"
"v_mfma_f32_16x16x32_bf16 v[28:31], a[220:223], v[84:87], v[28:31]\n"
"ds_read_b128 v[60:63], v66 offset:29696\n"
"buffer_load_dwordx4 v111, s[48:51], s72 offen offset:3072 lds\n"
"s_waitcnt vmcnt(8) lgkmcnt(0)\n"
"s_barrier\n"
"v_mfma_f32_16x16x32_bf16 v[16:19], a[224:227], v[48:51], v[16:19]\n"
"ds_read_b64_tr_b16 a[240:241], v112 offset:16384\n"
"ds_read_b64_tr_b16 a[242:243], v112 offset:16896\n"
"v_mfma_f32_16x16x32_bf16 v[24:27], a[224:227], v[52:55], v[24:27]\n"
"ds_read_b64_tr_b16 a[244:245], v112 offset:18432\n"
"s_add_u32 m0, s73, 65536\n"
"s_xor_b32 s72, s101, 1048576\n"
"buffer_load_dwordx4 v108, s[48:51], s72 offen offset:0 lds\n"
"v_mfma_f32_16x16x32_bf16 v[20:23], a[232:235], v[48:51], v[20:23]\n"
"ds_read_b64_tr_b16 a[246:247], v112 offset:18944\n"
"ds_read_b64_tr_b16 a[248:249], v113 offset:16384\n"
"v_mfma_f32_16x16x32_bf16 v[28:31], a[232:235], v[52:55], v[28:31]\n"
"ds_read_b64_tr_b16 a[250:251], v113 offset:16896\n"
"buffer_load_dwordx4 v109, s[48:51], s72 offen offset:1024 lds\n"
"v_mfma_f32_16x16x32_bf16 v[16:19], a[228:231], v[56:59], v[16:19]\n"
"ds_read_b64_tr_b16 a[252:253], v113 offset:18432\n"
"ds_read_b64_tr_b16 a[254:255], v113 offset:18944\n"
"v_mfma_f32_16x16x32_bf16 v[24:27], a[228:231], v[60:63], v[24:27]\n"
"ds_read_b128 v[72:75], v66 offset:14336\n"
"buffer_load_dwordx4 v110, s[48:51], s72 offen offset:2048 lds\n"
"v_mfma_f32_16x16x32_bf16 v[20:23], a[236:239], v[56:59], v[20:23]\n"
"ds_read_b128 v[76:79], v66 offset:30720\n"
"ds_read_b128 v[80:83], v66 offset:15360\n"
"v_mfma_f32_16x16x32_bf16 v[28:31], a[236:239], v[60:63], v[28:31]\n"
"ds_read_b128 v[84:87], v66 offset:31744\n"
"buffer_load_dwordx4 v111, s[48:51], s72 offen offset:3072 lds\n"
"s_waitcnt vmcnt(8) lgkmcnt(0)\n"
"s_barrier\n"
"v_mfma_f32_16x16x32_bf16 v[16:19], a[240:243], v[72:75], v[16:19]\n"
"ds_read_b64_tr_b16 v[128:129], v112 offset:32768\n"
"ds_read_b64_tr_b16 v[130:131], v112 offset:33280\n"
"v_mfma_f32_16x16x32_bf16 v[24:27], a[240:243], v[76:79], v[24:27]\n"
"ds_read_b64_tr_b16 v[132:133], v112 offset:34816\n"
"s_add_u32 m0, s73, 81920\n"
"s_xor_b32 s72, s101, 1572864\n"
"buffer_load_dwordx4 v108, s[48:51], s72 offen offset:0 lds\n"
"v_mfma_f32_16x16x32_bf16 v[20:23], a[248:251], v[72:75], v[20:23]\n"
"ds_read_b64_tr_b16 v[134:135], v112 offset:35328\n"
"ds_read_b64_tr_b16 v[136:137], v113 offset:32768\n"
"v_mfma_f32_16x16x32_bf16 v[28:31], a[248:251], v[76:79], v[28:31]\n"
"ds_read_b64_tr_b16 v[138:139], v113 offset:33280\n"
"buffer_load_dwordx4 v109, s[48:51], s72 offen offset:1024 lds\n"
"v_mfma_f32_16x16x32_bf16 v[16:19], a[244:247], v[80:83], v[16:19]\n"
"ds_read_b64_tr_b16 v[140:141], v113 offset:34816\n"
"ds_read_b64_tr_b16 v[142:143], v113 offset:35328\n"
"v_mfma_f32_16x16x32_bf16 v[24:27], a[244:247], v[84:87], v[24:27]\n"
"ds_read_b128 v[48:51], v66 offset:0\n"
"buffer_load_dwordx4 v110, s[48:51], s72 offen offset:2048 lds\n"
"v_mfma_f32_16x16x32_bf16 v[20:23], a[252:255], v[80:83], v[20:23]\n"
"ds_read_b128 v[52:55], v66 offset:16384\n"
"ds_read_b128 v[56:59], v66 offset:1024\n"
"v_mfma_f32_16x16x32_bf16 v[28:31], a[252:255], v[84:87], v[28:31]\n"
"ds_read_b128 v[60:63], v66 offset:17408\n"
"buffer_load_dwordx4 v111, s[48:51], s72 offen offset:3072 lds\n"
"s_waitcnt vmcnt(8) lgkmcnt(0)\n"
"s_barrier\n"
"v_mfma_f32_16x16x32_bf16 v[32:35], v[48:51], v[128:131], 0\n"
"ds_read_b64_tr_b16 v[144:145], v112 offset:49152\n"
"ds_read_b64_tr_b16 v[146:147], v112 offset:49664\n"
"v_mfma_f32_16x16x32_bf16 v[40:43], v[52:55], v[128:131], 0\n"
"ds_read_b64_tr_b16 v[148:149], v112 offset:51200\n"
"s_add_u32 m0, s73, 0\n"
"s_xor_b32 s72, s101, 2097152\n"
"buffer_load_dwordx4 v108, s[48:51], s72 offen offset:0 lds\n"
"v_mfma_f32_16x16x32_bf16 v[36:39], v[48:51], v[136:139], 0\n"
"ds_read_b64_tr_b16 v[150:151], v112 offset:51712\n"
"ds_read_b64_tr_b16 v[152:153], v113 offset:49152\n"
"v_mfma_f32_16x16x32_bf16 v[44:47], v[52:55], v[136:139], 0\n"
"ds_read_b64_tr_b16 v[154:155], v113 offset:49664\n"
"buffer_load_dwordx4 v109, s[48:51], s72 offen offset:1024 lds\n"
"v_mfma_f32_16x16x32_bf16 v[32:35], v[56:59], v[132:135], v[32:35]\n"
"ds_read_b64_tr_b16 v[156:157], v113 offset:51200\n"
"ds_read_b64_tr_b16 v[158:159], v113 offset:51712\n"
"v_mfma_f32_16x16x32_bf16 v[40:43], v[60:63], v[132:135], v[40:43]\n"
"ds_read_b128 v[72:75], v66 offset:2048\n"
"buffer_load_dwordx4 v110, s[48:51], s72 offen offset:2048 lds\n"
"v_mfma_f32_16x16x32_bf16 v[36:39], v[56:59], v[140:143], v[36:39]\n"
"ds_read_b128 v[76:79], v66 offset:18432\n"
"ds_read_b128 v[80:83], v66 offset:3072\n"
"v_mfma_f32_16x16x32_bf16 v[44:47], v[60:63], v[140:143], v[44:47]\n"
"ds_read_b128 v[84:87], v66 offset:19456\n"
"buffer_load_dwordx4 v111, s[48:51], s72 offen offset:3072 lds\n"
"s_waitcnt vmcnt(8) lgkmcnt(0)\n"
"s_barrier\n"
"v_mfma_f32_16x16x32_bf16 v[32:35], v[72:75], v[144:147], v[32:35]\n"
"ds_read_b64_tr_b16 v[160:161], v114 offset:0\n"
"ds_read_b64_tr_b16 v[162:163], v114 offset:512\n"
"v_mfma_f32_16x16x32_bf16 v[40:43], v[76:79], v[144:147], v[40:43]\n"
"ds_read_b64_tr_b16 v[164:165], v114 offset:2048\n"
"s_add_u32 m0, s73, 16384\n"
"s_xor_b32 s72, s101, 2621440\n"
"buffer_load_dwordx4 v108, s[48:51], s72 offen offset:0 lds\n"
"v_mfma_f32_16x16x32_bf16 v[36:39], v[72:75], v[152:155], v[36:39]\n"
"ds_read_b64_tr_b16 v[166:167], v114 offset:2560\n"
"ds_read_b64_tr_b16 v[168:169], v115 offset:0\n"
"v_mfma_f32_16x16x32_bf16 v[44:47], v[76:79], v[152:155], v[44:47]\n"
"ds_read_b64_tr_b16 v[170:171], v115 offset:512\n"
"buffer_load_dwordx4 v109, s[48:51], s72 offen offset:1024 lds\n"
"v_mfma_f32_16x16x32_bf16 v[32:35], v[80:83], v[148:151], v[32:35]\n"
"ds_read_b64_tr_b16 v[172:173], v115 offset:2048\n"
"ds_read_b64_tr_b16 v[174:175], v115 offset:2560\n"
"v_mfma_f32_16x16x32_bf16 v[40:43], v[84:87], v[148:151], v[40:43]\n"
"ds_read_b128 v[48:51], v66 offset:4096\n"
"buffer_load_dwordx4 v110, s[48:51], s72 offen offset:2048 lds\n"
"v_mfma_f32_16x16x32_bf16 v[36:39], v[80:83], v[156:159], v[36:39]\n"
"ds_read_b128 v[52:55], v66 offset:20480\n"
"ds_read_b128 v[56:59], v66 offset:5120\n"
"v_mfma_f32_16x16x32_bf16 v[44:47], v[84:87], v[156:159], v[44:47]\n"
"ds_read_b128 v[60:63], v66 offset:21504\n"
"buffer_load_dwordx4 v111, s[48:51], s72 offen offset:3072 lds\n"
"s_waitcnt vmcnt(8) lgkmcnt(0)\n"
"s_barrier\n"
"v_mfma_f32_16x16x32_bf16 v[32:35], v[48:51], v[160:163], v[32:35]\n"
"ds_read_b64_tr_b16 v[176:177], v114 offset:16384\n"
"ds_read_b64_tr_b16 v[178:179], v114 offset:16896\n"
"v_mfma_f32_16x16x32_bf16 v[40:43], v[52:55], v[160:163], v[40:43]\n"
"ds_read_b64_tr_b16 v[180:181], v114 offset:18432\n"
"s_add_u32 m0, s73, 32768\n"
"s_xor_b32 s72, s101, 3145728\n"
"buffer_load_dwordx4 v108, s[48:51], s72 offen offset:0 lds\n"
"v_mfma_f32_16x16x32_bf16 v[36:39], v[48:51], v[168:171], v[36:39]\n"
"ds_read_b64_tr_b16 v[182:183], v114 offset:18944\n"
"ds_read_b64_tr_b16 v[184:185], v115 offset:16384\n"
"v_mfma_f32_16x16x32_bf16 v[44:47], v[52:55], v[168:171], v[44:47]\n"
"ds_read_b64_tr_b16 v[186:187], v115 offset:16896\n"
"buffer_load_dwordx4 v109, s[48:51], s72 offen offset:1024 lds\n"
"v_mfma_f32_16x16x32_bf16 v[32:35], v[56:59], v[164:167], v[32:35]\n"
"ds_read_b64_tr_b16 v[188:189], v115 offset:18432\n"
"ds_read_b64_tr_b16 v[190:191], v115 offset:18944\n"
"v_mfma_f32_16x16x32_bf16 v[40:43], v[60:63], v[164:167], v[40:43]\n"
"ds_read_b128 v[72:75], v66 offset:6144\n"
"buffer_load_dwordx4 v110, s[48:51], s72 offen offset:2048 lds\n"
"v_mfma_f32_16x16x32_bf16 v[36:39], v[56:59], v[172:175], v[36:39]\n"
"ds_read_b128 v[76:79], v66 offset:22528\n"
"ds_read_b128 v[80:83], v66 offset:7168\n"
"v_mfma_f32_16x16x32_bf16 v[44:47], v[60:63], v[172:175], v[44:47]\n"
"ds_read_b128 v[84:87], v66 offset:23552\n"
"buffer_load_dwordx4 v111, s[48:51], s72 offen offset:3072 lds\n"
"s_waitcnt vmcnt(8) lgkmcnt(0)\n"
"s_barrier\n"
"v_mfma_f32_16x16x32_bf16 v[32:35], v[72:75], v[176:179], v[32:35]\n"
"ds_read_b64_tr_b16 v[192:193], v112 offset:0\n"
"ds_read_b64_tr_b16 v[194:195], v112 offset:512\n"
"v_mfma_f32_16x16x32_bf16 v[40:43], v[76:79], v[176:179], v[40:43]\n"
"ds_read_b64_tr_b16 v[196:197], v112 offset:2048\n"
"s_add_u32 m0, s73, 49152\n"
"s_xor_b32 s72, s101, 3670016\n"
"buffer_load_dwordx4 v108, s[48:51], s72 offen offset:0 lds\n"
"v_mfma_f32_16x16x32_bf16 v[36:39], v[72:75], v[184:187], v[36:39]\n"
"ds_read_b64_tr_b16 v[198:199], v112 offset:2560\n"
"ds_read_b64_tr_b16 v[200:201], v113 offset:0\n"
"v_mfma_f32_16x16x32_bf16 v[44:47], v[76:79], v[184:187], v[44:47]\n"
"ds_read_b64_tr_b16 v[202:203], v113 offset:512\n"
"buffer_load_dwordx4 v109, s[48:51], s72 offen offset:1024 lds\n"
"v_mfma_f32_16x16x32_bf16 v[32:35], v[80:83], v[180:183], v[32:35]\n"
"ds_read_b64_tr_b16 v[204:205], v113 offset:2048\n"
"ds_read_b64_tr_b16 v[206:207], v113 offset:2560\n"
"v_mfma_f32_16x16x32_bf16 v[40:43], v[84:87], v[180:183], v[40:43]\n"
"ds_read_b128 v[48:51], v66 offset:8192\n"
"buffer_load_dwordx4 v110, s[48:51], s72 offen offset:2048 lds\n"
"v_mfma_f32_16x16x32_bf16 v[36:39], v[80:83], v[188:191], v[36:39]\n"
"ds_read_b128 v[52:55], v66 offset:24576\n"
"ds_read_b128 v[56:59], v66 offset:9216\n"
"v_mfma_f32_16x16x32_bf16 v[44:47], v[84:87], v[188:191], v[44:47]\n"
"ds_read_b128 v[60:63], v66 offset:25600\n"
"buffer_load_dwordx4 v111, s[48:51], s72 offen offset:3072 lds\n"
"v_lshlrev_b32 v100, 3, v119\n"
"s_mov_b32 s74, 3146752\n"
"buffer_load_dwordx2 v[96:97], v100, s[60:63], s74 offen sc1\n"
"buffer_load_dwordx2 v[98:99], v100, s[60:63], s74 offen offset:1024 sc1\n"
"s_waitcnt vmcnt(10) lgkmcnt(0)\n"
"s_barrier\n"
"v_mfma_f32_16x16x32_bf16 v[32:35], v[48:51], v[192:195], v[32:35]\n"
"ds_read_b64_tr_b16 v[208:209], v112 offset:16384\n"
"ds_read_b64_tr_b16 v[210:211], v112 offset:16896\n"
"v_mfma_f32_16x16x32_bf16 v[40:43], v[52:55], v[192:195], v[40:43]\n"
"ds_read_b64_tr_b16 v[212:213], v112 offset:18432\n"
"s_add_u32 m0, s73, 65536\n"
"s_xor_b32 s72, s101, 0\n"
"buffer_load_dwordx4 v108, s[40:43], s72 offen offset:0 lds\n"
"v_mfma_f32_16x16x32_bf16 v[36:39], v[48:51], v[200:203], v[36:39]\n"
"ds_read_b64_tr_b16 v[214:215], v112 offset:18944\n"
"ds_read_b64_tr_b16 v[216:217], v113 offset:16384\n"
"v_mfma_f32_16x16x32_bf16 v[44:47], v[52:55], v[200:203], v[44:47]\n"
"ds_read_b64_tr_b16 v[218:219], v113 offset:16896\n"
"buffer_load_dwordx4 v109, s[40:43], s72 offen offset:1024 lds\n"
"v_mfma_f32_16x16x32_bf16 v[32:35], v[56:59], v[196:199], v[32:35]\n"
"ds_read_b64_tr_b16 v[220:221], v113 offset:18432\n"
"ds_read_b64_tr_b16 v[222:223], v113 offset:18944\n"
"v_mfma_f32_16x16x32_bf16 v[40:43], v[60:63], v[196:199], v[40:43]\n"
"ds_read_b128 v[72:75], v66 offset:10240\n"
"buffer_load_dwordx4 v110, s[40:43], s72 offen offset:2048 lds\n"
"v_mfma_f32_16x16x32_bf16 v[36:39], v[56:59], v[204:207], v[36:39]\n"
"ds_read_b128 v[76:79], v66 offset:26624\n"
"ds_read_b128 v[80:83], v66 offset:11264\n"
"v_mfma_f32_16x16x32_bf16 v[44:47], v[60:63], v[204:207], v[44:47]\n"
"ds_read_b128 v[84:87], v66 offset:27648\n"
"buffer_load_dwordx4 v111, s[40:43], s72 offen offset:3072 lds\n"
"s_waitcnt vmcnt(10) lgkmcnt(0)\n"
"s_barrier\n"
"v_mfma_f32_16x16x32_bf16 v[32:35], v[72:75], v[208:211], v[32:35]\n"
"ds_read_b64_tr_b16 v[224:225], v112 offset:32768\n"
"ds_read_b64_tr_b16 v[226:227], v112 offset:33280\n"
"v_mfma_f32_16x16x32_bf16 v[40:43], v[76:79], v[208:211], v[40:43]\n"
"ds_read_b64_tr_b16 v[228:229], v112 offset:34816\n"
"s_add_u32 m0, s73, 81920\n"
"s_xor_b32 s72, s101, 524288\n"
"buffer_load_dwordx4 v108, s[40:43], s72 offen offset:0 lds\n"
"v_mfma_f32_16x16x32_bf16 v[36:39], v[72:75], v[216:219], v[36:39]\n"
"ds_read_b64_tr_b16 v[230:231], v112 offset:35328\n"
"ds_read_b64_tr_b16 v[232:233], v113 offset:32768\n"
"v_mfma_f32_16x16x32_bf16 v[44:47], v[76:79], v[216:219], v[44:47]\n"
"ds_read_b64_tr_b16 v[234:235], v113 offset:33280\n"
"buffer_load_dwordx4 v109, s[40:43], s72 offen offset:1024 lds\n"
"v_mfma_f32_16x16x32_bf16 v[32:35], v[80:83], v[212:215], v[32:35]\n"
"ds_read_b64_tr_b16 v[236:237], v113 offset:34816\n"
"ds_read_b64_tr_b16 v[238:239], v113 offset:35328\n"
"v_mfma_f32_16x16x32_bf16 v[40:43], v[84:87], v[212:215], v[40:43]\n"
"ds_read_b128 v[48:51], v66 offset:12288\n"
"buffer_load_dwordx4 v110, s[40:43], s72 offen offset:2048 lds\n"
"v_mfma_f32_16x16x32_bf16 v[36:39], v[80:83], v[220:223], v[36:39]\n"
"ds_read_b128 v[52:55], v66 offset:28672\n"
"ds_read_b128 v[56:59], v66 offset:13312\n"
"v_mfma_f32_16x16x32_bf16 v[44:47], v[84:87], v[220:223], v[44:47]\n"
"ds_read_b128 v[60:63], v66 offset:29696\n"
"buffer_load_dwordx4 v111, s[40:43], s72 offen offset:3072 lds\n"
"s_waitcnt vmcnt(10) lgkmcnt(0)\n"
"s_barrier\n"
"v_mfma_f32_16x16x32_bf16 v[32:35], v[48:51], v[224:227], v[32:35]\n"
"ds_read_b64_tr_b16 v[240:241], v112 offset:49152\n"
"ds_read_b64_tr_b16 v[242:243], v112 offset:49664\n"
"v_mfma_f32_16x16x32_bf16 v[40:43], v[52:55], v[224:227], v[40:43]\n"
"ds_read_b64_tr_b16 v[244:245], v112 offset:51200\n"
"s_add_u32 m0, s73, 0\n"
"s_xor_b32 s72, s101, 1048576\n"
"buffer_load_dwordx4 v108, s[40:43], s72 offen offset:0 lds\n"
"v_mfma_f32_16x16x32_bf16 v[36:39], v[48:51], v[232:235], v[36:39]\n"
"ds_read_b64_tr_b16 v[246:247], v112 offset:51712\n"
"ds_read_b64_tr_b16 v[248:249], v113 offset:49152\n"
"v_mfma_f32_16x16x32_bf16 v[44:47], v[52:55], v[232:235], v[44:47]\n"
"ds_read_b64_tr_b16 v[250:251], v113 offset:49664\n"
"buffer_load_dwordx4 v109, s[40:43], s72 offen offset:1024 lds\n"
"v_mfma_f32_16x16x32_bf16 v[32:35], v[56:59], v[228:231], v[32:35]\n"
"ds_read_b64_tr_b16 v[252:253], v113 offset:51200\n"
"ds_read_b64_tr_b16 v[254:255], v113 offset:51712\n"
"v_mfma_f32_16x16x32_bf16 v[40:43], v[60:63], v[228:231], v[40:43]\n"
"ds_read_b128 v[72:75], v66 offset:14336\n"
"buffer_load_dwordx4 v110, s[40:43], s72 offen offset:2048 lds\n"
"v_mfma_f32_16x16x32_bf16 v[36:39], v[56:59], v[236:239], v[36:39]\n"
"ds_read_b128 v[76:79], v66 offset:30720\n"
"ds_read_b128 v[80:83], v66 offset:15360\n"
"v_mfma_f32_16x16x32_bf16 v[44:47], v[60:63], v[236:239], v[44:47]\n"
"ds_read_b128 v[84:87], v66 offset:31744\n"
"buffer_load_dwordx4 v111, s[40:43], s72 offen offset:3072 lds\n"
"s_waitcnt vmcnt(12)\n"
"s_mov_b32 s78, 16384\n"
".Law71_%=:\n"
"v_xor_b32 v96, s34, v96\n"
"v_xor_b32 v97, s35, v97\n"
"v_and_or_b32 v96, v97, -4, v96\n"
"v_cmp_ne_u32 vcc, 0, v96\n"
"v_cmp_ne_u64 s[64:65], s[34:35], v[98:99]\n"
"s_nop 0\n"
"s_or_b64 vcc, vcc, s[64:65]\n"
"s_cbranch_vccz .Laok72_%=\n"
"s_sub_u32 s78, s78, 1\n"
"s_cbranch_scc1 .Lfail5_%=\n"
"s_sleep 8\n"
"buffer_load_dwordx2 v[96:97], v100, s[60:63], s74 offen sc1\n"
"buffer_load_dwordx2 v[98:99], v100, s[60:63], s74 offen offset:1024 sc1\n"
"s_waitcnt vmcnt(0)\n"
"s_branch .Law71_%=\n"
".Laok72_%=:\n"
"s_mov_b32 s77, 0\n"
"v_cmp_lt_u32 vcc, 1, v97\n"
"v_and_b32 v97, 1, v97\n"
"s_and_b32 s64, vcc_lo, 0xf00ff00f\n"
"s_and_b32 s65, vcc_hi, 0xf00ff00f\n"
"s_bcnt1_i32_b64 s64, s[64:65]\n"
"s_cmp_ge_u32 s64, 16\n"
"s_addc_u32 s77, s77, s77\n"
"s_and_b32 s64, vcc_lo, 0xff00ff0\n"
"s_and_b32 s65, vcc_hi, 0xff00ff0\n"
"s_bcnt1_i32_b64 s64, s[64:65]\n"
"s_cmp_ge_u32 s64, 16\n"
"s_addc_u32 s77, s77, s77\n"
"v_cmp_ne_u32 vcc, 0, v97\n"
"s_nop 0\n"
"s_and_b32 s64, vcc_lo, 0xffffffff\n"
"s_and_b32 s65, vcc_hi, 0xffffffff\n"
"s_bcnt1_i32_b64 s64, s[64:65]\n"
"s_cmp_ge_u32 s64, 32\n"
"s_addc_u32 s77, s77, s77\n"
"s_add_u32 s23, s39, 2\n"
"s_mov_b32 s9, 32768\n"
"s_and_b32 s25, s77, 1\n"
"s_lshl_b32 s25, s25, 13\n"
"s_add_u32 s25, s25, 1048576\n"
"s_lshl_b32 s64, s100, 2\n"
"s_add_u32 s25, s25, s64\n"
"s_lshl_b32 s71, s23, 15\n"
"s_lshl_b32 s65, s37, 13\n"
"s_add_u32 s71, s71, s65\n"
"s_add_u32 s71, s71, 4194304\n"
"s_add_u32 m0, s65, s9\n"
"s_xor_b32 s71, s71, s100\n"
"buffer_load_dwordx4 v64, s[60:63], s71 offen offset:0 sc1 lds\n"
"buffer_load_dwordx4 v64, s[60:63], s71 offen offset:1024 sc1 lds\n"
"buffer_load_dwordx4 v64, s[60:63], s71 offen offset:2048 sc1 lds\n"
"buffer_load_dwordx4 v64, s[60:63], s71 offen offset:3072 sc1 lds\n"
"s_add_u32 m0, m0, 4096\n"
"s_add_u32 s71, s71, 4096\n"
"buffer_load_dwordx4 v64, s[60:63], s71 offen offset:0 sc1 lds\n"
"buffer_load_dwordx4 v64, s[60:63], s71 offen offset:1024 sc1 lds\n"
"buffer_load_dwordx4 v64, s[60:63], s71 offen offset:2048 sc1 lds\n"
"buffer_load_dwordx4 v64, s[60:63], s71 offen offset:3072 sc1 lds\n"
"s_waitcnt vmcnt(16) lgkmcnt(0)\n"
"s_barrier\n"
"v_mfma_f32_16x16x32_bf16 v[32:35], v[72:75], v[240:243], v[32:35]\n"
"ds_read_b64_tr_b16 a[0:1], v114 offset:0\n"
"ds_read_b64_tr_b16 a[2:3], v114 offset:512\n"
"v_mfma_f32_16x16x32_bf16 v[40:43], v[76:79], v[240:243], v[40:43]\n"
"ds_read_b64_tr_b16 a[4:5], v114 offset:2048\n"
"v_mfma_f32_16x16x32_bf16 v[36:39], v[72:75], v[248:251], v[36:39]\n"
"ds_read_b64_tr_b16 a[6:7], v114 offset:2560\n"
"ds_read_b64_tr_b16 a[8:9], v115 offset:0\n"
"v_mfma_f32_16x16x32_bf16 v[44:47], v[76:79], v[248:251], v[44:47]\n"
"ds_read_b64_tr_b16 a[10:11], v115 offset:512\n"
"v_mfma_f32_16x16x32_bf16 v[32:35], v[80:83], v[244:247], v[32:35]\n"
"ds_read_b64_tr_b16 a[12:13], v115 offset:2048\n"
"ds_read_b64_tr_b16 a[14:15], v115 offset:2560\n"
"v_mfma_f32_16x16x32_bf16 v[40:43], v[84:87], v[244:247], v[40:43]\n"
"v_mfma_f32_16x16x32_bf16 v[36:39], v[80:83], v[252:255], v[36:39]\n"
"v_mfma_f32_16x16x32_bf16 v[44:47], v[84:87], v[252:255], v[44:47]\n"
"v_lshrrev_b32 v104, 1, v64\n"
"s_add_u32 s27, s22, 1\n"
"s_and_b32 s64, s22, 7\n"
"s_lshl_b32 s65, s22, 12\n"
"s_xor_b32 s65, s65, s25\n"
"s_and_b32 s65, s65, 8192\n"
"s_lshl_b32 s64, s64, 20\n"
"s_add_u32 s64, s64, s65\n"
"s_add_u32 s64, s64, s10\n"
"s_add_u32 s65, s64, 16384\n"
"v_cvt_pk_bf16_f32 v16, v16, v20\n"
"v_cvt_pk_bf16_f32 v17, v17, v21\n"
"v_cvt_pk_bf16_f32 v18, v18, v22\n"
"v_cvt_pk_bf16_f32 v19, v19, v23\n"
"buffer_store_dwordx4 v[16:19], v69, s[60:63], s64 offen offset:0 sc1\n"
"v_cvt_pk_bf16_f32 v20, v24, v28\n"
"v_cvt_pk_bf16_f32 v21, v25, v29\n"
"v_cvt_pk_bf16_f32 v22, v26, v30\n"
"v_cvt_pk_bf16_f32 v23, v27, v31\n"
"buffer_store_dwordx4 v[20:23], v69, s[60:63], s64 offen offset:1024 sc1\n"
"v_cvt_pk_bf16_f32 v24, v32, v33\n"
"v_cvt_pk_bf16_f32 v25, v40, v41\n"
"buffer_store_dwordx2 v[24:25], v104, s[60:63], s65 offen offset:0 sc1\n"
"v_cvt_pk_bf16_f32 v26, v34, v35\n"
"v_cvt_pk_bf16_f32 v27, v42, v43\n"
"buffer_store_dwordx2 v[26:27], v104, s[60:63], s65 offen offset:512 sc1\n"
"v_cvt_pk_bf16_f32 v28, v36, v37\n"
"v_cvt_pk_bf16_f32 v29, v44, v45\n"
"buffer_store_dwordx2 v[28:29], v104, s[60:63], s65 offen offset:1024 sc1\n"
"v_cvt_pk_bf16_f32 v30, v38, v39\n"
"v_cvt_pk_bf16_f32 v31, v46, v47\n"
"buffer_store_dwordx2 v[30:31], v104, s[60:63], s65 offen offset:1536 sc1\n"
"s_waitcnt vmcnt(6)\n"
"s_add_u32 m0, s73, 16384\n"
"s_xor_b32 s72, s101, 1572864\n"
"buffer_load_dwordx4 v108, s[40:43], s72 offen offset:0 lds\n"
"buffer_load_dwordx4 v109, s[40:43], s72 offen offset:1024 lds\n"
"buffer_load_dwordx4 v110, s[40:43], s72 offen offset:2048 lds\n"
"buffer_load_dwordx4 v111, s[40:43], s72 offen offset:3072 lds\n"
"ds_read_b128 v[48:51], v66 offset:0\n"
"ds_read_b128 v[52:55], v66 offset:16384\n"
"ds_read_b128 v[56:59], v66 offset:1024\n"
"ds_read_b128 v[60:63], v66 offset:17408\n"
"s_waitcnt lgkmcnt(0)\n"
"s_barrier\n"
"v_mfma_f32_16x16x32_bf16 v[0:3], a[0:3], v[48:51], 0\n"
"ds_read_b128 v[32:35], v66 offset:32768\n"
"v_mfma_f32_16x16x32_bf16 v[8:11], a[0:3], v[52:55], 0\n"
"ds_read_b128 v[36:39], v66 offset:49152\n"
"v_mfma_f32_16x16x32_bf16 v[4:7], a[8:11], v[48:51], 0\n"
"ds_read_b128 v[40:43], v66 offset:33792\n"
"v_mfma_f32_16x16x32_bf16 v[12:15], a[8:11], v[52:55], 0\n"
"ds_read_b128 v[44:47], v66 offset:50176\n"
"v_mfma_f32_16x16x32_bf16 v[0:3], a[4:7], v[56:59], v[0:3]\n"
"v_mfma_f32_16x16x32_bf16 v[8:11], a[4:7], v[60:63], v[8:11]\n"
"v_mfma_f32_16x16x32_bf16 v[4:7], a[12:15], v[56:59], v[4:7]\n"
"v_mfma_f32_16x16x32_bf16 v[12:15], a[12:15], v[60:63], v[12:15]\n"
"s_waitcnt lgkmcnt(0)\n"
"v_mfma_f32_16x16x32_bf16 v[72:75], a[128:131], v[32:35], 0\n"
"ds_read_b64_tr_b16 a[16:17], v114 offset:16384\n"
"v_mfma_f32_16x16x32_bf16 v[80:83], a[128:131], v[36:39], 0\n"
"ds_read_b64_tr_b16 a[18:19], v114 offset:16896\n"
"v_mfma_f32_16x16x32_bf16 v[76:79], a[136:139], v[32:35], 0\n"
"ds_read_b64_tr_b16 a[20:21], v114 offset:18432\n"
"v_mfma_f32_16x16x32_bf16 v[84:87], a[136:139], v[36:39], 0\n"
"s_add_u32 m0, s73, 32768\n"
"s_xor_b32 s72, s101, 2097152\n"
"buffer_load_dwordx4 v108, s[40:43], s72 offen offset:0 lds\n"
"v_mfma_f32_16x16x32_bf16 v[72:75], a[132:135], v[40:43], v[72:75]\n"
"ds_read_b64_tr_b16 a[22:23], v114 offset:18944\n"
"v_mfma_f32_16x16x32_bf16 v[80:83], a[132:135], v[44:47], v[80:83]\n"
"ds_read_b64_tr_b16 a[24:25], v115 offset:16384\n"
"v_mfma_f32_16x16x32_bf16 v[76:79], a[140:143], v[40:43], v[76:79]\n"
"ds_read_b64_tr_b16 a[26:27], v115 offset:16896\n"
"v_mfma_f32_16x16x32_bf16 v[84:87], a[140:143], v[44:47], v[84:87]\n"
"buffer_load_dwordx4 v109, s[40:43], s72 offen offset:1024 lds\n"
"v_mfma_f32_16x16x32_bf16 v[88:91], v[32:35], v[128:131], 0\n"
"ds_read_b64_tr_b16 a[28:29], v115 offset:18432\n"
"v_mfma_f32_16x16x32_bf16 v[96:99], v[36:39], v[128:131], 0\n"
"ds_read_b64_tr_b16 a[30:31], v115 offset:18944\n"
"v_mfma_f32_16x16x32_bf16 v[92:95], v[32:35], v[136:139], 0\n"
"ds_read_b128 v[48:51], v66 offset:2048\n"
"v_mfma_f32_16x16x32_bf16 v[100:103], v[36:39], v[136:139], 0\n"
"buffer_load_dwordx4 v110, s[40:43], s72 offen offset:2048 lds\n"
"v_mfma_f32_16x16x32_bf16 v[88:91], v[40:43], v[132:135], v[88:91]\n"
"ds_read_b128 v[52:55], v66 offset:18432\n"
"v_mfma_f32_16x16x32_bf16 v[96:99], v[44:47], v[132:135], v[96:99]\n"
"ds_read_b128 v[56:59], v66 offset:3072\n"
"v_mfma_f32_16x16x32_bf16 v[92:95], v[40:43], v[140:143], v[92:95]\n"
"ds_read_b128 v[60:63], v66 offset:19456\n"
"v_mfma_f32_16x16x32_bf16 v[100:103], v[44:47], v[140:143], v[100:103]\n"
"buffer_load_dwordx4 v111, s[40:43], s72 offen offset:3072 lds\n"
"s_waitcnt vmcnt(8)\n"
"v_mov_b32 v120, s34\n"
"s_add_u32 s64, s35, s27\n"
"v_mov_b32 v121, s64\n"
"s_mov_b64 exec, 1\n"
"buffer_store_dwordx2 v[120:121], off, s[60:63], s21 sc1\n"
"s_mov_b64 exec, -1\n"
"s_waitcnt lgkmcnt(0)\n"
"s_barrier\n"
"v_mfma_f32_16x16x32_bf16 v[0:3], a[16:19], v[48:51], v[0:3]\n"
"ds_read_b128 v[32:35], v66 offset:34816\n"
"v_mfma_f32_16x16x32_bf16 v[8:11], a[16:19], v[52:55], v[8:11]\n"
"ds_read_b128 v[36:39], v66 offset:51200\n"
"v_mfma_f32_16x16x32_bf16 v[4:7], a[24:27], v[48:51], v[4:7]\n"
"ds_read_b128 v[40:43], v66 offset:35840\n"
"v_mfma_f32_16x16x32_bf16 v[12:15], a[24:27], v[52:55], v[12:15]\n"
"ds_read_b128 v[44:47], v66 offset:52224\n"
"v_mfma_f32_16x16x32_bf16 v[0:3], a[20:23], v[56:59], v[0:3]\n"
"v_mfma_f32_16x16x32_bf16 v[8:11], a[20:23], v[60:63], v[8:11]\n"
"v_mfma_f32_16x16x32_bf16 v[4:7], a[28:31], v[56:59], v[4:7]\n"
"v_mfma_f32_16x16x32_bf16 v[12:15], a[28:31], v[60:63], v[12:15]\n"
"s_waitcnt lgkmcnt(0)\n"
"v_mfma_f32_16x16x32_bf16 v[72:75], a[144:147], v[32:35], v[72:75]\n"
"ds_read_b64_tr_b16 a[32:33], v112 offset:0\n"
"v_mfma_f32_16x16x32_bf16 v[80:83], a[144:147], v[36:39], v[80:83]\n"
"ds_read_b64_tr_b16 a[34:35], v112 offset:512\n"
"v_mfma_f32_16x16x32_bf16 v[76:79], a[152:155], v[32:35], v[76:79]\n"
"ds_read_b64_tr_b16 a[36:37], v112 offset:2048\n"
"v_mfma_f32_16x16x32_bf16 v[84:87], a[152:155], v[36:39], v[84:87]\n"
"s_add_u32 m0, s73, 49152\n"
"s_xor_b32 s72, s101, 2621440\n"
"buffer_load_dwordx4 v108, s[40:43], s72 offen offset:0 lds\n"
"v_mfma_f32_16x16x32_bf16 v[72:75], a[148:151], v[40:43], v[72:75]\n"
"ds_read_b64_tr_b16 a[38:39], v112 offset:2560\n"
"v_mfma_f32_16x16x32_bf16 v[80:83], a[148:151], v[44:47], v[80:83]\n"
"ds_read_b64_tr_b16 a[40:41], v113 offset:0\n"
"v_mfma_f32_16x16x32_bf16 v[76:79], a[156:159], v[40:43], v[76:79]\n"
"ds_read_b64_tr_b16 a[42:43], v113 offset:512\n"
"v_mfma_f32_16x16x32_bf16 v[84:87], a[156:159], v[44:47], v[84:87]\n"
"buffer_load_dwordx4 v109, s[40:43], s72 offen offset:1024 lds\n"
"v_mfma_f32_16x16x32_bf16 v[88:91], v[32:35], v[144:147], v[88:91]\n"
"ds_read_b64_tr_b16 a[44:45], v113 offset:2048\n"
"v_mfma_f32_16x16x32_bf16 v[96:99], v[36:39], v[144:147], v[96:99]\n"
"ds_read_b64_tr_b16 a[46:47], v113 offset:2560\n"
"v_mfma_f32_16x16x32_bf16 v[92:95], v[32:35], v[152:155], v[92:95]\n"
"ds_read_b128 v[48:51], v66 offset:4096\n"
"v_mfma_f32_16x16x32_bf16 v[100:103], v[36:39], v[152:155], v[100:103]\n"
"buffer_load_dwordx4 v110, s[40:43], s72 offen offset:2048 lds\n"
"v_mfma_f32_16x16x32_bf16 v[88:91], v[40:43], v[148:151], v[88:91]\n"
"ds_read_b128 v[52:55], v66 offset:20480\n"
"v_mfma_f32_16x16x32_bf16 v[96:99], v[44:47], v[148:151], v[96:99]\n"
"ds_read_b128 v[56:59], v66 offset:5120\n"
"v_mfma_f32_16x16x32_bf16 v[92:95], v[40:43], v[156:159], v[92:95]\n"
"ds_read_b128 v[60:63], v66 offset:21504\n"
"v_mfma_f32_16x16x32_bf16 v[100:103], v[44:47], v[156:159], v[100:103]\n"
"buffer_load_dwordx4 v111, s[40:43], s72 offen offset:3072 lds\n"
"s_waitcnt vmcnt(9) lgkmcnt(0)\n"
"s_barrier\n"
"v_mfma_f32_16x16x32_bf16 v[0:3], a[32:35], v[48:51], v[0:3]\n"
"ds_read_b128 v[32:35], v66 offset:36864\n"
"v_mfma_f32_16x16x32_bf16 v[8:11], a[32:35], v[52:55], v[8:11]\n"
"ds_read_b128 v[36:39], v66 offset:53248\n"
"v_mfma_f32_16x16x32_bf16 v[4:7], a[40:43], v[48:51], v[4:7]\n"
"ds_read_b128 v[40:43], v66 offset:37888\n"
"v_mfma_f32_16x16x32_bf16 v[12:15], a[40:43], v[52:55], v[12:15]\n"
"ds_read_b128 v[44:47], v66 offset:54272\n"
"v_mfma_f32_16x16x32_bf16 v[0:3], a[36:39], v[56:59], v[0:3]\n"
"v_mfma_f32_16x16x32_bf16 v[8:11], a[36:39], v[60:63], v[8:11]\n"
"v_mfma_f32_16x16x32_bf16 v[4:7], a[44:47], v[56:59], v[4:7]\n"
"v_mfma_f32_16x16x32_bf16 v[12:15], a[44:47], v[60:63], v[12:15]\n"
"s_waitcnt lgkmcnt(0)\n"
"v_mfma_f32_16x16x32_bf16 v[72:75], a[160:163], v[32:35], v[72:75]\n"
"ds_read_b64_tr_b16 a[48:49], v112 offset:16384\n"
"v_mfma_f32_16x16x32_bf16 v[80:83], a[160:163], v[36:39], v[80:83]\n"
"ds_read_b64_tr_b16 a[50:51], v112 offset:16896\n"
"v_mfma_f32_16x16x32_bf16 v[76:79], a[168:171], v[32:35], v[76:79]\n"
"ds_read_b64_tr_b16 a[52:53], v112 offset:18432\n"
"v_mfma_f32_16x16x32_bf16 v[84:87], a[168:171], v[36:39], v[84:87]\n"
"s_add_u32 m0, s73, 65536\n"
"s_xor_b32 s72, s101, 3145728\n"
"buffer_load_dwordx4 v108, s[40:43], s72 offen offset:0 lds\n"
"v_mfma_f32_16x16x32_bf16 v[72:75], a[164:167], v[40:43], v[72:75]\n"
"ds_read_b64_tr_b16 a[54:55], v112 offset:18944\n"
"v_mfma_f32_16x16x32_bf16 v[80:83], a[164:167], v[44:47], v[80:83]\n"
"ds_read_b64_tr_b16 a[56:57], v113 offset:16384\n"
"v_mfma_f32_16x16x32_bf16 v[76:79], a[172:175], v[40:43], v[76:79]\n"
"ds_read_b64_tr_b16 a[58:59], v113 offset:16896\n"
"v_mfma_f32_16x16x32_bf16 v[84:87], a[172:175], v[44:47], v[84:87]\n"
"buffer_load_dwordx4 v109, s[40:43], s72 offen offset:1024 lds\n"
"v_mfma_f32_16x16x32_bf16 v[88:91], v[32:35], v[160:163], v[88:91]\n"
"ds_read_b64_tr_b16 a[60:61], v113 offset:18432\n"
"v_mfma_f32_16x16x32_bf16 v[96:99], v[36:39], v[160:163], v[96:99]\n"
"ds_read_b64_tr_b16 a[62:63], v113 offset:18944\n"
"v_mfma_f32_16x16x32_bf16 v[92:95], v[32:35], v[168:171], v[92:95]\n"
"ds_read_b128 v[48:51], v66 offset:6144\n"
"v_mfma_f32_16x16x32_bf16 v[100:103], v[36:39], v[168:171], v[100:103]\n"
"buffer_load_dwordx4 v110, s[40:43], s72 offen offset:2048 lds\n"
"v_mfma_f32_16x16x32_bf16 v[88:91], v[40:43], v[164:167], v[88:91]\n"
"ds_read_b128 v[52:55], v66 offset:22528\n"
"v_mfma_f32_16x16x32_bf16 v[96:99], v[44:47], v[164:167], v[96:99]\n"
"ds_read_b128 v[56:59], v66 offset:7168\n"
"v_mfma_f32_16x16x32_bf16 v[92:95], v[40:43], v[172:175], v[92:95]\n"
"ds_read_b128 v[60:63], v66 offset:23552\n"
"v_mfma_f32_16x16x32_bf16 v[100:103], v[44:47], v[172:175], v[100:103]\n"
"buffer_load_dwordx4 v111, s[40:43], s72 offen offset:3072 lds\n"
"s_getpc_b64 s[64:65]\n"
".Lwc73_%=:\n"
"s_add_u32 s64, s64, .Lql70_%=-.Lwc73_%=\n"
"s_addc_u32 s65, s65, 0\n"
"s_lshl_b32 s66, s37, 13\n"
"v_lshlrev_b32 v116, 7, v119\n"
"v_add_u32 v116, s66, v116\n"
"s_mov_b32 s66, .Lq14_%=-.Lql70_%=\n"
"v_min_u32 v116, s66, v116\n"
"global_load_dword v117, v116, s[64:65]\n"
"s_waitcnt vmcnt(10) lgkmcnt(0)\n"
"s_barrier\n"
"v_mfma_f32_16x16x32_bf16 v[0:3], a[48:51], v[48:51], v[0:3]\n"
"ds_read_b128 v[32:35], v66 offset:38912\n"
"v_mfma_f32_16x16x32_bf16 v[8:11], a[48:51], v[52:55], v[8:11]\n"
"ds_read_b128 v[36:39], v66 offset:55296\n"
"v_mfma_f32_16x16x32_bf16 v[4:7], a[56:59], v[48:51], v[4:7]\n"
"ds_read_b128 v[40:43], v66 offset:39936\n"
"v_mfma_f32_16x16x32_bf16 v[12:15], a[56:59], v[52:55], v[12:15]\n"
"ds_read_b128 v[44:47], v66 offset:56320\n"
"v_mfma_f32_16x16x32_bf16 v[0:3], a[52:55], v[56:59], v[0:3]\n"
"v_mfma_f32_16x16x32_bf16 v[8:11], a[52:55], v[60:63], v[8:11]\n"
"v_mfma_f32_16x16x32_bf16 v[4:7], a[60:63], v[56:59], v[4:7]\n"
"v_mfma_f32_16x16x32_bf16 v[12:15], a[60:63], v[60:63], v[12:15]\n"
"s_waitcnt lgkmcnt(0)\n"
"v_mfma_f32_16x16x32_bf16 v[72:75], a[176:179], v[32:35], v[72:75]\n"
"ds_read_b64_tr_b16 a[64:65], v112 offset:32768\n"
"v_mfma_f32_16x16x32_bf16 v[80:83], a[176:179], v[36:39], v[80:83]\n"
"ds_read_b64_tr_b16 a[66:67], v112 offset:33280\n"
"v_mfma_f32_16x16x32_bf16 v[76:79], a[184:187], v[32:35], v[76:79]\n"
"ds_read_b64_tr_b16 a[68:69], v112 offset:34816\n"
"v_mfma_f32_16x16x32_bf16 v[84:87], a[184:187], v[36:39], v[84:87]\n"
"s_add_u32 m0, s73, 81920\n"
"s_xor_b32 s72, s101, 3670016\n"
"buffer_load_dwordx4 v108, s[40:43], s72 offen offset:0 lds\n"
"v_mfma_f32_16x16x32_bf16 v[72:75], a[180:183], v[40:43], v[72:75]\n"
"ds_read_b64_tr_b16 a[70:71], v112 offset:35328\n"
"v_mfma_f32_16x16x32_bf16 v[80:83], a[180:183], v[44:47], v[80:83]\n"
"ds_read_b64_tr_b16 a[72:73], v113 offset:32768\n"
"v_mfma_f32_16x16x32_bf16 v[76:79], a[188:191], v[40:43], v[76:79]\n"
"ds_read_b64_tr_b16 a[74:75], v113 offset:33280\n"
"v_mfma_f32_16x16x32_bf16 v[84:87], a[188:191], v[44:47], v[84:87]\n"
"buffer_load_dwordx4 v109, s[40:43], s72 offen offset:1024 lds\n"
"v_mfma_f32_16x16x32_bf16 v[88:91], v[32:35], v[176:179], v[88:91]\n"
"ds_read_b64_tr_b16 a[76:77], v113 offset:34816\n"
"v_mfma_f32_16x16x32_bf16 v[96:99], v[36:39], v[176:179], v[96:99]\n"
"ds_read_b64_tr_b16 a[78:79], v113 offset:35328\n"
"v_mfma_f32_16x16x32_bf16 v[92:95], v[32:35], v[184:187], v[92:95]\n"
"ds_read_b128 v[48:51], v66 offset:8192\n"
"v_mfma_f32_16x16x32_bf16 v[100:103], v[36:39], v[184:187], v[100:103]\n"
"buffer_load_dwordx4 v110, s[40:43], s72 offen offset:2048 lds\n"
"v_mfma_f32_16x16x32_bf16 v[88:91], v[40:43], v[180:183], v[88:91]\n"
"ds_read_b128 v[52:55], v66 offset:24576\n"
"v_mfma_f32_16x16x32_bf16 v[96:99], v[44:47], v[180:183], v[96:99]\n"
"ds_read_b128 v[56:59], v66 offset:9216\n"
"v_mfma_f32_16x16x32_bf16 v[92:95], v[40:43], v[188:191], v[92:95]\n"
"ds_read_b128 v[60:63], v66 offset:25600\n"
"v_mfma_f32_16x16x32_bf16 v[100:103], v[44:47], v[188:191], v[100:103]\n"
"buffer_load_dwordx4 v111, s[40:43], s72 offen offset:3072 lds\n"
"s_waitcnt vmcnt(9) lgkmcnt(0)\n"
"s_barrier\n"
"v_mfma_f32_16x16x32_bf16 v[0:3], a[64:67], v[48:51], v[0:3]\n"
"ds_read_b128 v[32:35], v66 offset:40960\n"
"v_mfma_f32_16x16x32_bf16 v[8:11], a[64:67], v[52:55], v[8:11]\n"
"ds_read_b128 v[36:39], v66 offset:57344\n"
"v_mfma_f32_16x16x32_bf16 v[4:7], a[72:75], v[48:51], v[4:7]\n"
"ds_read_b128 v[40:43], v66 offset:41984\n"
"v_mfma_f32_16x16x32_bf16 v[12:15], a[72:75], v[52:55], v[12:15]\n"
"ds_read_b128 v[44:47], v66 offset:58368\n"
"v_mfma_f32_16x16x32_bf16 v[0:3], a[68:71], v[56:59], v[0:3]\n"
"v_mfma_f32_16x16x32_bf16 v[8:11], a[68:71], v[60:63], v[8:11]\n"
"v_mfma_f32_16x16x32_bf16 v[4:7], a[76:79], v[56:59], v[4:7]\n"
"v_mfma_f32_16x16x32_bf16 v[12:15], a[76:79], v[60:63], v[12:15]\n"
"s_waitcnt lgkmcnt(0)\n"
"v_mfma_f32_16x16x32_bf16 v[72:75], a[192:195], v[32:35], v[72:75]\n"
"ds_read_b64_tr_b16 a[80:81], v112 offset:49152\n"
"v_mfma_f32_16x16x32_bf16 v[80:83], a[192:195], v[36:39], v[80:83]\n"
"ds_read_b64_tr_b16 a[82:83], v112 offset:49664\n"
"v_mfma_f32_16x16x32_bf16 v[76:79], a[200:203], v[32:35], v[76:79]\n"
"ds_read_b64_tr_b16 a[84:85], v112 offset:51200\n"
"v_mfma_f32_16x16x32_bf16 v[84:87], a[200:203], v[36:39], v[84:87]\n"
"ds_read_b64_tr_b16 a[86:87], v112 offset:51712\n"
"v_mfma_f32_16x16x32_bf16 v[72:75], a[196:199], v[40:43], v[72:75]\n"
"ds_read_b64_tr_b16 a[88:89], v113 offset:49152\n"
"v_mfma_f32_16x16x32_bf16 v[80:83], a[196:199], v[44:47], v[80:83]\n"
"ds_read_b64_tr_b16 a[90:91], v113 offset:49664\n"
"v_mfma_f32_16x16x32_bf16 v[76:79], a[204:207], v[40:43], v[76:79]\n"
"ds_read_b64_tr_b16 a[92:93], v113 offset:51200\n"
"v_mfma_f32_16x16x32_bf16 v[84:87], a[204:207], v[44:47], v[84:87]\n"
"ds_read_b64_tr_b16 a[94:95], v113 offset:51712\n"
"v_mfma_f32_16x16x32_bf16 v[88:91], v[32:35], v[192:195], v[88:91]\n"
"ds_read_b128 v[48:51], v66 offset:10240\n"
"v_mfma_f32_16x16x32_bf16 v[96:99], v[36:39], v[192:195], v[96:99]\n"
"ds_read_b128 v[52:55], v66 offset:26624\n"
"v_mfma_f32_16x16x32_bf16 v[92:95], v[32:35], v[200:203], v[92:95]\n"
"ds_read_b128 v[56:59], v66 offset:11264\n"
"v_mfma_f32_16x16x32_bf16 v[100:103], v[36:39], v[200:203], v[100:103]\n"
"ds_read_b128 v[60:63], v66 offset:27648\n"
"v_mfma_f32_16x16x32_bf16 v[88:91], v[40:43], v[196:199], v[88:91]\n"
"v_mfma_f32_16x16x32_bf16 v[96:99], v[44:47], v[196:199], v[96:99]\n"
"v_mfma_f32_16x16x32_bf16 v[92:95], v[40:43], v[204:207], v[92:95]\n"
"v_mfma_f32_16x16x32_bf16 v[100:103], v[44:47], v[204:207], v[100:103]\n"
"s_waitcnt vmcnt(5) lgkmcnt(0)\n"
"s_barrier\n"
"v_mfma_f32_16x16x32_bf16 v[0:3], a[80:83], v[48:51], v[0:3]\n"
"ds_read_b128 v[32:35], v66 offset:43008\n"
"v_mfma_f32_16x16x32_bf16 v[8:11], a[80:83], v[52:55], v[8:11]\n"
"ds_read_b128 v[36:39], v66 offset:59392\n"
"v_mfma_f32_16x16x32_bf16 v[4:7], a[88:91], v[48:51], v[4:7]\n"
"ds_read_b128 v[40:43], v66 offset:44032\n"
"v_mfma_f32_16x16x32_bf16 v[12:15], a[88:91], v[52:55], v[12:15]\n"
"ds_read_b128 v[44:47], v66 offset:60416\n"
"v_mfma_f32_16x16x32_bf16 v[0:3], a[84:87], v[56:59], v[0:3]\n"
"v_mfma_f32_16x16x32_bf16 v[8:11], a[84:87], v[60:63], v[8:11]\n"
"v_mfma_f32_16x16x32_bf16 v[4:7], a[92:95], v[56:59], v[4:7]\n"
"v_mfma_f32_16x16x32_bf16 v[12:15], a[92:95], v[60:63], v[12:15]\n"
"s_waitcnt lgkmcnt(0)\n"
"v_mfma_f32_16x16x32_bf16 v[72:75], a[208:211], v[32:35], v[72:75]\n"
"ds_read_b64_tr_b16 a[96:97], v114 offset:0\n"
"v_mfma_f32_16x16x32_bf16 v[80:83], a[208:211], v[36:39], v[80:83]\n"
"ds_read_b64_tr_b16 a[98:99], v114 offset:512\n"
"v_mfma_f32_16x16x32_bf16 v[76:79], a[216:219], v[32:35], v[76:79]\n"
"ds_read_b64_tr_b16 a[100:101], v114 offset:2048\n"
"v_mfma_f32_16x16x32_bf16 v[84:87], a[216:219], v[36:39], v[84:87]\n"
"ds_read_b64_tr_b16 a[102:103], v114 offset:2560\n"
"v_mfma_f32_16x16x32_bf16 v[72:75], a[212:215], v[40:43], v[72:75]\n"
"ds_read_b64_tr_b16 a[104:105], v115 offset:0\n"
"v_mfma_f32_16x16x32_bf16 v[80:83], a[212:215], v[44:47], v[80:83]\n"
"ds_read_b64_tr_b16 a[106:107], v115 offset:512\n"
"v_mfma_f32_16x16x32_bf16 v[76:79], a[220:223], v[40:43], v[76:79]\n"
"ds_read_b64_tr_b16 a[108:109], v115 offset:2048\n"
"v_mfma_f32_16x16x32_bf16 v[84:87], a[220:223], v[44:47], v[84:87]\n"
"ds_read_b64_tr_b16 a[110:111], v115 offset:2560\n"
"v_mfma_f32_16x16x32_bf16 v[88:91], v[32:35], v[208:211], v[88:91]\n"
"ds_read_b128 v[48:51], v66 offset:12288\n"
"v_mfma_f32_16x16x32_bf16 v[96:99], v[36:39], v[208:211], v[96:99]\n"
"ds_read_b128 v[52:55], v66 offset:28672\n"
"v_mfma_f32_16x16x32_bf16 v[92:95], v[32:35], v[216:219], v[92:95]\n"
"ds_read_b128 v[56:59], v66 offset:13312\n"
"v_mfma_f32_16x16x32_bf16 v[100:103], v[36:39], v[216:219], v[100:103]\n"
"ds_read_b128 v[60:63], v66 offset:29696\n"
"v_mfma_f32_16x16x32_bf16 v[88:91], v[40:43], v[212:215], v[88:91]\n"
"v_mfma_f32_16x16x32_bf16 v[96:99], v[44:47], v[212:215], v[96:99]\n"
"v_mfma_f32_16x16x32_bf16 v[92:95], v[40:43], v[220:223], v[92:95]\n"
"v_mfma_f32_16x16x32_bf16 v[100:103], v[44:47], v[220:223], v[100:103]\n"
"s_waitcnt vmcnt(0) lgkmcnt(0)\n"
"s_barrier\n"
"v_mfma_f32_16x16x32_bf16 v[0:3], a[96:99], v[48:51], v[0:3]\n"
"ds_read_b128 v[32:35], v66 offset:45056\n"
"v_mfma_f32_16x16x32_bf16 v[8:11], a[96:99], v[52:55], v[8:11]\n"
"ds_read_b128 v[36:39], v66 offset:61440\n"
"v_mfma_f32_16x16x32_bf16 v[4:7], a[104:107], v[48:51], v[4:7]\n"
"ds_read_b128 v[40:43], v66 offset:46080\n"
"v_mfma_f32_16x16x32_bf16 v[12:15], a[104:107], v[52:55], v[12:15]\n"
"ds_read_b128 v[44:47], v66 offset:62464\n"
"v_mfma_f32_16x16x32_bf16 v[0:3], a[100:103], v[56:59], v[0:3]\n"
"v_mfma_f32_16x16x32_bf16 v[8:11], a[100:103], v[60:63], v[8:11]\n"
"v_mfma_f32_16x16x32_bf16 v[4:7], a[108:111], v[56:59], v[4:7]\n"
"v_mfma_f32_16x16x32_bf16 v[12:15], a[108:111], v[60:63], v[12:15]\n"
"s_waitcnt lgkmcnt(0)\n"
"v_mfma_f32_16x16x32_bf16 v[72:75], a[224:227], v[32:35], v[72:75]\n"
"ds_read_b64_tr_b16 a[112:113], v114 offset:16384\n"
"v_mfma_f32_16x16x32_bf16 v[80:83], a[224:227], v[36:39], v[80:83]\n"
"ds_read_b64_tr_b16 a[114:115], v114 offset:16896\n"
"v_mfma_f32_16x16x32_bf16 v[76:79], a[232:235], v[32:35], v[76:79]\n"
"ds_read_b64_tr_b16 a[116:117], v114 offset:18432\n"
"v_mfma_f32_16x16x32_bf16 v[84:87], a[232:235], v[36:39], v[84:87]\n"
"ds_read_b64_tr_b16 a[118:119], v114 offset:18944\n"
"v_mfma_f32_16x16x32_bf16 v[72:75], a[228:231], v[40:43], v[72:75]\n"
"ds_read_b64_tr_b16 a[120:121], v115 offset:16384\n"
"v_mfma_f32_16x16x32_bf16 v[80:83], a[228:231], v[44:47], v[80:83]\n"
"ds_read_b64_tr_b16 a[122:123], v115 offset:16896\n"
"v_mfma_f32_16x16x32_bf16 v[76:79], a[236:239], v[40:43], v[76:79]\n"
"ds_read_b64_tr_b16 a[124:125], v115 offset:18432\n"
"v_mfma_f32_16x16x32_bf16 v[84:87], a[236:239], v[44:47], v[84:87]\n"
"ds_read_b64_tr_b16 a[126:127], v115 offset:18944\n"
"v_mfma_f32_16x16x32_bf16 v[88:91], v[32:35], v[224:227], v[88:91]\n"
"ds_read_b128 v[48:51], v66 offset:14336\n"
"v_mfma_f32_16x16x32_bf16 v[96:99], v[36:39], v[224:227], v[96:99]\n"
"ds_read_b128 v[52:55], v66 offset:30720\n"
"v_mfma_f32_16x16x32_bf16 v[92:95], v[32:35], v[232:235], v[92:95]\n"
"ds_read_b128 v[56:59], v66 offset:15360\n"
"v_mfma_f32_16x16x32_bf16 v[100:103], v[36:39], v[232:235], v[100:103]\n"
"ds_read_b128 v[60:63], v66 offset:31744\n"
"v_mfma_f32_16x16x32_bf16 v[88:91], v[40:43], v[228:231], v[88:91]\n"
"v_mfma_f32_16x16x32_bf16 v[96:99], v[44:47], v[228:231], v[96:99]\n"
"v_mfma_f32_16x16x32_bf16 v[92:95], v[40:43], v[236:239], v[92:95]\n"
"v_mfma_f32_16x16x32_bf16 v[100:103], v[44:47], v[236:239], v[100:103]\n"
"s_waitcnt lgkmcnt(0)\n"
"s_barrier\n"
".Lql70_%=:\n"
"v_mfma_f32_16x16x32_bf16 v[0:3], a[112:115], v[48:51], v[0:3]\n"
"ds_read_b128 v[32:35], v66 offset:47104\n"
"v_mfma_f32_16x16x32_bf16 v[8:11], a[112:115], v[52:55], v[8:11]\n"
"ds_read_b128 v[36:39], v66 offset:63488\n"
"v_mfma_f32_16x16x32_bf16 v[4:7], a[120:123], v[48:51], v[4:7]\n"
"ds_read_b128 v[40:43], v66 offset:48128\n"
"v_mfma_f32_16x16x32_bf16 v[12:15], a[120:123], v[52:55], v[12:15]\n"
"ds_read_b128 v[44:47], v66 offset:64512\n"
"v_mfma_f32_16x16x32_bf16 v[0:3], a[116:119], v[56:59], v[0:3]\n"
"v_mfma_f32_16x16x32_bf16 v[8:11], a[116:119], v[60:63], v[8:11]\n"
"v_mfma_f32_16x16x32_bf16 v[4:7], a[124:127], v[56:59], v[4:7]\n"
"v_mfma_f32_16x16x32_bf16 v[12:15], a[124:127], v[60:63], v[12:15]\n"
"s_waitcnt lgkmcnt(0)\n"
"v_mfma_f32_16x16x32_bf16 v[72:75], a[240:243], v[32:35], v[72:75]\n"
"v_mfma_f32_16x16x32_bf16 v[80:83], a[240:243], v[36:39], v[80:83]\n"
"v_mfma_f32_16x16x32_bf16 v[76:79], a[248:251], v[32:35], v[76:79]\n"
"v_mfma_f32_16x16x32_bf16 v[84:87], a[248:251], v[36:39], v[84:87]\n"
"v_mfma_f32_16x16x32_bf16 v[72:75], a[244:247], v[40:43], v[72:75]\n"
"v_mfma_f32_16x16x32_bf16 v[80:83], a[244:247], v[44:47], v[80:83]\n"
"v_mfma_f32_16x16x32_bf16 v[76:79], a[252:255], v[40:43], v[76:79]\n"
"v_mfma_f32_16x16x32_bf16 v[84:87], a[252:255], v[44:47], v[84:87]\n"
"v_mfma_f32_16x16x32_bf16 v[88:91], v[32:35], v[240:243], v[88:91]\n"
"v_mfma_f32_16x16x32_bf16 v[96:99], v[36:39], v[240:243], v[96:99]\n"
"v_mfma_f32_16x16x32_bf16 v[92:95], v[32:35], v[248:251], v[92:95]\n"
"v_mfma_f32_16x16x32_bf16 v[100:103], v[36:39], v[248:251], v[100:103]\n"
"v_mfma_f32_16x16x32_bf16 v[88:91], v[40:43], v[244:247], v[88:91]\n"
"v_mfma_f32_16x16x32_bf16 v[96:99], v[44:47], v[244:247], v[96:99]\n"
"v_mfma_f32_16x16x32_bf16 v[92:95], v[40:43], v[252:255], v[92:95]\n"
"v_mfma_f32_16x16x32_bf16 v[100:103], v[44:47], v[252:255], v[100:103]\n"
"s_add_u32 s22, s22, 2\n"
"s_add_u32 s27, s22, 1\n"
"s_and_b32 s64, s22, 7\n"
"s_lshl_b32 s65, s22, 12\n"
"s_xor_b32 s65, s65, s25\n"
"s_and_b32 s65, s65, 8192\n"
"s_lshl_b32 s64, s64, 20\n"
"s_add_u32 s64, s64, s65\n"
"s_add_u32 s64, s64, s10\n"
"s_add_u32 s65, s64, 16384\n"
"v_cvt_pk_bf16_f32 v72, v72, v76\n"
"v_cvt_pk_bf16_f32 v73, v73, v77\n"
"v_cvt_pk_bf16_f32 v74, v74, v78\n"
"v_cvt_pk_bf16_f32 v75, v75, v79\n"
"buffer_store_dwordx4 v[72:75], v69, s[60:63], s64 offen offset:0 sc1\n"
"v_cvt_pk_bf16_f32 v76, v80, v84\n"
"v_cvt_pk_bf16_f32 v77, v81, v85\n"
"v_cvt_pk_bf16_f32 v78, v82, v86\n"
"v_cvt_pk_bf16_f32 v79, v83, v87\n"
"buffer_store_dwordx4 v[76:79], v69, s[60:63], s64 offen offset:1024 sc1\n"
"v_cvt_pk_bf16_f32 v80, v88, v89\n"
"v_cvt_pk_bf16_f32 v81, v96, v97\n"
"buffer_store_dwordx2 v[80:81], v104, s[60:63], s65 offen offset:0 sc1\n"
"v_cvt_pk_bf16_f32 v82, v90, v91\n"
"v_cvt_pk_bf16_f32 v83, v98, v99\n"
"buffer_store_dwordx2 v[82:83], v104, s[60:63], s65 offen offset:512 sc1\n"
"v_cvt_pk_bf16_f32 v84, v92, v93\n"
"v_cvt_pk_bf16_f32 v85, v100, v101\n"
"buffer_store_dwordx2 v[84:85], v104, s[60:63], s65 offen offset:1024 sc1\n"
"v_cvt_pk_bf16_f32 v86, v94, v95\n"
"v_cvt_pk_bf16_f32 v87, v102, v103\n"
"buffer_store_dwordx2 v[86:87], v104, s[60:63], s65 offen offset:1536 sc1\n"
"ds_write_b128 v67, v[16:19] offset:8192\n"
"ds_write_b128 v67, v[20:23] offset:9216\n"
"ds_write_b128 v67, v[24:27] offset:16384\n"
"ds_write_b128 v67, v[28:31] offset:17408\n"
"v_cvt_pk_bf16_f32 v0, v0, v4\n"
"v_cvt_pk_bf16_f32 v1, v1, v5\n"
"v_cvt_pk_bf16_f32 v2, v2, v6\n"
"v_cvt_pk_bf16_f32 v3, v3, v7\n"
"ds_write_b128 v67, v[0:3] offset:0\n"
"v_cvt_pk_bf16_f32 v4, v8, v12\n"
"v_cvt_pk_bf16_f32 v5, v9, v13\n"
"v_cvt_pk_bf16_f32 v6, v10, v14\n"
"v_cvt_pk_bf16_f32 v7, v11, v15\n"
"ds_write_b128 v67, v[4:7] offset:1024\n"
"s_add_u32 s28, s28, 1\n"
"s_cmp_eq_u32 s28, 3\n"
"s_cselect_b32 s28, 0, s28\n"
"s_cselect_b32 s64, 4294893568, 0\n"
"s_add_u32 s64, s64, 24576\n"
"v_add_u32 v67, s64, v67\n"
"v_xor_b32 v66, 32768, v66\n"
"s_add_u32 s23, s22, 2\n"
"s_mov_b32 s9, 0\n"
"ds_read_b128 v[48:51], v66 offset:0\n"
"ds_read_b128 v[52:55], v66 offset:16384\n"
"s_waitcnt lgkmcnt(0)\n"
"v_mfma_f32_16x16x32_bf16 v[32:35], a[0:3], v[48:51], 0\n"
"ds_read_b128 v[56:59], v66 offset:1024\n"
"v_mfma_f32_16x16x32_bf16 v[40:43], a[0:3], v[52:55], 0\n"
"ds_read_b128 v[60:63], v66 offset:17408\n"
"v_mfma_f32_16x16x32_bf16 v[36:39], a[8:11], v[48:51], 0\n"
"s_lshl_b32 s71, s23, 15\n"
"s_lshl_b32 s65, s37, 13\n"
"s_add_u32 s71, s71, s65\n"
"s_add_u32 s71, s71, 4194304\n"
"s_add_u32 m0, s65, s9\n"
"s_xor_b32 s71, s71, s100\n"
"v_mfma_f32_16x16x32_bf16 v[44:47], a[8:11], v[52:55], 0\n"
"s_waitcnt lgkmcnt(0)\n"
"buffer_load_dwordx4 v64, s[60:63], s71 offen offset:0 sc1 lds\n"
"v_mfma_f32_16x16x32_bf16 v[32:35], a[4:7], v[56:59], v[32:35]\n"
"ds_read_b128 v[48:51], v66 offset:2048\n"
"v_mfma_f32_16x16x32_bf16 v[40:43], a[4:7], v[60:63], v[40:43]\n"
"ds_read_b128 v[52:55], v66 offset:18432\n"
"v_mfma_f32_16x16x32_bf16 v[36:39], a[12:15], v[56:59], v[36:39]\n"
"buffer_load_dwordx4 v64, s[60:63], s71 offen offset:1024 sc1 lds\n"
"v_mfma_f32_16x16x32_bf16 v[44:47], a[12:15], v[60:63], v[44:47]\n"
"s_waitcnt lgkmcnt(0)\n"
"buffer_load_dwordx4 v64, s[60:63], s71 offen offset:2048 sc1 lds\n"
"v_mfma_f32_16x16x32_bf16 v[32:35], a[16:19], v[48:51], v[32:35]\n"
"ds_read_b128 v[56:59], v66 offset:3072\n"
"v_mfma_f32_16x16x32_bf16 v[40:43], a[16:19], v[52:55], v[40:43]\n"
"ds_read_b128 v[60:63], v66 offset:19456\n"
"v_mfma_f32_16x16x32_bf16 v[36:39], a[24:27], v[48:51], v[36:39]\n"
"buffer_load_dwordx4 v64, s[60:63], s71 offen offset:3072 sc1 lds\n"
"v_mfma_f32_16x16x32_bf16 v[44:47], a[24:27], v[52:55], v[44:47]\n"
"s_waitcnt lgkmcnt(0)\n"
"s_add_u32 m0, m0, 4096\n"
"s_add_u32 s71, s71, 4096\n"
"buffer_load_dwordx4 v64, s[60:63], s71 offen offset:0 sc1 lds\n"
"v_mfma_f32_16x16x32_bf16 v[32:35], a[20:23], v[56:59], v[32:35]\n"
"ds_read_b128 v[48:51], v66 offset:4096\n"
"v_mfma_f32_16x16x32_bf16 v[40:43], a[20:23], v[60:63], v[40:43]\n"
"ds_read_b128 v[52:55], v66 offset:20480\n"
"v_mfma_f32_16x16x32_bf16 v[36:39], a[28:31], v[56:59], v[36:39]\n"
"buffer_load_dwordx4 v64, s[60:63], s71 offen offset:1024 sc1 lds\n"
"v_mfma_f32_16x16x32_bf16 v[44:47], a[28:31], v[60:63], v[44:47]\n"
"s_waitcnt lgkmcnt(0)\n"
"buffer_load_dwordx4 v64, s[60:63], s71 offen offset:2048 sc1 lds\n"
"v_mfma_f32_16x16x32_bf16 v[32:35], a[32:35], v[48:51], v[32:35]\n"
"ds_read_b128 v[56:59], v66 offset:5120\n"
"v_mfma_f32_16x16x32_bf16 v[40:43], a[32:35], v[52:55], v[40:43]\n"
"ds_read_b128 v[60:63], v66 offset:21504\n"
"v_mfma_f32_16x16x32_bf16 v[36:39], a[40:43], v[48:51], v[36:39]\n"
"buffer_load_dwordx4 v64, s[60:63], s71 offen offset:3072 sc1 lds\n"
"v_mfma_f32_16x16x32_bf16 v[44:47], a[40:43], v[52:55], v[44:47]\n"
"s_waitcnt lgkmcnt(0)\n"
"v_mfma_f32_16x16x32_bf16 v[32:35], a[36:39], v[56:59], v[32:35]\n"
"ds_read_b128 v[48:51], v66 offset:6144\n"
"v_mfma_f32_16x16x32_bf16 v[40:43], a[36:39], v[60:63], v[40:43]\n"
"ds_read_b128 v[52:55], v66 offset:22528\n"
"v_mfma_f32_16x16x32_bf16 v[36:39], a[44:47], v[56:59], v[36:39]\n"
"v_mfma_f32_16x16x32_bf16 v[44:47], a[44:47], v[60:63], v[44:47]\n"
"s_waitcnt lgkmcnt(0)\n"
"v_mfma_f32_16x16x32_bf16 v[32:35], a[48:51], v[48:51], v[32:35]\n"
"ds_read_b128 v[56:59], v66 offset:7168\n"
"v_mfma_f32_16x16x32_bf16 v[40:43], a[48:51], v[52:55], v[40:43]\n"
"ds_read_b128 v[60:63], v66 offset:23552\n"
"v_mfma_f32_16x16x32_bf16 v[36:39], a[56:59], v[48:51], v[36:39]\n"
"v_mfma_f32_16x16x32_bf16 v[44:47], a[56:59], v[52:55], v[44:47]\n"
"s_waitcnt lgkmcnt(0)\n"
"v_mfma_f32_16x16x32_bf16 v[32:35], a[52:55], v[56:59], v[32:35]\n"
"ds_read_b128 v[48:51], v66 offset:8192\n"
"v_mfma_f32_16x16x32_bf16 v[40:43], a[52:55], v[60:63], v[40:43]\n"
"ds_read_b128 v[52:55], v66 offset:24576\n"
"v_mfma_f32_16x16x32_bf16 v[36:39], a[60:63], v[56:59], v[36:39]\n"
"v_mfma_f32_16x16x32_bf16 v[44:47], a[60:63], v[60:63], v[44:47]\n"
"s_waitcnt lgkmcnt(0)\n"
"v_mfma_f32_16x16x32_bf16 v[32:35], a[64:67], v[48:51], v[32:35]\n"
"ds_read_b128 v[56:59], v66 offset:9216\n"
"v_mfma_f32_16x16x32_bf16 v[40:43], a[64:67], v[52:55], v[40:43]\n"
"ds_read_b128 v[60:63], v66 offset:25600\n"
"v_mfma_f32_16x16x32_bf16 v[36:39], a[72:75], v[48:51], v[36:39]\n"
"v_mfma_f32_16x16x32_bf16 v[44:47], a[72:75], v[52:55], v[44:47]\n"
"s_waitcnt lgkmcnt(0)\n"
"s_waitcnt vmcnt(8)\n"
"v_mov_b32 v120, s34\n"
"s_add_u32 s64, s35, s27\n"
"v_mov_b32 v121, s64\n"
"s_mov_b64 exec, 1\n"
"buffer_store_dwordx2 v[120:121], off, s[60:63], s21 sc1\n"
"s_mov_b64 exec, -1\n"
"v_mfma_f32_16x16x32_bf16 v[32:35], a[68:71], v[56:59], v[32:35]\n"
"ds_read_b128 v[48:51], v66 offset:10240\n"
"v_mfma_f32_16x16x32_bf16 v[40:43], a[68:71], v[60:63], v[40:43]\n"
"ds_read_b128 v[52:55], v66 offset:26624\n"
"v_mfma_f32_16x16x32_bf16 v[36:39], a[76:79], v[56:59], v[36:39]\n"
"v_mfma_f32_16x16x32_bf16 v[44:47], a[76:79], v[60:63], v[44:47]\n"
"s_waitcnt lgkmcnt(0)\n"
"v_mfma_f32_16x16x32_bf16 v[32:35], a[80:83], v[48:51], v[32:35]\n"
"ds_read_b128 v[56:59], v66 offset:11264\n"
"v_mfma_f32_16x16x32_bf16 v[40:43], a[80:83], v[52:55], v[40:43]\n"
"ds_read_b128 v[60:63], v66 offset:27648\n"
"v_mfma_f32_16x16x32_bf16 v[36:39], a[88:91], v[48:51], v[36:39]\n"
"v_mfma_f32_16x16x32_bf16 v[44:47], a[88:91], v[52:55], v[44:47]\n"
"s_waitcnt lgkmcnt(0)\n"
"v_mfma_f32_16x16x32_bf16 v[32:35], a[84:87], v[56:59], v[32:35]\n"
"ds_read_b128 v[48:51], v66 offset:12288\n"
"v_mfma_f32_16x16x32_bf16 v[40:43], a[84:87], v[60:63], v[40:43]\n"
"ds_read_b128 v[52:55], v66 offset:28672\n"
"v_mfma_f32_16x16x32_bf16 v[36:39], a[92:95], v[56:59], v[36:39]\n"
"v_mfma_f32_16x16x32_bf16 v[44:47], a[92:95], v[60:63], v[44:47]\n"
"s_waitcnt lgkmcnt(0)\n"
"v_mfma_f32_16x16x32_bf16 v[32:35], a[96:99], v[48:51], v[32:35]\n"
"ds_read_b128 v[56:59], v66 offset:13312\n"
"v_mfma_f32_16x16x32_bf16 v[40:43], a[96:99], v[52:55], v[40:43]\n"
"ds_read_b128 v[60:63], v66 offset:29696\n"
"v_mfma_f32_16x16x32_bf16 v[36:39], a[104:107], v[48:51], v[36:39]\n"
"v_mfma_f32_16x16x32_bf16 v[44:47], a[104:107], v[52:55], v[44:47]\n"
"s_waitcnt lgkmcnt(0)\n"
"v_mfma_f32_16x16x32_bf16 v[32:35], a[100:103], v[56:59], v[32:35]\n"
"ds_read_b128 v[48:51], v66 offset:14336\n"
"v_mfma_f32_16x16x32_bf16 v[40:43], a[100:103], v[60:63], v[40:43]\n"
"ds_read_b128 v[52:55], v66 offset:30720\n"
"v_mfma_f32_16x16x32_bf16 v[36:39], a[108:111], v[56:59], v[36:39]\n"
"v_mfma_f32_16x16x32_bf16 v[44:47], a[108:111], v[60:63], v[44:47]\n"
"s_waitcnt lgkmcnt(0)\n"
"v_mfma_f32_16x16x32_bf16 v[32:35], a[112:115], v[48:51], v[32:35]\n"
"ds_read_b128 v[56:59], v66 offset:15360\n"
"v_mfma_f32_16x16x32_bf16 v[40:43], a[112:115], v[52:55], v[40:43]\n"
"ds_read_b128 v[60:63], v66 offset:31744\n"
"v_mfma_f32_16x16x32_bf16 v[36:39], a[120:123], v[48:51], v[36:39]\n"
"v_mfma_f32_16x16x32_bf16 v[44:47], a[120:123], v[52:55], v[44:47]\n"
"s_waitcnt lgkmcnt(0)\n"
"v_mfma_f32_16x16x32_bf16 v[32:35], a[116:119], v[56:59], v[32:35]\n"
"v_mfma_f32_16x16x32_bf16 v[40:43], a[116:119], v[60:63], v[40:43]\n"
"v_mfma_f32_16x16x32_bf16 v[36:39], a[124:127], v[56:59], v[36:39]\n"
"v_mfma_f32_16x16x32_bf16 v[44:47], a[124:127], v[60:63], v[44:47]\n"
"ds_write_b128 v67, v[72:75] offset:8192\n"
"ds_write_b128 v67, v[76:79] offset:9216\n"
"ds_write_b128 v67, v[80:83] offset:16384\n"
"ds_write_b128 v67, v[84:87] offset:17408\n"
"s_nop 0\n"
"v_cvt_pk_bf16_f32 v32, v32, v36\n"
"v_cvt_pk_bf16_f32 v33, v33, v37\n"
"v_cvt_pk_bf16_f32 v34, v34, v38\n"
"v_cvt_pk_bf16_f32 v35, v35, v39\n"
"ds_write_b128 v67, v[32:35] offset:0\n"
"v_cvt_pk_bf16_f32 v36, v40, v44\n"
"v_cvt_pk_bf16_f32 v37, v41, v45\n"
"v_cvt_pk_bf16_f32 v38, v42, v46\n"
"v_cvt_pk_bf16_f32 v39, v43, v47\n"
"ds_write_b128 v67, v[36:39] offset:1024\n"
"s_add_u32 s28, s28, 1\n"
"s_cmp_eq_u32 s28, 3\n"
"s_cselect_b32 s28, 0, s28\n"
"s_cselect_b32 s64, 4294893568, 0\n"
"s_add_u32 s64, s64, 24576\n"
"v_add_u32 v67, s64, v67\n"
"v_xor_b32 v66, 32768, v66\n"
"s_mov_b32 s66, 0x6000000\n"
"s_waitcnt vmcnt(0) lgkmcnt(0)\n"
".Lqent78_%=:\n"
"s_barrier\n"
"ds_read_b128 v[48:51], v66 offset:0\n"
"ds_read_b128 v[52:55], v66 offset:16384\n"
"s_branch .Lqnxt76_%=\n"
".Lqtop74_%=:\n"
"s_add_u32 s22, s8, 4\n"
"s_and_b32 s68, s8, 15\n"
"s_lshl_b32 s68, s68, 16\n"
"s_add_u32 s68, s68, s25\n"
"s_xor_b32 s69, s68, 32768\n"
"s_mov_b32 s11, 0\n"
"s_cmp_ge_u32 s22, 64\n"
"s_cbranch_scc1 .Lqlast77_%=\n"
"s_add_u32 s23, s22, 2\n"
"s_min_u32 s23, s23, 63\n"
"s_xor_b32 s9, s9, 32768\n"
"s_waitcnt lgkmcnt(0)\n"
"v_mfma_f32_16x16x32_bf16 v[16:19], a[128:131], v[48:51], 0\n"
"ds_read_b128 v[56:59], v66 offset:1024\n"
"v_mfma_f32_16x16x32_bf16 v[24:27], a[128:131], v[52:55], 0\n"
"ds_read_b128 v[60:63], v66 offset:17408\n"
"v_mfma_f32_16x16x32_bf16 v[20:23], a[136:139], v[48:51], 0\n"
"v_rcp_f32 v76, v76\n"
"v_mfma_f32_16x16x32_bf16 v[28:31], a[136:139], v[52:55], 0\n"
"v_mul_f32 v104, v104, v76\n"
"v_mul_f32 v105, v105, v76\n"
"v_mfma_f32_16x16x32_bf16 v[32:35], v[48:51], v[128:131], 0\n"
"v_mul_f32 v106, v106, v76\n"
"v_mul_f32 v107, v107, v76\n"
"v_mfma_f32_16x16x32_bf16 v[40:43], v[52:55], v[128:131], 0\n"
"v_mul_f32 v108, v108, v76\n"
"v_mul_f32 v109, v109, v76\n"
"v_mfma_f32_16x16x32_bf16 v[36:39], v[48:51], v[136:139], 0\n"
"v_mul_f32 v110, v110, v76\n"
"v_mul_f32 v111, v111, v76\n"
"v_mfma_f32_16x16x32_bf16 v[44:47], v[52:55], v[136:139], 0\n"
"v_cvt_pk_bf16_f32 v88, v104, v105\n"
"v_cvt_pk_bf16_f32 v89, v106, v107\n"
"v_mfma_f32_16x16x32_bf16 v[0:3], a[0:3], v[48:51], 0\n"
"v_cvt_pk_bf16_f32 v90, v108, v109\n"
"v_cvt_pk_bf16_f32 v91, v110, v111\n"
"v_mfma_f32_16x16x32_bf16 v[8:11], a[0:3], v[52:55], 0\n"
"buffer_store_dwordx4 v[88:91], v64, s[60:63], s66 offen offset:0 sc1\n"
"v_mfma_f32_16x16x32_bf16 v[4:7], a[8:11], v[48:51], 0\n"
"v_mul_f32 v112, v112, v76\n"
"v_mul_f32 v113, v113, v76\n"
"v_mfma_f32_16x16x32_bf16 v[12:15], a[8:11], v[52:55], 0\n"
"s_waitcnt lgkmcnt(0)\n"
"v_mul_f32 v114, v114, v76\n"
"v_mul_f32 v115, v115, v76\n"
"v_mfma_f32_16x16x32_bf16 v[16:19], a[132:135], v[56:59], v[16:19]\n"
"ds_read_b128 v[48:51], v66 offset:2048\n"
"v_mfma_f32_16x16x32_bf16 v[24:27], a[132:135], v[60:63], v[24:27]\n"
"ds_read_b128 v[52:55], v66 offset:18432\n"
"v_mfma_f32_16x16x32_bf16 v[20:23], a[140:143], v[56:59], v[20:23]\n"
"v_mul_f32 v116, v116, v76\n"
"v_mul_f32 v117, v117, v76\n"
"v_mfma_f32_16x16x32_bf16 v[28:31], a[140:143], v[60:63], v[28:31]\n"
"v_mul_f32 v118, v118, v76\n"
"v_mul_f32 v119, v119, v76\n"
"v_mfma_f32_16x16x32_bf16 v[32:35], v[56:59], v[132:135], v[32:35]\n"
"v_cvt_pk_bf16_f32 v92, v112, v113\n"
"v_cvt_pk_bf16_f32 v93, v114, v115\n"
"v_mfma_f32_16x16x32_bf16 v[40:43], v[60:63], v[132:135], v[40:43]\n"
"v_cvt_pk_bf16_f32 v94, v116, v117\n"
"v_cvt_pk_bf16_f32 v95, v118, v119\n"
"v_mfma_f32_16x16x32_bf16 v[36:39], v[56:59], v[140:143], v[36:39]\n"
"buffer_store_dwordx4 v[92:95], v64, s[60:63], s66 offen offset:1024 sc1\n"
"v_mfma_f32_16x16x32_bf16 v[44:47], v[60:63], v[140:143], v[44:47]\n"
"s_lshl_b32 s71, s23, 15\n"
"s_lshl_b32 s65, s37, 13\n"
"s_add_u32 s71, s71, s65\n"
"s_add_u32 s71, s71, 4194304\n"
"s_add_u32 m0, s65, s9\n"
"s_xor_b32 s71, s71, s100\n"
"v_mfma_f32_16x16x32_bf16 v[0:3], a[4:7], v[56:59], v[0:3]\n"
"v_mfma_f32_16x16x32_bf16 v[8:11], a[4:7], v[60:63], v[8:11]\n"
"v_mfma_f32_16x16x32_bf16 v[4:7], a[12:15], v[56:59], v[4:7]\n"
"v_mfma_f32_16x16x32_bf16 v[12:15], a[12:15], v[60:63], v[12:15]\n"
"s_waitcnt lgkmcnt(0)\n"
"s_barrier\n"
"v_mfma_f32_16x16x32_bf16 v[16:19], a[144:147], v[48:51], v[16:19]\n"
"ds_read_b128 v[56:59], v66 offset:3072\n"
"v_mfma_f32_16x16x32_bf16 v[24:27], a[144:147], v[52:55], v[24:27]\n"
"ds_read_b128 v[60:63], v66 offset:19456\n"
"v_mfma_f32_16x16x32_bf16 v[20:23], a[152:155], v[48:51], v[20:23]\n"
"buffer_load_dwordx4 v64, s[60:63], s71 offen offset:0 sc1 lds\n"
"v_mfma_f32_16x16x32_bf16 v[28:31], a[152:155], v[52:55], v[28:31]\n"
"v_mfma_f32_16x16x32_bf16 v[32:35], v[48:51], v[144:147], v[32:35]\n"
"v_mfma_f32_16x16x32_bf16 v[40:43], v[52:55], v[144:147], v[40:43]\n"
"v_mfma_f32_16x16x32_bf16 v[36:39], v[48:51], v[152:155], v[36:39]\n"
"v_mfma_f32_16x16x32_bf16 v[44:47], v[52:55], v[152:155], v[44:47]\n"
"buffer_load_dwordx4 v64, s[60:63], s71 offen offset:1024 sc1 lds\n"
"v_mfma_f32_16x16x32_bf16 v[0:3], a[16:19], v[48:51], v[0:3]\n"
"v_mfma_f32_16x16x32_bf16 v[8:11], a[16:19], v[52:55], v[8:11]\n"
"v_mfma_f32_16x16x32_bf16 v[4:7], a[24:27], v[48:51], v[4:7]\n"
"v_mfma_f32_16x16x32_bf16 v[12:15], a[24:27], v[52:55], v[12:15]\n"
"s_waitcnt lgkmcnt(0)\n"
"v_mfma_f32_16x16x32_bf16 v[16:19], a[148:151], v[56:59], v[16:19]\n"
"ds_read_b128 v[48:51], v66 offset:4096\n"
"v_mfma_f32_16x16x32_bf16 v[24:27], a[148:151], v[60:63], v[24:27]\n"
"ds_read_b128 v[52:55], v66 offset:20480\n"
"v_mfma_f32_16x16x32_bf16 v[20:23], a[156:159], v[56:59], v[20:23]\n"
"buffer_load_dwordx4 v64, s[60:63], s71 offen offset:2048 sc1 lds\n"
"v_mfma_f32_16x16x32_bf16 v[28:31], a[156:159], v[60:63], v[28:31]\n"
"v_mfma_f32_16x16x32_bf16 v[32:35], v[56:59], v[148:151], v[32:35]\n"
"v_mfma_f32_16x16x32_bf16 v[40:43], v[60:63], v[148:151], v[40:43]\n"
"v_mfma_f32_16x16x32_bf16 v[36:39], v[56:59], v[156:159], v[36:39]\n"
"v_mfma_f32_16x16x32_bf16 v[44:47], v[60:63], v[156:159], v[44:47]\n"
"buffer_load_dwordx4 v64, s[60:63], s71 offen offset:3072 sc1 lds\n"
"v_mfma_f32_16x16x32_bf16 v[0:3], a[20:23], v[56:59], v[0:3]\n"
"v_mfma_f32_16x16x32_bf16 v[8:11], a[20:23], v[60:63], v[8:11]\n"
"v_mfma_f32_16x16x32_bf16 v[4:7], a[28:31], v[56:59], v[4:7]\n"
"v_mfma_f32_16x16x32_bf16 v[12:15], a[28:31], v[60:63], v[12:15]\n"
"s_waitcnt lgkmcnt(0)\n"
"v_mfma_f32_16x16x32_bf16 v[16:19], a[160:163], v[48:51], v[16:19]\n"
"ds_read_b128 v[56:59], v66 offset:5120\n"
"v_mfma_f32_16x16x32_bf16 v[24:27], a[160:163], v[52:55], v[24:27]\n"
"ds_read_b128 v[60:63], v66 offset:21504\n"
"v_mfma_f32_16x16x32_bf16 v[20:23], a[168:171], v[48:51], v[20:23]\n"
"s_add_u32 m0, m0, 4096\n"
"s_add_u32 s71, s71, 4096\n"
"buffer_load_dwordx4 v64, s[60:63], s71 offen offset:0 sc1 lds\n"
"v_mfma_f32_16x16x32_bf16 v[28:31], a[168:171], v[52:55], v[28:31]\n"
"v_mfma_f32_16x16x32_bf16 v[32:35], v[48:51], v[160:163], v[32:35]\n"
"v_mfma_f32_16x16x32_bf16 v[40:43], v[52:55], v[160:163], v[40:43]\n"
"v_mfma_f32_16x16x32_bf16 v[36:39], v[48:51], v[168:171], v[36:39]\n"
"v_mfma_f32_16x16x32_bf16 v[44:47], v[52:55], v[168:171], v[44:47]\n"
"buffer_load_dwordx4 v64, s[60:63], s71 offen offset:1024 sc1 lds\n"
"v_mfma_f32_16x16x32_bf16 v[0:3], a[32:35], v[48:51], v[0:3]\n"
"v_mfma_f32_16x16x32_bf16 v[8:11], a[32:35], v[52:55], v[8:11]\n"
"v_mfma_f32_16x16x32_bf16 v[4:7], a[40:43], v[48:51], v[4:7]\n"
"v_mfma_f32_16x16x32_bf16 v[12:15], a[40:43], v[52:55], v[12:15]\n"
"s_waitcnt lgkmcnt(0)\n"
"v_mfma_f32_16x16x32_bf16 v[16:19], a[164:167], v[56:59], v[16:19]\n"
"ds_read_b128 v[48:51], v66 offset:6144\n"
"v_mfma_f32_16x16x32_bf16 v[24:27], a[164:167], v[60:63], v[24:27]\n"
"ds_read_b128 v[52:55], v66 offset:22528\n"
"v_mfma_f32_16x16x32_bf16 v[20:23], a[172:175], v[56:59], v[20:23]\n"
"buffer_load_dwordx4 v64, s[60:63], s71 offen offset:2048 sc1 lds\n"
"v_mfma_f32_16x16x32_bf16 v[28:31], a[172:175], v[60:63], v[28:31]\n"
"v_mfma_f32_16x16x32_bf16 v[32:35], v[56:59], v[164:167], v[32:35]\n"
"v_mfma_f32_16x16x32_bf16 v[40:43], v[60:63], v[164:167], v[40:43]\n"
"v_mfma_f32_16x16x32_bf16 v[36:39], v[56:59], v[172:175], v[36:39]\n"
"v_mfma_f32_16x16x32_bf16 v[44:47], v[60:63], v[172:175], v[44:47]\n"
"buffer_load_dwordx4 v64, s[60:63], s71 offen offset:3072 sc1 lds\n"
"v_mfma_f32_16x16x32_bf16 v[0:3], a[36:39], v[56:59], v[0:3]\n"
"v_mfma_f32_16x16x32_bf16 v[8:11], a[36:39], v[60:63], v[8:11]\n"
"v_mfma_f32_16x16x32_bf16 v[4:7], a[44:47], v[56:59], v[4:7]\n"
"v_mfma_f32_16x16x32_bf16 v[12:15], a[44:47], v[60:63], v[12:15]\n"
"s_waitcnt lgkmcnt(0)\n"
"s_waitcnt vmcnt(8)\n"
"buffer_load_dword v121, off, s[60:63], s24 sc1\n"
"v_mov_b32 v122, s34\n"
"v_mov_b32 v123, s35\n"
"s_mov_b64 exec, 1\n"
"buffer_store_dwordx2 v[122:123], off, s[60:63], s26 sc1\n"
"s_mov_b64 exec, -1\n"
"v_mfma_f32_16x16x32_bf16 v[16:19], a[176:179], v[48:51], v[16:19]\n"
"ds_read_b128 v[56:59], v66 offset:7168\n"
"v_mfma_f32_16x16x32_bf16 v[24:27], a[176:179], v[52:55], v[24:27]\n"
"ds_read_b128 v[60:63], v66 offset:23552\n"
"v_mfma_f32_16x16x32_bf16 v[20:23], a[184:187], v[48:51], v[20:23]\n"
"v_mfma_f32_16x16x32_bf16 v[28:31], a[184:187], v[52:55], v[28:31]\n"
"v_mfma_f32_16x16x32_bf16 v[32:35], v[48:51], v[176:179], v[32:35]\n"
"v_mfma_f32_16x16x32_bf16 v[40:43], v[52:55], v[176:179], v[40:43]\n"
"v_mfma_f32_16x16x32_bf16 v[36:39], v[48:51], v[184:187], v[36:39]\n"
"v_mfma_f32_16x16x32_bf16 v[44:47], v[52:55], v[184:187], v[44:47]\n"
"v_mfma_f32_16x16x32_bf16 v[0:3], a[48:51], v[48:51], v[0:3]\n"
"v_mfma_f32_16x16x32_bf16 v[8:11], a[48:51], v[52:55], v[8:11]\n"
"v_mfma_f32_16x16x32_bf16 v[4:7], a[56:59], v[48:51], v[4:7]\n"
"v_mfma_f32_16x16x32_bf16 v[12:15], a[56:59], v[52:55], v[12:15]\n"
"s_waitcnt lgkmcnt(0)\n"
"v_mfma_f32_16x16x32_bf16 v[16:19], a[180:183], v[56:59], v[16:19]\n"
"ds_read_b128 v[48:51], v66 offset:8192\n"
"v_mfma_f32_16x16x32_bf16 v[24:27], a[180:183], v[60:63], v[24:27]\n"
"ds_read_b128 v[52:55], v66 offset:24576\n"
"v_mfma_f32_16x16x32_bf16 v[20:23], a[188:191], v[56:59], v[20:23]\n"
"v_mfma_f32_16x16x32_bf16 v[28:31], a[188:191], v[60:63], v[28:31]\n"
"v_mfma_f32_16x16x32_bf16 v[32:35], v[56:59], v[180:183], v[32:35]\n"
"v_mfma_f32_16x16x32_bf16 v[40:43], v[60:63], v[180:183], v[40:43]\n"
"v_mfma_f32_16x16x32_bf16 v[36:39], v[56:59], v[188:191], v[36:39]\n"
"v_mfma_f32_16x16x32_bf16 v[44:47], v[60:63], v[188:191], v[44:47]\n"
"v_mfma_f32_16x16x32_bf16 v[0:3], a[52:55], v[56:59], v[0:3]\n"
"v_mfma_f32_16x16x32_bf16 v[8:11], a[52:55], v[60:63], v[8:11]\n"
"v_mfma_f32_16x16x32_bf16 v[4:7], a[60:63], v[56:59], v[4:7]\n"
"v_mfma_f32_16x16x32_bf16 v[12:15], a[60:63], v[60:63], v[12:15]\n"
"s_waitcnt lgkmcnt(0)\n"
"v_mfma_f32_16x16x32_bf16 v[16:19], a[192:195], v[48:51], v[16:19]\n"
"ds_read_b128 v[56:59], v66 offset:9216\n"
"v_mfma_f32_16x16x32_bf16 v[24:27], a[192:195], v[52:55], v[24:27]\n"
"ds_read_b128 v[60:63], v66 offset:25600\n"
"v_mfma_f32_16x16x32_bf16 v[20:23], a[200:203], v[48:51], v[20:23]\n"
"v_mfma_f32_16x16x32_bf16 v[28:31], a[200:203], v[52:55], v[28:31]\n"
"v_mfma_f32_16x16x32_bf16 v[32:35], v[48:51], v[192:195], v[32:35]\n"
"v_mfma_f32_16x16x32_bf16 v[40:43], v[52:55], v[192:195], v[40:43]\n"
"v_mfma_f32_16x16x32_bf16 v[36:39], v[48:51], v[200:203], v[36:39]\n"
"v_mfma_f32_16x16x32_bf16 v[44:47], v[52:55], v[200:203], v[44:47]\n"
"v_mfma_f32_16x16x32_bf16 v[0:3], a[64:67], v[48:51], v[0:3]\n"
"v_mfma_f32_16x16x32_bf16 v[8:11], a[64:67], v[52:55], v[8:11]\n"
"v_mfma_f32_16x16x32_bf16 v[4:7], a[72:75], v[48:51], v[4:7]\n"
"v_mfma_f32_16x16x32_bf16 v[12:15], a[72:75], v[52:55], v[12:15]\n"
"s_waitcnt lgkmcnt(0)\n"
"v_mfma_f32_16x16x32_bf16 v[16:19], a[196:199], v[56:59], v[16:19]\n"
"ds_read_b128 v[48:51], v66 offset:10240\n"
"v_mfma_f32_16x16x32_bf16 v[24:27], a[196:199], v[60:63], v[24:27]\n"
"ds_read_b128 v[52:55], v66 offset:26624\n"
"v_mfma_f32_16x16x32_bf16 v[20:23], a[204:207], v[56:59], v[20:23]\n"
"v_mfma_f32_16x16x32_bf16 v[28:31], a[204:207], v[60:63], v[28:31]\n"
"v_mfma_f32_16x16x32_bf16 v[32:35], v[56:59], v[196:199], v[32:35]\n"
"v_mfma_f32_16x16x32_bf16 v[40:43], v[60:63], v[196:199], v[40:43]\n"
"v_mfma_f32_16x16x32_bf16 v[36:39], v[56:59], v[204:207], v[36:39]\n"
"v_mfma_f32_16x16x32_bf16 v[44:47], v[60:63], v[204:207], v[44:47]\n"
"v_mfma_f32_16x16x32_bf16 v[0:3], a[68:71], v[56:59], v[0:3]\n"
"v_mfma_f32_16x16x32_bf16 v[8:11], a[68:71], v[60:63], v[8:11]\n"
"v_mfma_f32_16x16x32_bf16 v[4:7], a[76:79], v[56:59], v[4:7]\n"
"v_mfma_f32_16x16x32_bf16 v[12:15], a[76:79], v[60:63], v[12:15]\n"
"s_waitcnt lgkmcnt(0)\n"
"s_waitcnt vmcnt(1)\n"
"v_readfirstlane_b32 s64, v121\n"
"s_cmp_eq_u32 s8, 0\n"
"s_cselect_b32 s64, 0, s64\n"
"s_cmp_gt_u32 s64, s8\n"
"s_cselect_b32 s11, 1, 0\n"
"s_cselect_b32 s70, s68, 0x6000000\n"
"v_mfma_f32_16x16x32_bf16 v[16:19], a[208:211], v[48:51], v[16:19]\n"
"ds_read_b128 v[56:59], v66 offset:11264\n"
"v_mfma_f32_16x16x32_bf16 v[24:27], a[208:211], v[52:55], v[24:27]\n"
"ds_read_b128 v[60:63], v66 offset:27648\n"
"v_mfma_f32_16x16x32_bf16 v[20:23], a[216:219], v[48:51], v[20:23]\n"
"buffer_load_dwordx4 v[88:91], v64, s[60:63], s70 offen offset:0 sc1\n"
"v_mfma_f32_16x16x32_bf16 v[28:31], a[216:219], v[52:55], v[28:31]\n"
"v_mfma_f32_16x16x32_bf16 v[32:35], v[48:51], v[208:211], v[32:35]\n"
"v_mfma_f32_16x16x32_bf16 v[40:43], v[52:55], v[208:211], v[40:43]\n"
"v_mfma_f32_16x16x32_bf16 v[36:39], v[48:51], v[216:219], v[36:39]\n"
"v_mfma_f32_16x16x32_bf16 v[44:47], v[52:55], v[216:219], v[44:47]\n"
"buffer_load_dwordx4 v[92:95], v64, s[60:63], s70 offen offset:1024 sc1\n"
"v_mfma_f32_16x16x32_bf16 v[0:3], a[80:83], v[48:51], v[0:3]\n"
"v_mfma_f32_16x16x32_bf16 v[8:11], a[80:83], v[52:55], v[8:11]\n"
"v_mfma_f32_16x16x32_bf16 v[4:7], a[88:91], v[48:51], v[4:7]\n"
"v_mfma_f32_16x16x32_bf16 v[12:15], a[88:91], v[52:55], v[12:15]\n"
"s_waitcnt lgkmcnt(0)\n"
"v_mfma_f32_16x16x32_bf16 v[16:19], a[212:215], v[56:59], v[16:19]\n"
"ds_read_b128 v[48:51], v66 offset:12288\n"
"v_mfma_f32_16x16x32_bf16 v[24:27], a[212:215], v[60:63], v[24:27]\n"
"ds_read_b128 v[52:55], v66 offset:28672\n"
"v_mfma_f32_16x16x32_bf16 v[20:23], a[220:223], v[56:59], v[20:23]\n"
"buffer_load_dwordx4 v[96:99], v64, s[60:63], s70 offen offset:2048 sc1\n"
"v_mfma_f32_16x16x32_bf16 v[28:31], a[220:223], v[60:63], v[28:31]\n"
"v_mfma_f32_16x16x32_bf16 v[32:35], v[56:59], v[212:215], v[32:35]\n"
"v_mfma_f32_16x16x32_bf16 v[40:43], v[60:63], v[212:215], v[40:43]\n"
"v_mfma_f32_16x16x32_bf16 v[36:39], v[56:59], v[220:223], v[36:39]\n"
"v_mfma_f32_16x16x32_bf16 v[44:47], v[60:63], v[220:223], v[44:47]\n"
"buffer_load_dwordx4 v[100:103], v64, s[60:63], s70 offen offset:3072 sc1\n"
"v_mfma_f32_16x16x32_bf16 v[0:3], a[84:87], v[56:59], v[0:3]\n"
"v_mfma_f32_16x16x32_bf16 v[8:11], a[84:87], v[60:63], v[8:11]\n"
"v_mfma_f32_16x16x32_bf16 v[4:7], a[92:95], v[56:59], v[4:7]\n"
"v_mfma_f32_16x16x32_bf16 v[12:15], a[92:95], v[60:63], v[12:15]\n"
"s_waitcnt lgkmcnt(0)\n"
"v_mfma_f32_16x16x32_bf16 v[16:19], a[224:227], v[48:51], v[16:19]\n"
"ds_read_b128 v[56:59], v66 offset:13312\n"
"v_mfma_f32_16x16x32_bf16 v[24:27], a[224:227], v[52:55], v[24:27]\n"
"ds_read_b128 v[60:63], v66 offset:29696\n"
"v_mfma_f32_16x16x32_bf16 v[20:23], a[232:235], v[48:51], v[20:23]\n"
"s_xor_b32 s70, s70, 32768\n"
"buffer_load_dwordx4 v[104:107], v64, s[60:63], s70 offen offset:0 sc1\n"
"v_mfma_f32_16x16x32_bf16 v[28:31], a[232:235], v[52:55], v[28:31]\n"
"v_mfma_f32_16x16x32_bf16 v[32:35], v[48:51], v[224:227], v[32:35]\n"
"v_mfma_f32_16x16x32_bf16 v[40:43], v[52:55], v[224:227], v[40:43]\n"
"v_mfma_f32_16x16x32_bf16 v[36:39], v[48:51], v[232:235], v[36:39]\n"
"v_mfma_f32_16x16x32_bf16 v[44:47], v[52:55], v[232:235], v[44:47]\n"
"buffer_load_dwordx4 v[108:111], v64, s[60:63], s70 offen offset:1024 sc1\n"
"v_mfma_f32_16x16x32_bf16 v[0:3], a[96:99], v[48:51], v[0:3]\n"
"v_mfma_f32_16x16x32_bf16 v[8:11], a[96:99], v[52:55], v[8:11]\n"
"v_mfma_f32_16x16x32_bf16 v[4:7], a[104:107], v[48:51], v[4:7]\n"
"v_mfma_f32_16x16x32_bf16 v[12:15], a[104:107], v[52:55], v[12:15]\n"
"s_waitcnt lgkmcnt(0)\n"
"v_mfma_f32_16x16x32_bf16 v[16:19], a[228:231], v[56:59], v[16:19]\n"
"ds_read_b128 v[48:51], v66 offset:14336\n"
"v_mfma_f32_16x16x32_bf16 v[24:27], a[228:231], v[60:63], v[24:27]\n"
"ds_read_b128 v[52:55], v66 offset:30720\n"
"v_mfma_f32_16x16x32_bf16 v[20:23], a[236:239], v[56:59], v[20:23]\n"
"buffer_load_dwordx4 v[112:115], v64, s[60:63], s70 offen offset:2048 sc1\n"
"v_mfma_f32_16x16x32_bf16 v[28:31], a[236:239], v[60:63], v[28:31]\n"
"v_mfma_f32_16x16x32_bf16 v[32:35], v[56:59], v[228:231], v[32:35]\n"
"v_mfma_f32_16x16x32_bf16 v[40:43], v[60:63], v[228:231], v[40:43]\n"
"v_mfma_f32_16x16x32_bf16 v[36:39], v[56:59], v[236:239], v[36:39]\n"
"v_mfma_f32_16x16x32_bf16 v[44:47], v[60:63], v[236:239], v[44:47]\n"
"buffer_load_dwordx4 v[116:119], v64, s[60:63], s70 offen offset:3072 sc1\n"
"v_mfma_f32_16x16x32_bf16 v[0:3], a[100:103], v[56:59], v[0:3]\n"
"v_mfma_f32_16x16x32_bf16 v[8:11], a[100:103], v[60:63], v[8:11]\n"
"v_mfma_f32_16x16x32_bf16 v[4:7], a[108:111], v[56:59], v[4:7]\n"
"v_mfma_f32_16x16x32_bf16 v[12:15], a[108:111], v[60:63], v[12:15]\n"
"s_waitcnt lgkmcnt(0)\n"
"v_mfma_f32_16x16x32_bf16 v[16:19], a[240:243], v[48:51], v[16:19]\n"
"ds_read_b128 v[56:59], v66 offset:15360\n"
"ds_read_b128 v[60:63], v66 offset:31744\n"
"v_mfma_f32_16x16x32_bf16 v[24:27], a[240:243], v[52:55], v[24:27]\n"
"s_and_b32 s64, s22, 7\n"
"s_lshl_b32 s65, s22, 12\n"
"v_mfma_f32_16x16x32_bf16 v[20:23], a[248:251], v[48:51], v[20:23]\n"
"s_xor_b32 s65, s65, s25\n"
"s_and_b32 s65, s65, 8192\n"
"v_mfma_f32_16x16x32_bf16 v[28:31], a[248:251], v[52:55], v[28:31]\n"
"s_waitcnt lgkmcnt(0)\n"
"v_mfma_f32_16x16x32_bf16 v[16:19], a[244:247], v[56:59], v[16:19]\n"
"s_lshl_b32 s64, s64, 20\n"
"s_add_u32 s64, s64, s65\n"
"v_mfma_f32_16x16x32_bf16 v[24:27], a[244:247], v[60:63], v[24:27]\n"
"s_add_u32 s64, s64, s10\n"
"s_add_u32 s65, s64, 16384\n"
"v_mfma_f32_16x16x32_bf16 v[20:23], a[252:255], v[56:59], v[20:23]\n"
"v_mfma_f32_16x16x32_bf16 v[28:31], a[252:255], v[60:63], v[28:31]\n"
"s_add_u32 s27, s22, 1\n"
"v_lshrrev_b32 v120, 1, v64\n"
"v_mfma_f32_16x16x32_bf16 v[32:35], v[48:51], v[240:243], v[32:35]\n"
"v_mfma_f32_16x16x32_bf16 v[40:43], v[52:55], v[240:243], v[40:43]\n"
"v_cvt_pk_bf16_f32 v72, v16, v20\n"
"v_cvt_pk_bf16_f32 v73, v17, v21\n"
"v_mfma_f32_16x16x32_bf16 v[36:39], v[48:51], v[248:251], v[36:39]\n"
"v_cvt_pk_bf16_f32 v74, v18, v22\n"
"v_cvt_pk_bf16_f32 v75, v19, v23\n"
"v_mfma_f32_16x16x32_bf16 v[44:47], v[52:55], v[248:251], v[44:47]\n"
"v_cvt_pk_bf16_f32 v76, v24, v28\n"
"v_cvt_pk_bf16_f32 v77, v25, v29\n"
"v_mfma_f32_16x16x32_bf16 v[32:35], v[56:59], v[244:247], v[32:35]\n"
"buffer_store_dwordx4 v[72:75], v69, s[60:63], s64 offen offset:0 sc1\n"
"v_cvt_pk_bf16_f32 v78, v26, v30\n"
"v_mfma_f32_16x16x32_bf16 v[40:43], v[60:63], v[244:247], v[40:43]\n"
"ds_write_b128 v67, v[72:75] offset:8192\n"
"v_cvt_pk_bf16_f32 v79, v27, v31\n"
"v_mfma_f32_16x16x32_bf16 v[36:39], v[56:59], v[252:255], v[36:39]\n"
"buffer_store_dwordx4 v[76:79], v69, s[60:63], s64 offen offset:1024 sc1\n"
"v_mfma_f32_16x16x32_bf16 v[44:47], v[60:63], v[252:255], v[44:47]\n"
"ds_write_b128 v67, v[76:79] offset:9216\n"
"v_mfma_f32_16x16x32_bf16 v[0:3], a[112:115], v[48:51], v[0:3]\n"
"v_cvt_pk_bf16_f32 v80, v32, v33\n"
"v_cvt_pk_bf16_f32 v81, v40, v41\n"
"v_mfma_f32_16x16x32_bf16 v[8:11], a[112:115], v[52:55], v[8:11]\n"
"v_cvt_pk_bf16_f32 v82, v34, v35\n"
"v_cvt_pk_bf16_f32 v83, v42, v43\n"
"v_mfma_f32_16x16x32_bf16 v[4:7], a[120:123], v[48:51], v[4:7]\n"
"v_cvt_pk_bf16_f32 v84, v36, v37\n"
"v_cvt_pk_bf16_f32 v85, v44, v45\n"
"v_mfma_f32_16x16x32_bf16 v[12:15], a[120:123], v[52:55], v[12:15]\n"
"buffer_store_dwordx2 v[80:81], v120, s[60:63], s65 offen offset:0 sc1\n"
"v_cvt_pk_bf16_f32 v86, v38, v39\n"
"v_mfma_f32_16x16x32_bf16 v[0:3], a[116:119], v[56:59], v[0:3]\n"
"ds_write_b128 v67, v[80:83] offset:16384\n"
"v_cvt_pk_bf16_f32 v87, v46, v47\n"
"v_mfma_f32_16x16x32_bf16 v[8:11], a[116:119], v[60:63], v[8:11]\n"
"buffer_store_dwordx2 v[82:83], v120, s[60:63], s65 offen offset:512 sc1\n"
"v_mfma_f32_16x16x32_bf16 v[4:7], a[124:127], v[56:59], v[4:7]\n"
"buffer_store_dwordx2 v[84:85], v120, s[60:63], s65 offen offset:1024 sc1\n"
"v_mfma_f32_16x16x32_bf16 v[12:15], a[124:127], v[60:63], v[12:15]\n"
"ds_write_b128 v67, v[84:87] offset:17408\n"
"buffer_store_dwordx2 v[86:87], v120, s[60:63], s65 offen offset:1536 sc1\n"
"v_xor_b32 v66, 32768, v66\n"
"s_nop 1\n"
"v_cvt_pk_bf16_f32 v48, v0, v4\n"
"v_cvt_pk_bf16_f32 v49, v1, v5\n"
"v_cvt_pk_bf16_f32 v50, v2, v6\n"
"v_cvt_pk_bf16_f32 v51, v3, v7\n"
"ds_write_b128 v67, v[48:51] offset:0\n"
"v_cvt_pk_bf16_f32 v52, v8, v12\n"
"v_cvt_pk_bf16_f32 v53, v9, v13\n"
"v_cvt_pk_bf16_f32 v54, v10, v14\n"
"v_cvt_pk_bf16_f32 v55, v11, v15\n"
"ds_write_b128 v67, v[52:55] offset:1024\n"
"s_add_u32 s28, s28, 1\n"
"s_cmp_eq_u32 s28, 3\n"
"s_cselect_b32 s28, 0, s28\n"
"s_cselect_b32 s64, 4294893568, 0\n"
"s_add_u32 s64, s64, 24576\n"
"v_add_u32 v67, s64, v67\n"
"s_branch .Lqatt75_%=\n"
".Lqlast77_%=:\n"
"v_rcp_f32 v76, v76\n"
"s_nop 0\n"
"v_mul_f32 v104, v104, v76\n"
"v_mul_f32 v105, v105, v76\n"
"v_mul_f32 v106, v106, v76\n"
"v_mul_f32 v107, v107, v76\n"
"v_mul_f32 v108, v108, v76\n"
"v_mul_f32 v109, v109, v76\n"
"v_mul_f32 v110, v110, v76\n"
"v_mul_f32 v111, v111, v76\n"
"v_cvt_pk_bf16_f32 v88, v104, v105\n"
"v_cvt_pk_bf16_f32 v89, v106, v107\n"
"v_cvt_pk_bf16_f32 v90, v108, v109\n"
"v_cvt_pk_bf16_f32 v91, v110, v111\n"
"buffer_store_dwordx4 v[88:91], v64, s[60:63], s66 offen offset:0 sc1\n"
"v_mul_f32 v112, v112, v76\n"
"v_mul_f32 v113, v113, v76\n"
"v_mul_f32 v114, v114, v76\n"
"v_mul_f32 v115, v115, v76\n"
"v_mul_f32 v116, v116, v76\n"
"v_mul_f32 v117, v117, v76\n"
"v_mul_f32 v118, v118, v76\n"
"v_mul_f32 v119, v119, v76\n"
"v_cvt_pk_bf16_f32 v92, v112, v113\n"
"v_cvt_pk_bf16_f32 v93, v114, v115\n"
"v_cvt_pk_bf16_f32 v94, v116, v117\n"
"v_cvt_pk_bf16_f32 v95, v118, v119\n"
"buffer_store_dwordx4 v[92:95], v64, s[60:63], s66 offen offset:1024 sc1\n"
"s_barrier\n"
"buffer_load_dword v121, off, s[60:63], s24 sc1\n"
"s_waitcnt vmcnt(0)\n"
"v_mov_b32 v122, s34\n"
"v_mov_b32 v123, s35\n"
"s_mov_b64 exec, 1\n"
"buffer_store_dwordx2 v[122:123], off, s[60:63], s26 sc1\n"
"s_mov_b64 exec, -1\n"
"s_branch .Lqask79_%=\n"
".Lqatt75_%=:\n"
"s_mov_b32 s78, 16384\n"
"s_cmp_lg_u32 s11, 0\n"
"s_waitcnt vmcnt(6)\n"
"s_cbranch_scc1 .Lqpre84_%=\n"
"s_cmp_eq_u32 s8, 0\n"
"s_cbranch_scc1 .Lqfirst80_%=\n"
"buffer_load_dword v121, off, s[60:63], s24 sc1\n"
"s_waitcnt vmcnt(0)\n"
".Lqask79_%=:\n"
"v_readfirstlane_b32 s64, v121\n"
"s_mov_b32 s78, 16384\n"
"s_add_u32 s23, s8, 1\n"
"s_cmp_gt_u32 s64, s8\n"
"s_cbranch_scc1 .Lqagain82_%=\n"
"v_mov_b32 v120, s34\n"
"s_add_u32 s64, s35, s27\n"
"v_mov_b32 v121, s64\n"
"s_mov_b64 exec, 1\n"
"buffer_store_dwordx2 v[120:121], off, s[60:63], s21 sc1\n"
"s_mov_b64 exec, -1\n"
"s_mov_b32 s78, 16384\n"
".Lsw85_%=:\n"
"buffer_load_dword v120, off, s[60:63], s24 sc1\n"
"s_waitcnt vmcnt(0)\n"
"v_readfirstlane_b32 s65, v120\n"
"s_nop 0\n"
"s_cmp_ge_u32 s65, s23\n"
"s_cbranch_scc1 .Lswok86_%=\n"
"s_sub_u32 s78, s78, 1\n"
"s_cbranch_scc1 .Lfail5_%=\n"
"s_sleep 1\n"
"s_branch .Lsw85_%=\n"
".Lswok86_%=:\n"
"s_mov_b32 s78, 16384\n"
".Lqagain82_%=:\n"
"buffer_load_dwordx4 v[88:91], v64, s[60:63], s68 offen offset:0 sc1\n"
"s_nop 3\n"
"buffer_load_dwordx4 v[92:95], v64, s[60:63], s68 offen offset:1024 sc1\n"
"s_nop 3\n"
"buffer_load_dwordx4 v[96:99], v64, s[60:63], s68 offen offset:2048 sc1\n"
"s_nop 3\n"
"buffer_load_dwordx4 v[100:103], v64, s[60:63], s68 offen offset:3072 sc1\n"
"s_nop 3\n"
"buffer_load_dwordx4 v[104:107], v64, s[60:63], s69 offen offset:0 sc1\n"
"s_nop 3\n"
"buffer_load_dwordx4 v[108:111], v64, s[60:63], s69 offen offset:1024 sc1\n"
"s_nop 3\n"
"buffer_load_dwordx4 v[112:115], v64, s[60:63], s69 offen offset:2048 sc1\n"
"s_nop 3\n"
"buffer_load_dwordx4 v[116:119], v64, s[60:63], s69 offen offset:3072 sc1\n"
"s_nop 3\n"
"s_branch .Lqgot81_%=\n"
".Lqfirst80_%=:\n"
"v_bfe_u32 v120, v64, 5, 3\n"
"v_lshlrev_b32 v120, 10, v120\n"
"v_lshrrev_b32 v121, 8, v64\n"
"v_lshl_add_u32 v120, v121, 5, v120\n"
"v_and_b32 v121, 16, v64\n"
"v_add_u32 v120, v120, v121\n"
"s_mov_b32 s78, 0\n"
"buffer_load_dwordx4 v[88:91], v120, s[56:59], 0 offen offset:0\n"
"buffer_load_dwordx4 v[92:95], v120, s[56:59], 0 offen offset:128\n"
"buffer_load_dwordx4 v[96:99], v120, s[56:59], 0 offen offset:256\n"
"buffer_load_dwordx4 v[100:103], v120, s[56:59], 0 offen offset:384\n"
"buffer_load_dwordx4 v[104:107], v120, s[56:59], 0 offen offset:512\n"
"buffer_load_dwordx4 v[108:111], v120, s[56:59], 0 offen offset:640\n"
"buffer_load_dwordx4 v[112:115], v120, s[56:59], 0 offen offset:768\n"
"buffer_load_dwordx4 v[116:119], v120, s[56:59], 0 offen offset:896\n"
".Lqgot81_%=:\n"
"v_mov_b32 v16, 0\n"
"s_and_b32 s64, s37, 1\n"
"s_lshl_b32 s64, s64, 10\n"
"v_add_u32 v17, s64, v68\n"
"s_waitcnt lgkmcnt(0)\n"
"v_mfma_f32_4x4x1_16b_f32 v[80:83], v16, v16, 0\n"
"v_mfma_f32_4x4x1_16b_f32 v[84:87], v16, v16, 0\n"
"v_mfma_f32_4x4x1_16b_f32 v[28:31], v16, v16, 0\n"
"v_mfma_f32_4x4x1_16b_f32 v[120:123], v16, v16, 0\n"
"s_waitcnt vmcnt(7)\n"
"v_smfmac_f32_16x16x64_bf16 v[80:83], v[88:91], a[128:135], v65\n"
"v_max3_u32 v16, v88, v90, v16\n"
"v_smfmac_f32_16x16x64_bf16 v[84:87], v[88:91], a[136:143], v65\n"
"ds_read_b128 v[0:3], v17 offset:0\n"
"v_smfmac_f32_16x16x64_bf16 v[28:31], v[88:91], v[128:135], v65\n"
"v_smfmac_f32_16x16x64_bf16 v[120:123], v[88:91], v[136:143], v65\n"
"ds_read_b128 v[4:7], v17 offset:2048\n"
"s_waitcnt vmcnt(6)\n"
"v_smfmac_f32_16x16x64_bf16 v[80:83], v[92:95], a[144:151], v65\n"
"v_max3_u32 v16, v92, v94, v16\n"
"v_smfmac_f32_16x16x64_bf16 v[84:87], v[92:95], a[152:159], v65\n"
"ds_read_b128 v[8:11], v17 offset:4096\n"
"v_smfmac_f32_16x16x64_bf16 v[28:31], v[92:95], v[144:151], v65\n"
"v_smfmac_f32_16x16x64_bf16 v[120:123], v[92:95], v[152:159], v65\n"
"ds_read_b128 v[12:15], v17 offset:6144\n"
"s_waitcnt vmcnt(5)\n"
"v_smfmac_f32_16x16x64_bf16 v[80:83], v[96:99], a[160:167], v65\n"
"v_max3_u32 v16, v96, v98, v16\n"
"v_smfmac_f32_16x16x64_bf16 v[84:87], v[96:99], a[168:175], v65\n"
"ds_read_b128 v[32:35], v68 offset:8192\n"
"v_smfmac_f32_16x16x64_bf16 v[28:31], v[96:99], v[160:167], v65\n"
"v_smfmac_f32_16x16x64_bf16 v[120:123], v[96:99], v[168:175], v65\n"
"ds_read_b128 v[36:39], v68 offset:9216\n"
"s_waitcnt vmcnt(4)\n"
"v_smfmac_f32_16x16x64_bf16 v[80:83], v[100:103], a[176:183], v65\n"
"v_max3_u32 v16, v100, v102, v16\n"
"v_smfmac_f32_16x16x64_bf16 v[84:87], v[100:103], a[184:191], v65\n"
"ds_read_b128 v[40:43], v68 offset:10240\n"
"v_smfmac_f32_16x16x64_bf16 v[28:31], v[100:103], v[176:183], v65\n"
"v_smfmac_f32_16x16x64_bf16 v[120:123], v[100:103], v[184:191], v65\n"
"ds_read_b128 v[44:47], v68 offset:11264\n"
"s_waitcnt vmcnt(3)\n"
"v_smfmac_f32_16x16x64_bf16 v[80:83], v[104:107], a[192:199], v65\n"
"v_max3_u32 v16, v104, v106, v16\n"
"v_smfmac_f32_16x16x64_bf16 v[84:87], v[104:107], a[200:207], v65\n"
"ds_read_b128 v[48:51], v68 offset:12288\n"
"v_smfmac_f32_16x16x64_bf16 v[28:31], v[104:107], v[192:199], v65\n"
"s_lshl_b32 s64, s37, 9\n"
"s_add_u32 s64, s64, 142336\n"
"v_lshrrev_b32 v73, 1, v64\n"
"v_smfmac_f32_16x16x64_bf16 v[120:123], v[104:107], v[200:207], v65\n"
"ds_read_b128 v[52:55], v68 offset:13312\n"
"s_waitcnt vmcnt(2)\n"
"v_smfmac_f32_16x16x64_bf16 v[80:83], v[108:111], a[208:215], v65\n"
"v_max3_u32 v16, v108, v110, v16\n"
"v_smfmac_f32_16x16x64_bf16 v[84:87], v[108:111], a[216:223], v65\n"
"ds_read_b128 v[56:59], v68 offset:14336\n"
"v_smfmac_f32_16x16x64_bf16 v[28:31], v[108:111], v[208:215], v65\n"
"v_add_u32 v72, s64, v73\n"
"v_smfmac_f32_16x16x64_bf16 v[120:123], v[108:111], v[216:223], v65\n"
"ds_read_b128 v[60:63], v68 offset:15360\n"
"s_waitcnt vmcnt(1)\n"
"v_smfmac_f32_16x16x64_bf16 v[80:83], v[112:115], a[224:231], v65\n"
"v_max3_u32 v16, v112, v114, v16\n"
"v_smfmac_f32_16x16x64_bf16 v[84:87], v[112:115], a[232:239], v65\n"
"v_smfmac_f32_16x16x64_bf16 v[28:31], v[112:115], v[224:231], v65\n"
"v_and_b32 v73, 4294966783, v72\n"
"v_smfmac_f32_16x16x64_bf16 v[120:123], v[112:115], v[232:239], v65\n"
"s_waitcnt vmcnt(0)\n"
"v_smfmac_f32_16x16x64_bf16 v[80:83], v[116:119], a[240:247], v65\n"
"v_max3_u32 v16, v116, v118, v16\n"
"v_smfmac_f32_16x16x64_bf16 v[84:87], v[116:119], a[248:255], v65\n"
"v_cmp_eq_u32 vcc, -1, v16\n"
"v_smfmac_f32_16x16x64_bf16 v[28:31], v[116:119], v[240:247], v65\n"
"v_smfmac_f32_16x16x64_bf16 v[120:123], v[116:119], v[248:255], v65\n"
"s_cbranch_vccz .Lqgood83_%=\n"
".Lqbad87_%=:\n"
"s_sub_u32 s78, s78, 1\n"
"s_cbranch_scc1 .Lfail5_%=\n"
"s_branch .Lqagain82_%=\n"
".Lqpre84_%=:\n"
"v_mov_b32 v16, 0\n"
"s_and_b32 s64, s37, 1\n"
"s_lshl_b32 s64, s64, 10\n"
"v_add_u32 v17, s64, v68\n"
"s_waitcnt lgkmcnt(0)\n"
"v_mfma_f32_4x4x1_16b_f32 v[80:83], v16, v16, 0\n"
"v_mfma_f32_4x4x1_16b_f32 v[84:87], v16, v16, 0\n"
"v_mfma_f32_4x4x1_16b_f32 v[28:31], v16, v16, 0\n"
"v_mfma_f32_4x4x1_16b_f32 v[120:123], v16, v16, 0\n"
"v_smfmac_f32_16x16x64_bf16 v[80:83], v[88:91], a[128:135], v65\n"
"v_max3_u32 v16, v88, v90, v16\n"
"v_smfmac_f32_16x16x64_bf16 v[84:87], v[88:91], a[136:143], v65\n"
"ds_read_b128 v[0:3], v17 offset:0\n"
"v_smfmac_f32_16x16x64_bf16 v[28:31], v[88:91], v[128:135], v65\n"
"v_smfmac_f32_16x16x64_bf16 v[120:123], v[88:91], v[136:143], v65\n"
"ds_read_b128 v[4:7], v17 offset:2048\n"
"v_smfmac_f32_16x16x64_bf16 v[80:83], v[92:95], a[144:151], v65\n"
"v_max3_u32 v16, v92, v94, v16\n"
"v_smfmac_f32_16x16x64_bf16 v[84:87], v[92:95], a[152:159], v65\n"
"ds_read_b128 v[8:11], v17 offset:4096\n"
"v_smfmac_f32_16x16x64_bf16 v[28:31], v[92:95], v[144:151], v65\n"
"v_smfmac_f32_16x16x64_bf16 v[120:123], v[92:95], v[152:159], v65\n"
"ds_read_b128 v[12:15], v17 offset:6144\n"
"v_smfmac_f32_16x16x64_bf16 v[80:83], v[96:99], a[160:167], v65\n"
"v_max3_u32 v16, v96, v98, v16\n"
"v_smfmac_f32_16x16x64_bf16 v[84:87], v[96:99], a[168:175], v65\n"
"ds_read_b128 v[32:35], v68 offset:8192\n"
"v_smfmac_f32_16x16x64_bf16 v[28:31], v[96:99], v[160:167], v65\n"
"v_smfmac_f32_16x16x64_bf16 v[120:123], v[96:99], v[168:175], v65\n"
"ds_read_b128 v[36:39], v68 offset:9216\n"
"v_smfmac_f32_16x16x64_bf16 v[80:83], v[100:103], a[176:183], v65\n"
"v_max3_u32 v16, v100, v102, v16\n"
"v_smfmac_f32_16x16x64_bf16 v[84:87], v[100:103], a[184:191], v65\n"
"ds_read_b128 v[40:43], v68 offset:10240\n"
"v_smfmac_f32_16x16x64_bf16 v[28:31], v[100:103], v[176:183], v65\n"
"v_smfmac_f32_16x16x64_bf16 v[120:123], v[100:103], v[184:191], v65\n"
"ds_read_b128 v[44:47], v68 offset:11264\n"
"v_smfmac_f32_16x16x64_bf16 v[80:83], v[104:107], a[192:199], v65\n"
"v_max3_u32 v16, v104, v106, v16\n"
"v_smfmac_f32_16x16x64_bf16 v[84:87], v[104:107], a[200:207], v65\n"
"ds_read_b128 v[48:51], v68 offset:12288\n"
"v_smfmac_f32_16x16x64_bf16 v[28:31], v[104:107], v[192:199], v65\n"
"s_lshl_b32 s64, s37, 9\n"
"s_add_u32 s64, s64, 142336\n"
"v_lshrrev_b32 v73, 1, v64\n"
"v_smfmac_f32_16x16x64_bf16 v[120:123], v[104:107], v[200:207], v65\n"
"ds_read_b128 v[52:55], v68 offset:13312\n"
"v_smfmac_f32_16x16x64_bf16 v[80:83], v[108:111], a[208:215], v65\n"
"v_max3_u32 v16, v108, v110, v16\n"
"v_smfmac_f32_16x16x64_bf16 v[84:87], v[108:111], a[216:223], v65\n"
"ds_read_b128 v[56:59], v68 offset:14336\n"
"v_smfmac_f32_16x16x64_bf16 v[28:31], v[108:111], v[208:215], v65\n"
"v_add_u32 v72, s64, v73\n"
"v_smfmac_f32_16x16x64_bf16 v[120:123], v[108:111], v[216:223], v65\n"
"ds_read_b128 v[60:63], v68 offset:15360\n"
"v_smfmac_f32_16x16x64_bf16 v[80:83], v[112:115], a[224:231], v65\n"
"v_max3_u32 v16, v112, v114, v16\n"
"v_smfmac_f32_16x16x64_bf16 v[84:87], v[112:115], a[232:239], v65\n"
"v_smfmac_f32_16x16x64_bf16 v[28:31], v[112:115], v[224:231], v65\n"
"v_and_b32 v73, 4294966783, v72\n"
"v_smfmac_f32_16x16x64_bf16 v[120:123], v[112:115], v[232:239], v65\n"
"v_smfmac_f32_16x16x64_bf16 v[80:83], v[116:119], a[240:247], v65\n"
"v_max3_u32 v16, v116, v118, v16\n"
"v_smfmac_f32_16x16x64_bf16 v[84:87], v[116:119], a[248:255], v65\n"
"v_cmp_eq_u32 vcc, -1, v16\n"
"v_smfmac_f32_16x16x64_bf16 v[28:31], v[116:119], v[240:247], v65\n"
"v_smfmac_f32_16x16x64_bf16 v[120:123], v[116:119], v[248:255], v65\n"
"s_cbranch_vccnz .Lqbad87_%=\n"
".Lqgood83_%=:\n"
"v_add_f32 v80, v80, v81\n"
"v_add_f32 v82, v82, v83\n"
"v_add_f32 v84, v84, v85\n"
"v_add_f32 v86, v86, v87\n"
"v_cvt_pk_bf16_f32 v80, v80, v84\n"
"v_cvt_pk_bf16_f32 v82, v82, v86\n"
"ds_write2_b32 v70, v80, v82 offset0:0 offset1:4\n"
"v_add_f32 v28, v28, v29\n"
"v_add_f32 v30, v30, v31\n"
"v_add_f32 v120, v120, v121\n"
"v_add_f32 v122, v122, v123\n"
"v_cvt_pk_bf16_f32 v84, v28, v30\n"
"v_cvt_pk_bf16_f32 v85, v120, v122\n"
"ds_write_b64 v72, v[84:85]\n"
"s_and_b32 s64, s37, 2\n"
"s_lshl_b32 s64, s64, 11\n"
"v_add_u32 v86, s64, v68\n"
"s_waitcnt lgkmcnt(2)\n"
"v_mfma_f32_16x16x32_bf16 v[16:19], v[32:35], v[0:3], 0\n"
"ds_read_b128 v[88:91], v86 offset:16384\n"
"v_mov_b32 v81, 0xd028965f\n"
"v_mfma_f32_16x16x32_bf16 v[20:23], v[36:39], v[0:3], 0\n"
"ds_read_b128 v[92:95], v86 offset:17408\n"
"v_mfma_f32_16x16x32_bf16 v[16:19], v[40:43], v[4:7], v[16:19]\n"
"ds_read_b128 v[96:99], v86 offset:18432\n"
"v_mfma_f32_16x16x32_bf16 v[20:23], v[44:47], v[4:7], v[20:23]\n"
"ds_read_b128 v[100:103], v86 offset:19456\n"
"v_mfma_f32_16x16x32_bf16 v[16:19], v[48:51], v[8:11], v[16:19]\n"
"s_waitcnt lgkmcnt(4)\n"
"s_barrier\n"
"ds_read_b128 v[104:107], v71 offset:0\n"
"v_mfma_f32_16x16x32_bf16 v[20:23], v[52:55], v[8:11], v[20:23]\n"
"ds_read_b128 v[108:111], v71 offset:576\n"
"v_mfma_f32_16x16x32_bf16 v[16:19], v[56:59], v[12:15], v[16:19]\n"
"ds_read_b128 v[112:115], v71 offset:1152\n"
"v_mfma_f32_16x16x32_bf16 v[20:23], v[60:63], v[12:15], v[20:23]\n"
"ds_read_b128 v[116:119], v71 offset:1728\n"
"ds_read2st64_b64 v[120:123], v73 offset0:0 offset1:1\n"
"s_add_u32 s29, s29, 1\n"
"s_cmp_eq_u32 s29, 3\n"
"s_cselect_b32 s29, 0, s29\n"
"s_cselect_b32 s64, 4294893568, 0\n"
"s_add_u32 s64, s64, 24576\n"
"v_add_u32 v68, s64, v68\n"
"s_waitcnt lgkmcnt(4)\n"
"v_mfma_f32_16x16x32_bf16 v[24:27], v[104:107], v[0:3], 0\n"
"v_cndmask_b32 v16, v81, v16, s[12:13]\n"
"v_cndmask_b32 v17, v81, v17, s[14:15]\n"
"s_waitcnt lgkmcnt(3)\n"
"v_mfma_f32_16x16x32_bf16 v[24:27], v[108:111], v[4:7], v[24:27]\n"
"v_cndmask_b32 v18, v81, v18, s[16:17]\n"
"v_cndmask_b32 v19, v81, v19, s[18:19]\n"
"s_waitcnt lgkmcnt(2)\n"
"v_mfma_f32_16x16x32_bf16 v[24:27], v[112:115], v[8:11], v[24:27]\n"
"v_cndmask_b32 v20, v81, v20, s[30:31]\n"
"v_cndmask_b32 v21, v81, v21, s[32:33]\n"
"s_waitcnt lgkmcnt(1)\n"
"v_mfma_f32_16x16x32_bf16 v[24:27], v[116:119], v[12:15], v[24:27]\n"
"v_cndmask_b32 v22, v81, v22, s[52:53]\n"
"v_cndmask_b32 v23, v81, v23, s[54:55]\n"
"v_max_f32 v82, v16, v17\n"
"v_max3_f32 v82, v82, v18, v19\n"
"v_max3_f32 v82, v82, v20, v21\n"
"v_max3_f32 v82, v82, v22, v23\n"
"s_lshr_b32 s66, s8, 1\n"
"s_xnor_b32 s66, s66, s77\n"
"s_and_b32 s66, s66, 1\n"
"s_lshl_b32 s66, s66, 13\n"
"s_lshl_b32 s74, s8, 20\n"
"s_add_u32 s66, s66, s74\n"
"s_lshl_b32 s26, s8, 1\n"
"s_and_b32 s74, s77, 1\n"
"s_add_u32 s26, s26, s74\n"
"s_lshl_b32 s26, s26, 13\n"
"s_add_u32 s26, s26, 6291456\n"
"v_max3_f32 v83, v82, v24, v25\n"
"v_max3_f32 v82, v82, v24, v25\n"
"s_add_u32 s66, s66, s79\n"
"s_nop 0\n"
"v_permlane32_swap_b32 v82, v83\n"
"v_max_f32 v82, v82, v83\n"
"v_mov_b32 v83, v82\n"
"s_add_u32 s26, s26, s76\n"
"s_waitcnt lgkmcnt(0)\n"
"v_permlane16_swap_b32 v82, v83\n"
"v_max_f32 v82, v82, v83\n"
"v_mul_f32 v82, s20, v82\n"
"v_fma_f32 v16, v16, s20, -v82\n"
"v_fma_f32 v17, v17, s20, -v82\n"
"v_fma_f32 v18, v18, s20, -v82\n"
"v_fma_f32 v19, v19, s20, -v82\n"
"v_fma_f32 v20, v20, s20, -v82\n"
"v_fma_f32 v21, v21, s20, -v82\n"
"v_fma_f32 v22, v22, s20, -v82\n"
"v_fma_f32 v23, v23, s20, -v82\n"
"v_fma_f32 v24, v24, s20, -v82\n"
"v_fma_f32 v25, v25, s20, -v82\n"
"v_exp_f32 v16, v16\n"
"v_exp_f32 v17, v17\n"
"v_exp_f32 v18, v18\n"
"v_exp_f32 v19, v19\n"
"v_exp_f32 v20, v20\n"
"v_exp_f32 v21, v21\n"
"v_exp_f32 v22, v22\n"
"v_exp_f32 v23, v23\n"
"v_exp_f32 v24, v24\n"
"v_exp_f32 v25, v25\n"
"v_cvt_pk_bf16_f32 v0, v16, v17\n"
"v_cvt_pk_bf16_f32 v1, v20, v21\n"
"v_cvt_pk_bf16_f32 v2, v18, v19\n"
"v_cvt_pk_bf16_f32 v3, v22, v23\n"
"v_cvt_pk_bf16_f32 v4, v24, v25\n"
"v_mov_b32 v5, 0\n"
"v_mfma_f32_16x16x32_bf16 v[76:79], v[124:127], v[0:3], 0\n"
"v_mov_b32 v6, 0\n"
"v_mov_b32 v7, v4\n"
"v_mfma_f32_16x16x32_bf16 v[104:107], v[88:91], v[0:3], 0\n"
"ds_read_b128 v[48:51], v66 offset:0\n"
"v_mfma_f32_16x16x32_bf16 v[108:111], v[92:95], v[0:3], 0\n"
"ds_read_b128 v[52:55], v66 offset:16384\n"
"v_mfma_f32_16x16x32_bf16 v[112:115], v[96:99], v[0:3], 0\n"
"v_mfma_f32_16x16x32_bf16 v[116:119], v[100:103], v[0:3], 0\n"
"v_mfma_f32_16x16x16_bf16 v[76:79], v[124:125], v[4:5], v[76:79]\n"
"s_waitcnt vmcnt(0)\n"
"v_mov_b32 v84, s34\n"
"s_add_u32 s64, s35, s27\n"
"v_mov_b32 v85, s64\n"
"s_mov_b64 exec, 1\n"
"buffer_store_dwordx2 v[84:85], off, s[60:63], s21 sc1\n"
"s_mov_b64 exec, -1\n"
"v_mfma_f32_16x16x16_bf16 v[104:107], v[120:121], v[4:5], v[104:107]\n"
"v_mfma_f32_16x16x16_bf16 v[108:111], v[120:121], v[6:7], v[108:111]\n"
"v_mfma_f32_16x16x16_bf16 v[112:115], v[122:123], v[4:5], v[112:115]\n"
"v_mfma_f32_16x16x16_bf16 v[116:119], v[122:123], v[6:7], v[116:119]\n"
".Lqnxt76_%=:\n"
"s_add_u32 s8, s8, 2\n"
"s_cmp_lt_i32 s8, 64\n"
"s_cbranch_scc1 .Lqtop74_%=\n"
"v_rcp_f32 v76, v76\n"
"s_nop 0\n"
"v_mul_f32 v104, v104, v76\n"
"v_mul_f32 v105, v105, v76\n"
"v_mul_f32 v106, v106, v76\n"
"v_mul_f32 v107, v107, v76\n"
"v_mul_f32 v108, v108, v76\n"
"v_mul_f32 v109, v109, v76\n"
"v_mul_f32 v110, v110, v76\n"
"v_mul_f32 v111, v111, v76\n"
"v_cvt_pk_bf16_f32 v88, v104, v105\n"
"v_cvt_pk_bf16_f32 v89, v106, v107\n"
"v_cvt_pk_bf16_f32 v90, v108, v109\n"
"v_cvt_pk_bf16_f32 v91, v110, v111\n"
"buffer_store_dwordx4 v[88:91], v64, s[60:63], s66 offen offset:0 sc1\n"
"v_mul_f32 v112, v112, v76\n"
"v_mul_f32 v113, v113, v76\n"
"v_mul_f32 v114, v114, v76\n"
"v_mul_f32 v115, v115, v76\n"
"v_mul_f32 v116, v116, v76\n"
"v_mul_f32 v117, v117, v76\n"
"v_mul_f32 v118, v118, v76\n"
"v_mul_f32 v119, v119, v76\n"
"v_cvt_pk_bf16_f32 v92, v112, v113\n"
"v_cvt_pk_bf16_f32 v93, v114, v115\n"
"v_cvt_pk_bf16_f32 v94, v116, v117\n"
"v_cvt_pk_bf16_f32 v95, v118, v119\n"
"buffer_store_dwordx4 v[92:95], v64, s[60:63], s66 offen offset:1024 sc1\n"
"s_waitcnt vmcnt(0)\n"
"v_mov_b32 v122, s34\n"
"v_mov_b32 v123, s35\n"
"s_mov_b64 exec, 1\n"
"buffer_store_dwordx2 v[122:123], off, s[60:63], s26 sc1\n"
"s_mov_b64 exec, -1\n"
"s_endpgm\n"
".Lq14_%=:\n"
".Lhi7_%=:\n"
"s_mov_b32 s52, s8\n"
"s_and_b32 s53, s9, 0xffff\n"
"s_mov_b32 s54, 0x200000\n"
"s_mov_b32 s55, 0x20000\n"
"s_lshr_b32 s66, s36, 2\n"
"s_andn2_b32 s66, s66, 1\n"
"s_bfe_u32 s67, s36, 0x10001\n"
"s_add_u32 s66, s66, s67\n"
"v_lshrrev_b32 v104, 3, v119\n"
"v_and_b32 v105, 7, v119\n"
"v_lshlrev_b32 v106, 10, v104\n"
"v_lshl_add_u32 v106, v105, 4, v106\n"
"s_lshl_b32 s64, s66, 2\n"
"s_add_u32 s64, s64, s37\n"
"s_lshl_b32 s64, s64, 13\n"
"buffer_load_dwordx4 v[72:75], v106, s[52:55], s64 offen offset:0\n"
"buffer_load_dwordx4 v[76:79], v106, s[52:55], s64 offen offset:128\n"
"buffer_load_dwordx4 v[80:83], v106, s[52:55], s64 offen offset:256\n"
"buffer_load_dwordx4 v[84:87], v106, s[52:55], s64 offen offset:384\n"
"buffer_load_dwordx4 v[88:91], v106, s[52:55], s64 offen offset:512\n"
"buffer_load_dwordx4 v[92:95], v106, s[52:55], s64 offen offset:640\n"
"buffer_load_dwordx4 v[96:99], v106, s[52:55], s64 offen offset:768\n"
"buffer_load_dwordx4 v[100:103], v106, s[52:55], s64 offen offset:896\n"
"s_waitcnt vmcnt(0)\n"
"v_readfirstlane_b32 s75, v118\n"
"s_nop 0\n"
"s_add_u32 s75, s75, 1\n"
"s_mul_i32 s34, s75, 0x9e3779b1\n"
"s_xor_b32 s35, s34, 0x5bd1e995\n"
"v_lshlrev_b32 v0, 16, v72\n"
"v_and_b32 v1, 0xffff0000, v72\n"
"v_lshlrev_b32 v2, 16, v73\n"
"v_and_b32 v3, 0xffff0000, v73\n"
"v_lshlrev_b32 v4, 16, v74\n"
"v_and_b32 v5, 0xffff0000, v74\n"
"v_lshlrev_b32 v6, 16, v75\n"
"v_and_b32 v7, 0xffff0000, v75\n"
"v_lshlrev_b32 v8, 16, v76\n"
"v_and_b32 v9, 0xffff0000, v76\n"
"v_lshlrev_b32 v10, 16, v77\n"
"v_and_b32 v11, 0xffff0000, v77\n"
"v_lshlrev_b32 v12, 16, v78\n"
"v_and_b32 v13, 0xffff0000, v78\n"
"v_lshlrev_b32 v14, 16, v79\n"
"v_and_b32 v15, 0xffff0000, v79\n"
"v_lshlrev_b32 v16, 16, v80\n"
"v_and_b32 v17, 0xffff0000, v80\n"
"v_lshlrev_b32 v18, 16, v81\n"
"v_and_b32 v19, 0xffff0000, v81\n"
"v_lshlrev_b32 v20, 16, v82\n"
"v_and_b32 v21, 0xffff0000, v82\n"
"v_lshlrev_b32 v22, 16, v83\n"
"v_and_b32 v23, 0xffff0000, v83\n"
"v_lshlrev_b32 v24, 16, v84\n"
"v_and_b32 v25, 0xffff0000, v84\n"
"v_lshlrev_b32 v26, 16, v85\n"
"v_and_b32 v27, 0xffff0000, v85\n"
"v_lshlrev_b32 v28, 16, v86\n"
"v_and_b32 v29, 0xffff0000, v86\n"
"v_lshlrev_b32 v30, 16, v87\n"
"v_and_b32 v31, 0xffff0000, v87\n"
"v_lshlrev_b32 v32, 16, v88\n"
"v_and_b32 v33, 0xffff0000, v88\n"
"v_lshlrev_b32 v34, 16, v89\n"
"v_and_b32 v35, 0xffff0000, v89\n"
"v_lshlrev_b32 v36, 16, v90\n"
"v_and_b32 v37, 0xffff0000, v90\n"
"v_lshlrev_b32 v38, 16, v91\n"
"v_and_b32 v39, 0xffff0000, v91\n"
"v_lshlrev_b32 v40, 16, v92\n"
"v_and_b32 v41, 0xffff0000, v92\n"
"v_lshlrev_b32 v42, 16, v93\n"
"v_and_b32 v43, 0xffff0000, v93\n"
"v_lshlrev_b32 v44, 16, v94\n"
"v_and_b32 v45, 0xffff0000, v94\n"
"v_lshlrev_b32 v46, 16, v95\n"
"v_and_b32 v47, 0xffff0000, v95\n"
"v_lshlrev_b32 v48, 16, v96\n"
"v_and_b32 v49, 0xffff0000, v96\n"
"v_lshlrev_b32 v50, 16, v97\n"
"v_and_b32 v51, 0xffff0000, v97\n"
"v_lshlrev_b32 v52, 16, v98\n"
"v_and_b32 v53, 0xffff0000, v98\n"
"v_lshlrev_b32 v54, 16, v99\n"
"v_and_b32 v55, 0xffff0000, v99\n"
"v_lshlrev_b32 v56, 16, v100\n"
"v_and_b32 v57, 0xffff0000, v100\n"
"v_lshlrev_b32 v58, 16, v101\n"
"v_and_b32 v59, 0xffff0000, v101\n"
"v_lshlrev_b32 v60, 16, v102\n"
"v_and_b32 v61, 0xffff0000, v102\n"
"v_lshlrev_b32 v62, 16, v103\n"
"v_and_b32 v63, 0xffff0000, v103\n"
"v_pk_add_f32 v[64:65], v[0:1], v[2:3]\n"
"v_pk_add_f32 v[66:67], v[4:5], v[6:7]\n"
"v_pk_add_f32 v[64:65], v[64:65], v[8:9]\n"
"v_pk_add_f32 v[66:67], v[66:67], v[10:11]\n"
"v_pk_add_f32 v[64:65], v[64:65], v[12:13]\n"
"v_pk_add_f32 v[66:67], v[66:67], v[14:15]\n"
"v_pk_add_f32 v[64:65], v[64:65], v[16:17]\n"
"v_pk_add_f32 v[66:67], v[66:67], v[18:19]\n"
"v_pk_add_f32 v[64:65], v[64:65], v[20:21]\n"
"v_pk_add_f32 v[66:67], v[66:67], v[22:23]\n"
"v_pk_add_f32 v[64:65], v[64:65], v[24:25]\n"
"v_pk_add_f32 v[66:67], v[66:67], v[26:27]\n"
"v_pk_add_f32 v[64:65], v[64:65], v[28:29]\n"
"v_pk_add_f32 v[66:67], v[66:67], v[30:31]\n"
"v_pk_add_f32 v[64:65], v[64:65], v[32:33]\n"
"v_pk_add_f32 v[66:67], v[66:67], v[34:35]\n"
"v_pk_add_f32 v[64:65], v[64:65], v[36:37]\n"
"v_pk_add_f32 v[66:67], v[66:67], v[38:39]\n"
"v_pk_add_f32 v[64:65], v[64:65], v[40:41]\n"
"v_pk_add_f32 v[66:67], v[66:67], v[42:43]\n"
"v_pk_add_f32 v[64:65], v[64:65], v[44:45]\n"
"v_pk_add_f32 v[66:67], v[66:67], v[46:47]\n"
"v_pk_add_f32 v[64:65], v[64:65], v[48:49]\n"
"v_pk_add_f32 v[66:67], v[66:67], v[50:51]\n"
"v_pk_add_f32 v[64:65], v[64:65], v[52:53]\n"
"v_pk_add_f32 v[66:67], v[66:67], v[54:55]\n"
"v_pk_add_f32 v[64:65], v[64:65], v[56:57]\n"
"v_pk_add_f32 v[66:67], v[66:67], v[58:59]\n"
"v_pk_add_f32 v[64:65], v[64:65], v[60:61]\n"
"v_pk_add_f32 v[66:67], v[66:67], v[62:63]\n"
"v_pk_add_f32 v[64:65], v[64:65], v[66:67]\n"
"v_add_f32 v64, v64, v65\n"
"s_nop 1\n"
"v_add_f32_dpp v64, v64, v64 quad_perm:[1,0,3,2] row_mask:0xf bank_mask:0xf\n"
"s_nop 1\n"
"v_add_f32_dpp v64, v64, v64 quad_perm:[2,3,0,1] row_mask:0xf bank_mask:0xf\n"
"s_nop 1\n"
"v_add_f32_dpp v64, v64, v64 row_half_mirror row_mask:0xf bank_mask:0xf\n"
"v_mul_f32 v64, 0x3b000000, v64\n"
"v_sub_f32 v64, 0, v64\n"
"v_mov_b32 v65, v64\n"
"v_pk_add_f32 v[0:1], v[0:1], v[64:65]\n"
"v_pk_add_f32 v[2:3], v[2:3], v[64:65]\n"
"v_pk_add_f32 v[4:5], v[4:5], v[64:65]\n"
"v_pk_add_f32 v[6:7], v[6:7], v[64:65]\n"
"v_pk_add_f32 v[8:9], v[8:9], v[64:65]\n"
"v_pk_add_f32 v[10:11], v[10:11], v[64:65]\n"
"v_pk_add_f32 v[12:13], v[12:13], v[64:65]\n"
"v_pk_add_f32 v[14:15], v[14:15], v[64:65]\n"
"v_pk_add_f32 v[16:17], v[16:17], v[64:65]\n"
"v_pk_add_f32 v[18:19], v[18:19], v[64:65]\n"
"v_pk_add_f32 v[20:21], v[20:21], v[64:65]\n"
"v_pk_add_f32 v[22:23], v[22:23], v[64:65]\n"
"v_pk_add_f32 v[24:25], v[24:25], v[64:65]\n"
"v_pk_add_f32 v[26:27], v[26:27], v[64:65]\n"
"v_pk_add_f32 v[28:29], v[28:29], v[64:65]\n"
"v_pk_add_f32 v[30:31], v[30:31], v[64:65]\n"
"v_pk_add_f32 v[32:33], v[32:33], v[64:65]\n"
"v_pk_add_f32 v[34:35], v[34:35], v[64:65]\n"
"v_pk_add_f32 v[36:37], v[36:37], v[64:65]\n"
"v_pk_add_f32 v[38:39], v[38:39], v[64:65]\n"
"v_pk_add_f32 v[40:41], v[40:41], v[64:65]\n"
"v_pk_add_f32 v[42:43], v[42:43], v[64:65]\n"
"v_pk_add_f32 v[44:45], v[44:45], v[64:65]\n"
"v_pk_add_f32 v[46:47], v[46:47], v[64:65]\n"
"v_pk_add_f32 v[48:49], v[48:49], v[64:65]\n"
"v_pk_add_f32 v[50:51], v[50:51], v[64:65]\n"
"v_pk_add_f32 v[52:53], v[52:53], v[64:65]\n"
"v_pk_add_f32 v[54:55], v[54:55], v[64:65]\n"
"v_pk_add_f32 v[56:57], v[56:57], v[64:65]\n"
"v_pk_add_f32 v[58:59], v[58:59], v[64:65]\n"
"v_pk_add_f32 v[60:61], v[60:61], v[64:65]\n"
"v_pk_add_f32 v[62:63], v[62:63], v[64:65]\n"
"v_pk_mul_f32 v[64:65], v[0:1], v[0:1]\n"
"v_pk_mul_f32 v[66:67], v[2:3], v[2:3]\n"
"v_pk_fma_f32 v[64:65], v[4:5], v[4:5], v[64:65]\n"
"v_pk_fma_f32 v[66:67], v[6:7], v[6:7], v[66:67]\n"
"v_pk_fma_f32 v[64:65], v[8:9], v[8:9], v[64:65]\n"
"v_pk_fma_f32 v[66:67], v[10:11], v[10:11], v[66:67]\n"
"v_pk_fma_f32 v[64:65], v[12:13], v[12:13], v[64:65]\n"
"v_pk_fma_f32 v[66:67], v[14:15], v[14:15], v[66:67]\n"
"v_pk_fma_f32 v[64:65], v[16:17], v[16:17], v[64:65]\n"
"v_pk_fma_f32 v[66:67], v[18:19], v[18:19], v[66:67]\n"
"v_pk_fma_f32 v[64:65], v[20:21], v[20:21], v[64:65]\n"
"v_pk_fma_f32 v[66:67], v[22:23], v[22:23], v[66:67]\n"
"v_pk_fma_f32 v[64:65], v[24:25], v[24:25], v[64:65]\n"
"v_pk_fma_f32 v[66:67], v[26:27], v[26:27], v[66:67]\n"
"v_pk_fma_f32 v[64:65], v[28:29], v[28:29], v[64:65]\n"
"v_pk_fma_f32 v[66:67], v[30:31], v[30:31], v[66:67]\n"
"v_pk_fma_f32 v[64:65], v[32:33], v[32:33], v[64:65]\n"
"v_pk_fma_f32 v[66:67], v[34:35], v[34:35], v[66:67]\n"
"v_pk_fma_f32 v[64:65], v[36:37], v[36:37], v[64:65]\n"
"v_pk_fma_f32 v[66:67], v[38:39], v[38:39], v[66:67]\n"
"v_pk_fma_f32 v[64:65], v[40:41], v[40:41], v[64:65]\n"
"v_pk_fma_f32 v[66:67], v[42:43], v[42:43], v[66:67]\n"
"v_pk_fma_f32 v[64:65], v[44:45], v[44:45], v[64:65]\n"
"v_pk_fma_f32 v[66:67], v[46:47], v[46:47], v[66:67]\n"
"v_pk_fma_f32 v[64:65], v[48:49], v[48:49], v[64:65]\n"
"v_pk_fma_f32 v[66:67], v[50:51], v[50:51], v[66:67]\n"
"v_pk_fma_f32 v[64:65], v[52:53], v[52:53], v[64:65]\n"
"v_pk_fma_f32 v[66:67], v[54:55], v[54:55], v[66:67]\n"
"v_pk_fma_f32 v[64:65], v[56:57], v[56:57], v[64:65]\n"
"v_pk_fma_f32 v[66:67], v[58:59], v[58:59], v[66:67]\n"
"v_pk_fma_f32 v[64:65], v[60:61], v[60:61], v[64:65]\n"
"v_pk_fma_f32 v[66:67], v[62:63], v[62:63], v[66:67]\n"
"v_pk_add_f32 v[64:65], v[64:65], v[66:67]\n"
"v_add_f32 v64, v64, v65\n"
"s_nop 1\n"
"v_add_f32_dpp v64, v64, v64 quad_perm:[1,0,3,2] row_mask:0xf bank_mask:0xf\n"
"s_nop 1\n"
"v_add_f32_dpp v64, v64, v64 quad_perm:[2,3,0,1] row_mask:0xf bank_mask:0xf\n"
"s_nop 1\n"
"v_add_f32_dpp v64, v64, v64 row_half_mirror row_mask:0xf bank_mask:0xf\n"
"v_mul_f32 v64, 0x3b000000, v64\n"
"v_add_f32 v64, 0x3727c5ac, v64\n"
"v_sqrt_f32 v64, v64\n"
"v_rcp_f32 v64, v64\n"
"s_nop 0\n"
"v_mov_b32 v65, v64\n"
"s_lshr_b32 s64, s37, 1\n"
"s_lshl_b32 s64, s64, 14\n"
"s_and_b32 s65, s37, 1\n"
"s_lshl_b32 s65, s65, 7\n"
"s_add_u32 s64, s64, s65\n"
"s_add_u32 s64, s64, 0\n"
"v_lshrrev_b32 v106, 1, v105\n"
"v_lshlrev_b32 v106, 9, v106\n"
"v_lshl_add_u32 v106, v104, 4, v106\n"
"v_and_b32 v66, 1, v105\n"
"v_lshl_add_u32 v106, v66, 2, v106\n"
"v_add_u32 v106, s64, v106\n"
"v_pk_mul_f32 v[0:1], v[0:1], v[64:65]\n"
"v_pk_mul_f32 v[2:3], v[2:3], v[64:65]\n"
"v_pk_mul_f32 v[4:5], v[4:5], v[64:65]\n"
"v_pk_mul_f32 v[6:7], v[6:7], v[64:65]\n"
"v_cvt_pk_bf16_f32 v72, v0, v1\n"
"v_cvt_pk_bf16_f32 v73, v2, v3\n"
"v_cvt_pk_bf16_f32 v74, v4, v5\n"
"v_cvt_pk_bf16_f32 v75, v6, v7\n"
"ds_write2_b32 v106, v72, v73 offset0:0 offset1:2\n"
"ds_write2_b32 v106, v74, v75 offset0:64 offset1:66\n"
"v_add_u32 v106, 2048, v106\n"
"v_pk_mul_f32 v[8:9], v[8:9], v[64:65]\n"
"v_pk_mul_f32 v[10:11], v[10:11], v[64:65]\n"
"v_pk_mul_f32 v[12:13], v[12:13], v[64:65]\n"
"v_pk_mul_f32 v[14:15], v[14:15], v[64:65]\n"
"v_cvt_pk_bf16_f32 v76, v8, v9\n"
"v_cvt_pk_bf16_f32 v77, v10, v11\n"
"v_cvt_pk_bf16_f32 v78, v12, v13\n"
"v_cvt_pk_bf16_f32 v79, v14, v15\n"
"ds_write2_b32 v106, v76, v77 offset0:0 offset1:2\n"
"ds_write2_b32 v106, v78, v79 offset0:64 offset1:66\n"
"v_add_u32 v106, 2048, v106\n"
"v_pk_mul_f32 v[16:17], v[16:17], v[64:65]\n"
"v_pk_mul_f32 v[18:19], v[18:19], v[64:65]\n"
"v_pk_mul_f32 v[20:21], v[20:21], v[64:65]\n"
"v_pk_mul_f32 v[22:23], v[22:23], v[64:65]\n"
"v_cvt_pk_bf16_f32 v80, v16, v17\n"
"v_cvt_pk_bf16_f32 v81, v18, v19\n"
"v_cvt_pk_bf16_f32 v82, v20, v21\n"
"v_cvt_pk_bf16_f32 v83, v22, v23\n"
"ds_write2_b32 v106, v80, v81 offset0:0 offset1:2\n"
"ds_write2_b32 v106, v82, v83 offset0:64 offset1:66\n"
"v_add_u32 v106, 2048, v106\n"
"v_pk_mul_f32 v[24:25], v[24:25], v[64:65]\n"
"v_pk_mul_f32 v[26:27], v[26:27], v[64:65]\n"
"v_pk_mul_f32 v[28:29], v[28:29], v[64:65]\n"
"v_pk_mul_f32 v[30:31], v[30:31], v[64:65]\n"
"v_cvt_pk_bf16_f32 v84, v24, v25\n"
"v_cvt_pk_bf16_f32 v85, v26, v27\n"
"v_cvt_pk_bf16_f32 v86, v28, v29\n"
"v_cvt_pk_bf16_f32 v87, v30, v31\n"
"ds_write2_b32 v106, v84, v85 offset0:0 offset1:2\n"
"ds_write2_b32 v106, v86, v87 offset0:64 offset1:66\n"
"v_add_u32 v106, 2048, v106\n"
"v_pk_mul_f32 v[32:33], v[32:33], v[64:65]\n"
"v_pk_mul_f32 v[34:35], v[34:35], v[64:65]\n"
"v_pk_mul_f32 v[36:37], v[36:37], v[64:65]\n"
"v_pk_mul_f32 v[38:39], v[38:39], v[64:65]\n"
"v_cvt_pk_bf16_f32 v88, v32, v33\n"
"v_cvt_pk_bf16_f32 v89, v34, v35\n"
"v_cvt_pk_bf16_f32 v90, v36, v37\n"
"v_cvt_pk_bf16_f32 v91, v38, v39\n"
"ds_write2_b32 v106, v88, v89 offset0:0 offset1:2\n"
"ds_write2_b32 v106, v90, v91 offset0:64 offset1:66\n"
"v_add_u32 v106, 2048, v106\n"
"v_pk_mul_f32 v[40:41], v[40:41], v[64:65]\n"
"v_pk_mul_f32 v[42:43], v[42:43], v[64:65]\n"
"v_pk_mul_f32 v[44:45], v[44:45], v[64:65]\n"
"v_pk_mul_f32 v[46:47], v[46:47], v[64:65]\n"
"v_cvt_pk_bf16_f32 v92, v40, v41\n"
"v_cvt_pk_bf16_f32 v93, v42, v43\n"
"v_cvt_pk_bf16_f32 v94, v44, v45\n"
"v_cvt_pk_bf16_f32 v95, v46, v47\n"
"ds_write2_b32 v106, v92, v93 offset0:0 offset1:2\n"
"ds_write2_b32 v106, v94, v95 offset0:64 offset1:66\n"
"v_add_u32 v106, 2048, v106\n"
"v_pk_mul_f32 v[48:49], v[48:49], v[64:65]\n"
"v_pk_mul_f32 v[50:51], v[50:51], v[64:65]\n"
"v_pk_mul_f32 v[52:53], v[52:53], v[64:65]\n"
"v_pk_mul_f32 v[54:55], v[54:55], v[64:65]\n"
"v_cvt_pk_bf16_f32 v96, v48, v49\n"
"v_cvt_pk_bf16_f32 v97, v50, v51\n"
"v_cvt_pk_bf16_f32 v98, v52, v53\n"
"v_cvt_pk_bf16_f32 v99, v54, v55\n"
"ds_write2_b32 v106, v96, v97 offset0:0 offset1:2\n"
"ds_write2_b32 v106, v98, v99 offset0:64 offset1:66\n"
"v_add_u32 v106, 2048, v106\n"
"v_pk_mul_f32 v[56:57], v[56:57], v[64:65]\n"
"v_pk_mul_f32 v[58:59], v[58:59], v[64:65]\n"
"v_pk_mul_f32 v[60:61], v[60:61], v[64:65]\n"
"v_pk_mul_f32 v[62:63], v[62:63], v[64:65]\n"
"v_cvt_pk_bf16_f32 v100, v56, v57\n"
"v_cvt_pk_bf16_f32 v101, v58, v59\n"
"v_cvt_pk_bf16_f32 v102, v60, v61\n"
"v_cvt_pk_bf16_f32 v103, v62, v63\n"
"ds_write2_b32 v106, v100, v101 offset0:0 offset1:2\n"
"ds_write2_b32 v106, v102, v103 offset0:64 offset1:66\n"
"v_lshlrev_b32 v107, 4, v119\n"
"s_lshl_b32 s64, s37, 13\n"
"v_add_u32 v112, s64, v107\n"
"s_lshl_b32 s65, s66, 15\n"
"s_add_u32 s64, s64, s65\n"
"s_add_u32 s64, s64, 4194304\n"
"s_add_u32 s65, s64, 4096\n"
"s_waitcnt lgkmcnt(0)\n"
"s_barrier\n"
"ds_read_b128 v[0:3], v112 offset:0\n"
"ds_read_b128 v[4:7], v112 offset:1024\n"
"ds_read_b128 v[8:11], v112 offset:2048\n"
"ds_read_b128 v[12:15], v112 offset:3072\n"
"ds_read_b128 v[16:19], v112 offset:4096\n"
"ds_read_b128 v[20:23], v112 offset:5120\n"
"ds_read_b128 v[24:27], v112 offset:6144\n"
"ds_read_b128 v[28:31], v112 offset:7168\n"
"s_waitcnt lgkmcnt(7)\n"
"buffer_store_dwordx4 v[0:3], v107, s[60:63], s64 offen offset:0 sc1\n"
"s_waitcnt lgkmcnt(6)\n"
"buffer_store_dwordx4 v[4:7], v107, s[60:63], s64 offen offset:1024 sc1\n"
"s_waitcnt lgkmcnt(5)\n"
"buffer_store_dwordx4 v[8:11], v107, s[60:63], s64 offen offset:2048 sc1\n"
"s_waitcnt lgkmcnt(4)\n"
"buffer_store_dwordx4 v[12:15], v107, s[60:63], s64 offen offset:3072 sc1\n"
"s_waitcnt lgkmcnt(3)\n"
"buffer_store_dwordx4 v[16:19], v107, s[60:63], s65 offen offset:0 sc1\n"
"s_waitcnt lgkmcnt(2)\n"
"buffer_store_dwordx4 v[20:23], v107, s[60:63], s65 offen offset:1024 sc1\n"
"s_waitcnt lgkmcnt(1)\n"
"buffer_store_dwordx4 v[24:27], v107, s[60:63], s65 offen offset:2048 sc1\n"
"s_waitcnt lgkmcnt(0)\n"
"buffer_store_dwordx4 v[28:31], v107, s[60:63], s65 offen offset:3072 sc1\n"
"s_lshl_b32 s66, s66, 3\n"
"s_add_u32 s66, s66, 3147776\n"
"s_waitcnt vmcnt(0)\n"
"s_barrier\n"
"s_cmp_lg_u32 s37, 0\n"
"s_cbranch_scc1 .Lrdone88_%=\n"
"v_mov_b32 v32, s34\n"
"v_mov_b32 v33, s35\n"
"s_mov_b64 exec, 1\n"
"buffer_store_dwordx2 v[32:33], off, s[60:63], s66 sc1\n"
"s_mov_b64 exec, -1\n"
".Lrdone88_%=:\n"
"s_and_b32 s64, s36, 3\n"
"s_lshr_b32 s65, s36, 3\n"
"s_and_b32 s38, s65, 7\n"
"s_cmp_eq_u32 s64, 3\n"
"s_cbranch_scc1 .Lx768_%=\n"
"s_mov_b32 s40, s18\n"
"s_and_b32 s41, s19, 0xffff\n"
"s_mov_b32 s42, 0x400000\n"
"s_mov_b32 s43, 0x20000\n"
"s_mov_b32 s48, s26\n"
"s_and_b32 s49, s27, 0xffff\n"
"s_mov_b32 s50, 0x200000\n"
"s_mov_b32 s51, 0x20000\n"
"s_sleep 32\n"
"s_sleep 32\n"
"s_sleep 32\n"
"s_sleep 32\n"
"s_sleep 32\n"
"v_lshlrev_b32 v114, 4, v119\n"
"v_lshrrev_b32 v0, 3, v119\n"
"v_lshlrev_b32 v1, 10, v0\n"
"v_lshrrev_b32 v2, 4, v119\n"
"v_lshlrev_b32 v2, 1, v2\n"
"v_and_b32 v3, 7, v119\n"
"v_xor_b32 v2, v2, v3\n"
"v_lshl_add_u32 v1, v2, 4, v1\n"
"s_lshl_b32 s64, s38, 7\n"
"v_add_u32 v4, s64, v1\n"
"s_lshl_b32 s66, s37, 13\n"
"v_lshrrev_b32 v1, 2, v119\n"
"v_lshlrev_b32 v1, 7, v1\n"
"v_and_b32 v0, 3, v0\n"
"v_lshl_add_u32 v1, v0, 5, v1\n"
"v_and_b32 v0, 3, v119\n"
"v_lshl_add_u32 v1, v0, 3, v1\n"
"v_add_u32 v8, s66, v1\n"
"s_mul_i32 s67, s37, 1048576\n"
"s_lshl_b32 s64, s37, 15\n"
"s_add_u32 s64, s64, 32768\n"
"v_add_u32 v121, s64, v114\n"
"v_add_u32 v5, 7168, v4\n"
"v_xor_b32 v9, 32, v8\n"
"v_add_u32 v6, 14336, v4\n"
"v_xor_b32 v10, 64, v8\n"
"v_add_u32 v7, 21504, v4\n"
"v_xor_b32 v11, 96, v8\n"
"s_add_u32 m0, s66, 0\n"
"s_add_u32 s64, s67, 0\n"
"buffer_load_dwordx4 v4, s[40:43], s64 offen offset:0 lds\n"
"buffer_load_dwordx4 v5, s[40:43], s64 offen offset:1024 lds\n"
"buffer_load_dwordx4 v6, s[40:43], s64 offen offset:2048 lds\n"
"buffer_load_dwordx4 v7, s[40:43], s64 offen offset:3072 lds\n"
"s_add_u32 m0, s66, 4096\n"
"s_add_u32 s64, s67, 32768\n"
"buffer_load_dwordx4 v4, s[40:43], s64 offen offset:0 lds\n"
"buffer_load_dwordx4 v5, s[40:43], s64 offen offset:1024 lds\n"
"buffer_load_dwordx4 v6, s[40:43], s64 offen offset:2048 lds\n"
"buffer_load_dwordx4 v7, s[40:43], s64 offen offset:3072 lds\n"
"s_waitcnt vmcnt(4)\n"
"ds_read_b64_tr_b16 a[0:1], v8 offset:0\n"
"ds_read_b64_tr_b16 a[2:3], v8 offset:2048\n"
"ds_read_b64_tr_b16 a[4:5], v9 offset:0\n"
"ds_read_b64_tr_b16 a[6:7], v9 offset:2048\n"
"ds_read_b64_tr_b16 v[128:129], v10 offset:0\n"
"ds_read_b64_tr_b16 v[130:131], v10 offset:2048\n"
"ds_read_b64_tr_b16 v[12:13], v11 offset:0\n"
"ds_read_b64_tr_b16 v[14:15], v11 offset:2048\n"
"s_waitcnt lgkmcnt(0)\n"
"s_add_u32 m0, s66, 0\n"
"s_add_u32 s64, s67, 65536\n"
"buffer_load_dwordx4 v4, s[40:43], s64 offen offset:0 lds\n"
"buffer_load_dwordx4 v5, s[40:43], s64 offen offset:1024 lds\n"
"buffer_load_dwordx4 v6, s[40:43], s64 offen offset:2048 lds\n"
"buffer_load_dwordx4 v7, s[40:43], s64 offen offset:3072 lds\n"
"ds_write_b128 v121, v[12:15] offset:0\n"
"s_waitcnt vmcnt(4)\n"
"ds_read_b64_tr_b16 a[8:9], v8 offset:4096\n"
"ds_read_b64_tr_b16 a[10:11], v8 offset:6144\n"
"ds_read_b64_tr_b16 a[12:13], v9 offset:4096\n"
"ds_read_b64_tr_b16 a[14:15], v9 offset:6144\n"
"ds_read_b64_tr_b16 v[132:133], v10 offset:4096\n"
"ds_read_b64_tr_b16 v[134:135], v10 offset:6144\n"
"ds_read_b64_tr_b16 v[12:13], v11 offset:4096\n"
"ds_read_b64_tr_b16 v[14:15], v11 offset:6144\n"
"s_waitcnt lgkmcnt(0)\n"
"s_add_u32 m0, s66, 4096\n"
"s_add_u32 s64, s67, 98304\n"
"buffer_load_dwordx4 v4, s[40:43], s64 offen offset:0 lds\n"
"buffer_load_dwordx4 v5, s[40:43], s64 offen offset:1024 lds\n"
"buffer_load_dwordx4 v6, s[40:43], s64 offen offset:2048 lds\n"
"buffer_load_dwordx4 v7, s[40:43], s64 offen offset:3072 lds\n"
"ds_write_b128 v121, v[12:15] offset:1024\n"
"s_waitcnt vmcnt(4)\n"
"ds_read_b64_tr_b16 a[16:17], v8 offset:0\n"
"ds_read_b64_tr_b16 a[18:19], v8 offset:2048\n"
"ds_read_b64_tr_b16 a[20:21], v9 offset:0\n"
"ds_read_b64_tr_b16 a[22:23], v9 offset:2048\n"
"ds_read_b64_tr_b16 v[136:137], v10 offset:0\n"
"ds_read_b64_tr_b16 v[138:139], v10 offset:2048\n"
"ds_read_b64_tr_b16 v[12:13], v11 offset:0\n"
"ds_read_b64_tr_b16 v[14:15], v11 offset:2048\n"
"s_waitcnt lgkmcnt(0)\n"
"s_add_u32 m0, s66, 0\n"
"s_add_u32 s64, s67, 131072\n"
"buffer_load_dwordx4 v4, s[40:43], s64 offen offset:0 lds\n"
"buffer_load_dwordx4 v5, s[40:43], s64 offen offset:1024 lds\n"
"buffer_load_dwordx4 v6, s[40:43], s64 offen offset:2048 lds\n"
"buffer_load_dwordx4 v7, s[40:43], s64 offen offset:3072 lds\n"
"ds_write_b128 v121, v[12:15] offset:2048\n"
"s_waitcnt vmcnt(4)\n"
"ds_read_b64_tr_b16 a[24:25], v8 offset:4096\n"
"ds_read_b64_tr_b16 a[26:27], v8 offset:6144\n"
"ds_read_b64_tr_b16 a[28:29], v9 offset:4096\n"
"ds_read_b64_tr_b16 a[30:31], v9 offset:6144\n"
"ds_read_b64_tr_b16 v[140:141], v10 offset:4096\n"
"ds_read_b64_tr_b16 v[142:143], v10 offset:6144\n"
"ds_read_b64_tr_b16 v[12:13], v11 offset:4096\n"
"ds_read_b64_tr_b16 v[14:15], v11 offset:6144\n"
"s_waitcnt lgkmcnt(0)\n"
"s_add_u32 m0, s66, 4096\n"
"s_add_u32 s64, s67, 163840\n"
"buffer_load_dwordx4 v4, s[40:43], s64 offen offset:0 lds\n"
"buffer_load_dwordx4 v5, s[40:43], s64 offen offset:1024 lds\n"
"buffer_load_dwordx4 v6, s[40:43], s64 offen offset:2048 lds\n"
"buffer_load_dwordx4 v7, s[40:43], s64 offen offset:3072 lds\n"
"ds_write_b128 v121, v[12:15] offset:3072\n"
"s_waitcnt vmcnt(4)\n"
"ds_read_b64_tr_b16 a[32:33], v8 offset:0\n"
"ds_read_b64_tr_b16 a[34:35], v8 offset:2048\n"
"ds_read_b64_tr_b16 a[36:37], v9 offset:0\n"
"ds_read_b64_tr_b16 a[38:39], v9 offset:2048\n"
"ds_read_b64_tr_b16 v[144:145], v10 offset:0\n"
"ds_read_b64_tr_b16 v[146:147], v10 offset:2048\n"
"ds_read_b64_tr_b16 v[12:13], v11 offset:0\n"
"ds_read_b64_tr_b16 v[14:15], v11 offset:2048\n"
"s_waitcnt lgkmcnt(0)\n"
"s_add_u32 m0, s66, 0\n"
"s_add_u32 s64, s67, 196608\n"
"buffer_load_dwordx4 v4, s[40:43], s64 offen offset:0 lds\n"
"buffer_load_dwordx4 v5, s[40:43], s64 offen offset:1024 lds\n"
"buffer_load_dwordx4 v6, s[40:43], s64 offen offset:2048 lds\n"
"buffer_load_dwordx4 v7, s[40:43], s64 offen offset:3072 lds\n"
"ds_write_b128 v121, v[12:15] offset:4096\n"
"s_waitcnt vmcnt(4)\n"
"ds_read_b64_tr_b16 a[40:41], v8 offset:4096\n"
"ds_read_b64_tr_b16 a[42:43], v8 offset:6144\n"
"ds_read_b64_tr_b16 a[44:45], v9 offset:4096\n"
"ds_read_b64_tr_b16 a[46:47], v9 offset:6144\n"
"ds_read_b64_tr_b16 v[148:149], v10 offset:4096\n"
"ds_read_b64_tr_b16 v[150:151], v10 offset:6144\n"
"ds_read_b64_tr_b16 v[12:13], v11 offset:4096\n"
"ds_read_b64_tr_b16 v[14:15], v11 offset:6144\n"
"s_waitcnt lgkmcnt(0)\n"
"s_add_u32 m0, s66, 4096\n"
"s_add_u32 s64, s67, 229376\n"
"buffer_load_dwordx4 v4, s[40:43], s64 offen offset:0 lds\n"
"buffer_load_dwordx4 v5, s[40:43], s64 offen offset:1024 lds\n"
"buffer_load_dwordx4 v6, s[40:43], s64 offen offset:2048 lds\n"
"buffer_load_dwordx4 v7, s[40:43], s64 offen offset:3072 lds\n"
"ds_write_b128 v121, v[12:15] offset:5120\n"
"s_waitcnt vmcnt(4)\n"
"ds_read_b64_tr_b16 a[48:49], v8 offset:0\n"
"ds_read_b64_tr_b16 a[50:51], v8 offset:2048\n"
"ds_read_b64_tr_b16 a[52:53], v9 offset:0\n"
"ds_read_b64_tr_b16 a[54:55], v9 offset:2048\n"
"ds_read_b64_tr_b16 v[152:153], v10 offset:0\n"
"ds_read_b64_tr_b16 v[154:155], v10 offset:2048\n"
"ds_read_b64_tr_b16 v[12:13], v11 offset:0\n"
"ds_read_b64_tr_b16 v[14:15], v11 offset:2048\n"
"s_waitcnt lgkmcnt(0)\n"
"s_add_u32 m0, s66, 0\n"
"s_add_u32 s64, s67, 262144\n"
"buffer_load_dwordx4 v4, s[40:43], s64 offen offset:0 lds\n"
"buffer_load_dwordx4 v5, s[40:43], s64 offen offset:1024 lds\n"
"buffer_load_dwordx4 v6, s[40:43], s64 offen offset:2048 lds\n"
"buffer_load_dwordx4 v7, s[40:43], s64 offen offset:3072 lds\n"
"ds_write_b128 v121, v[12:15] offset:6144\n"
"s_waitcnt vmcnt(4)\n"
"ds_read_b64_tr_b16 a[56:57], v8 offset:4096\n"
"ds_read_b64_tr_b16 a[58:59], v8 offset:6144\n"
"ds_read_b64_tr_b16 a[60:61], v9 offset:4096\n"
"ds_read_b64_tr_b16 a[62:63], v9 offset:6144\n"
"ds_read_b64_tr_b16 v[156:157], v10 offset:4096\n"
"ds_read_b64_tr_b16 v[158:159], v10 offset:6144\n"
"ds_read_b64_tr_b16 v[12:13], v11 offset:4096\n"
"ds_read_b64_tr_b16 v[14:15], v11 offset:6144\n"
"s_waitcnt lgkmcnt(0)\n"
"s_add_u32 m0, s66, 4096\n"
"s_add_u32 s64, s67, 294912\n"
"buffer_load_dwordx4 v4, s[40:43], s64 offen offset:0 lds\n"
"buffer_load_dwordx4 v5, s[40:43], s64 offen offset:1024 lds\n"
"buffer_load_dwordx4 v6, s[40:43], s64 offen offset:2048 lds\n"
"buffer_load_dwordx4 v7, s[40:43], s64 offen offset:3072 lds\n"
"ds_write_b128 v121, v[12:15] offset:7168\n"
"s_waitcnt vmcnt(4)\n"
"ds_read_b64_tr_b16 a[64:65], v8 offset:0\n"
"ds_read_b64_tr_b16 a[66:67], v8 offset:2048\n"
"ds_read_b64_tr_b16 a[68:69], v9 offset:0\n"
"ds_read_b64_tr_b16 a[70:71], v9 offset:2048\n"
"ds_read_b64_tr_b16 v[160:161], v10 offset:0\n"
"ds_read_b64_tr_b16 v[162:163], v10 offset:2048\n"
"ds_read_b64_tr_b16 v[12:13], v11 offset:0\n"
"ds_read_b64_tr_b16 v[14:15], v11 offset:2048\n"
"s_waitcnt lgkmcnt(0)\n"
"s_add_u32 m0, s66, 0\n"
"s_add_u32 s64, s67, 327680\n"
"buffer_load_dwordx4 v4, s[40:43], s64 offen offset:0 lds\n"
"buffer_load_dwordx4 v5, s[40:43], s64 offen offset:1024 lds\n"
"buffer_load_dwordx4 v6, s[40:43], s64 offen offset:2048 lds\n"
"buffer_load_dwordx4 v7, s[40:43], s64 offen offset:3072 lds\n"
"ds_write_b128 v121, v[12:15] offset:8192\n"
"s_waitcnt vmcnt(4)\n"
"ds_read_b64_tr_b16 a[72:73], v8 offset:4096\n"
"ds_read_b64_tr_b16 a[74:75], v8 offset:6144\n"
"ds_read_b64_tr_b16 a[76:77], v9 offset:4096\n"
"ds_read_b64_tr_b16 a[78:79], v9 offset:6144\n"
"ds_read_b64_tr_b16 v[164:165], v10 offset:4096\n"
"ds_read_b64_tr_b16 v[166:167], v10 offset:6144\n"
"ds_read_b64_tr_b16 v[12:13], v11 offset:4096\n"
"ds_read_b64_tr_b16 v[14:15], v11 offset:6144\n"
"s_waitcnt lgkmcnt(0)\n"
"s_add_u32 m0, s66, 4096\n"
"s_add_u32 s64, s67, 360448\n"
"buffer_load_dwordx4 v4, s[40:43], s64 offen offset:0 lds\n"
"buffer_load_dwordx4 v5, s[40:43], s64 offen offset:1024 lds\n"
"buffer_load_dwordx4 v6, s[40:43], s64 offen offset:2048 lds\n"
"buffer_load_dwordx4 v7, s[40:43], s64 offen offset:3072 lds\n"
"ds_write_b128 v121, v[12:15] offset:9216\n"
"s_waitcnt vmcnt(4)\n"
"ds_read_b64_tr_b16 a[80:81], v8 offset:0\n"
"ds_read_b64_tr_b16 a[82:83], v8 offset:2048\n"
"ds_read_b64_tr_b16 a[84:85], v9 offset:0\n"
"ds_read_b64_tr_b16 a[86:87], v9 offset:2048\n"
"ds_read_b64_tr_b16 v[168:169], v10 offset:0\n"
"ds_read_b64_tr_b16 v[170:171], v10 offset:2048\n"
"ds_read_b64_tr_b16 v[12:13], v11 offset:0\n"
"ds_read_b64_tr_b16 v[14:15], v11 offset:2048\n"
"s_waitcnt lgkmcnt(0)\n"
"s_add_u32 m0, s66, 0\n"
"s_add_u32 s64, s67, 393216\n"
"buffer_load_dwordx4 v4, s[40:43], s64 offen offset:0 lds\n"
"buffer_load_dwordx4 v5, s[40:43], s64 offen offset:1024 lds\n"
"buffer_load_dwordx4 v6, s[40:43], s64 offen offset:2048 lds\n"
"buffer_load_dwordx4 v7, s[40:43], s64 offen offset:3072 lds\n"
"ds_write_b128 v121, v[12:15] offset:10240\n"
"s_waitcnt vmcnt(4)\n"
"ds_read_b64_tr_b16 a[88:89], v8 offset:4096\n"
"ds_read_b64_tr_b16 a[90:91], v8 offset:6144\n"
"ds_read_b64_tr_b16 a[92:93], v9 offset:4096\n"
"ds_read_b64_tr_b16 a[94:95], v9 offset:6144\n"
"ds_read_b64_tr_b16 v[172:173], v10 offset:4096\n"
"ds_read_b64_tr_b16 v[174:175], v10 offset:6144\n"
"ds_read_b64_tr_b16 v[12:13], v11 offset:4096\n"
"ds_read_b64_tr_b16 v[14:15], v11 offset:6144\n"
"s_waitcnt lgkmcnt(0)\n"
"s_add_u32 m0, s66, 4096\n"
"s_add_u32 s64, s67, 425984\n"
"buffer_load_dwordx4 v4, s[40:43], s64 offen offset:0 lds\n"
"buffer_load_dwordx4 v5, s[40:43], s64 offen offset:1024 lds\n"
"buffer_load_dwordx4 v6, s[40:43], s64 offen offset:2048 lds\n"
"buffer_load_dwordx4 v7, s[40:43], s64 offen offset:3072 lds\n"
"ds_write_b128 v121, v[12:15] offset:11264\n"
"s_waitcnt vmcnt(4)\n"
"ds_read_b64_tr_b16 a[96:97], v8 offset:0\n"
"ds_read_b64_tr_b16 a[98:99], v8 offset:2048\n"
"ds_read_b64_tr_b16 a[100:101], v9 offset:0\n"
"ds_read_b64_tr_b16 a[102:103], v9 offset:2048\n"
"ds_read_b64_tr_b16 v[176:177], v10 offset:0\n"
"ds_read_b64_tr_b16 v[178:179], v10 offset:2048\n"
"ds_read_b64_tr_b16 v[12:13], v11 offset:0\n"
"ds_read_b64_tr_b16 v[14:15], v11 offset:2048\n"
"s_waitcnt lgkmcnt(0)\n"
"s_add_u32 m0, s66, 0\n"
"s_add_u32 s64, s67, 458752\n"
"buffer_load_dwordx4 v4, s[40:43], s64 offen offset:0 lds\n"
"buffer_load_dwordx4 v5, s[40:43], s64 offen offset:1024 lds\n"
"buffer_load_dwordx4 v6, s[40:43], s64 offen offset:2048 lds\n"
"buffer_load_dwordx4 v7, s[40:43], s64 offen offset:3072 lds\n"
"ds_write_b128 v121, v[12:15] offset:12288\n"
"s_waitcnt vmcnt(4)\n"
"ds_read_b64_tr_b16 a[104:105], v8 offset:4096\n"
"ds_read_b64_tr_b16 a[106:107], v8 offset:6144\n"
"ds_read_b64_tr_b16 a[108:109], v9 offset:4096\n"
"ds_read_b64_tr_b16 a[110:111], v9 offset:6144\n"
"ds_read_b64_tr_b16 v[180:181], v10 offset:4096\n"
"ds_read_b64_tr_b16 v[182:183], v10 offset:6144\n"
"ds_read_b64_tr_b16 v[12:13], v11 offset:4096\n"
"ds_read_b64_tr_b16 v[14:15], v11 offset:6144\n"
"s_waitcnt lgkmcnt(0)\n"
"s_add_u32 m0, s66, 4096\n"
"s_add_u32 s64, s67, 491520\n"
"buffer_load_dwordx4 v4, s[40:43], s64 offen offset:0 lds\n"
"buffer_load_dwordx4 v5, s[40:43], s64 offen offset:1024 lds\n"
"buffer_load_dwordx4 v6, s[40:43], s64 offen offset:2048 lds\n"
"buffer_load_dwordx4 v7, s[40:43], s64 offen offset:3072 lds\n"
"ds_write_b128 v121, v[12:15] offset:13312\n"
"s_waitcnt vmcnt(4)\n"
"ds_read_b64_tr_b16 a[112:113], v8 offset:0\n"
"ds_read_b64_tr_b16 a[114:115], v8 offset:2048\n"
"ds_read_b64_tr_b16 a[116:117], v9 offset:0\n"
"ds_read_b64_tr_b16 a[118:119], v9 offset:2048\n"
"ds_read_b64_tr_b16 v[184:185], v10 offset:0\n"
"ds_read_b64_tr_b16 v[186:187], v10 offset:2048\n"
"ds_read_b64_tr_b16 v[12:13], v11 offset:0\n"
"ds_read_b64_tr_b16 v[14:15], v11 offset:2048\n"
"s_waitcnt lgkmcnt(0)\n"
"s_add_u32 m0, s66, 0\n"
"s_add_u32 s64, s67, 524288\n"
"buffer_load_dwordx4 v4, s[40:43], s64 offen offset:0 lds\n"
"buffer_load_dwordx4 v5, s[40:43], s64 offen offset:1024 lds\n"
"buffer_load_dwordx4 v6, s[40:43], s64 offen offset:2048 lds\n"
"buffer_load_dwordx4 v7, s[40:43], s64 offen offset:3072 lds\n"
"ds_write_b128 v121, v[12:15] offset:14336\n"
"s_waitcnt vmcnt(4)\n"
"ds_read_b64_tr_b16 a[120:121], v8 offset:4096\n"
"ds_read_b64_tr_b16 a[122:123], v8 offset:6144\n"
"ds_read_b64_tr_b16 a[124:125], v9 offset:4096\n"
"ds_read_b64_tr_b16 a[126:127], v9 offset:6144\n"
"ds_read_b64_tr_b16 v[188:189], v10 offset:4096\n"
"ds_read_b64_tr_b16 v[190:191], v10 offset:6144\n"
"ds_read_b64_tr_b16 v[12:13], v11 offset:4096\n"
"ds_read_b64_tr_b16 v[14:15], v11 offset:6144\n"
"s_waitcnt lgkmcnt(0)\n"
"s_add_u32 m0, s66, 4096\n"
"s_add_u32 s64, s67, 557056\n"
"buffer_load_dwordx4 v4, s[40:43], s64 offen offset:0 lds\n"
"buffer_load_dwordx4 v5, s[40:43], s64 offen offset:1024 lds\n"
"buffer_load_dwordx4 v6, s[40:43], s64 offen offset:2048 lds\n"
"buffer_load_dwordx4 v7, s[40:43], s64 offen offset:3072 lds\n"
"ds_write_b128 v121, v[12:15] offset:15360\n"
"s_waitcnt vmcnt(4)\n"
"ds_read_b64_tr_b16 a[128:129], v8 offset:0\n"
"ds_read_b64_tr_b16 a[130:131], v8 offset:2048\n"
"ds_read_b64_tr_b16 a[132:133], v9 offset:0\n"
"ds_read_b64_tr_b16 a[134:135], v9 offset:2048\n"
"ds_read_b64_tr_b16 v[192:193], v10 offset:0\n"
"ds_read_b64_tr_b16 v[194:195], v10 offset:2048\n"
"ds_read_b64_tr_b16 v[12:13], v11 offset:0\n"
"ds_read_b64_tr_b16 v[14:15], v11 offset:2048\n"
"s_waitcnt lgkmcnt(0)\n"
"s_add_u32 m0, s66, 0\n"
"s_add_u32 s64, s67, 589824\n"
"buffer_load_dwordx4 v4, s[40:43], s64 offen offset:0 lds\n"
"buffer_load_dwordx4 v5, s[40:43], s64 offen offset:1024 lds\n"
"buffer_load_dwordx4 v6, s[40:43], s64 offen offset:2048 lds\n"
"buffer_load_dwordx4 v7, s[40:43], s64 offen offset:3072 lds\n"
"ds_write_b128 v121, v[12:15] offset:16384\n"
"s_waitcnt vmcnt(4)\n"
"ds_read_b64_tr_b16 a[136:137], v8 offset:4096\n"
"ds_read_b64_tr_b16 a[138:139], v8 offset:6144\n"
"ds_read_b64_tr_b16 a[140:141], v9 offset:4096\n"
"ds_read_b64_tr_b16 a[142:143], v9 offset:6144\n"
"ds_read_b64_tr_b16 v[196:197], v10 offset:4096\n"
"ds_read_b64_tr_b16 v[198:199], v10 offset:6144\n"
"ds_read_b64_tr_b16 v[12:13], v11 offset:4096\n"
"ds_read_b64_tr_b16 v[14:15], v11 offset:6144\n"
"s_waitcnt lgkmcnt(0)\n"
"s_add_u32 m0, s66, 4096\n"
"s_add_u32 s64, s67, 622592\n"
"buffer_load_dwordx4 v4, s[40:43], s64 offen offset:0 lds\n"
"buffer_load_dwordx4 v5, s[40:43], s64 offen offset:1024 lds\n"
"buffer_load_dwordx4 v6, s[40:43], s64 offen offset:2048 lds\n"
"buffer_load_dwordx4 v7, s[40:43], s64 offen offset:3072 lds\n"
"ds_write_b128 v121, v[12:15] offset:17408\n"
"s_waitcnt vmcnt(4)\n"
"ds_read_b64_tr_b16 a[144:145], v8 offset:0\n"
"ds_read_b64_tr_b16 a[146:147], v8 offset:2048\n"
"ds_read_b64_tr_b16 a[148:149], v9 offset:0\n"
"ds_read_b64_tr_b16 a[150:151], v9 offset:2048\n"
"ds_read_b64_tr_b16 v[200:201], v10 offset:0\n"
"ds_read_b64_tr_b16 v[202:203], v10 offset:2048\n"
"ds_read_b64_tr_b16 v[12:13], v11 offset:0\n"
"ds_read_b64_tr_b16 v[14:15], v11 offset:2048\n"
"s_waitcnt lgkmcnt(0)\n"
"s_add_u32 m0, s66, 0\n"
"s_add_u32 s64, s67, 655360\n"
"buffer_load_dwordx4 v4, s[40:43], s64 offen offset:0 lds\n"
"buffer_load_dwordx4 v5, s[40:43], s64 offen offset:1024 lds\n"
"buffer_load_dwordx4 v6, s[40:43], s64 offen offset:2048 lds\n"
"buffer_load_dwordx4 v7, s[40:43], s64 offen offset:3072 lds\n"
"ds_write_b128 v121, v[12:15] offset:18432\n"
"s_waitcnt vmcnt(4)\n"
"ds_read_b64_tr_b16 a[152:153], v8 offset:4096\n"
"ds_read_b64_tr_b16 a[154:155], v8 offset:6144\n"
"ds_read_b64_tr_b16 a[156:157], v9 offset:4096\n"
"ds_read_b64_tr_b16 a[158:159], v9 offset:6144\n"
"ds_read_b64_tr_b16 v[204:205], v10 offset:4096\n"
"ds_read_b64_tr_b16 v[206:207], v10 offset:6144\n"
"ds_read_b64_tr_b16 v[12:13], v11 offset:4096\n"
"ds_read_b64_tr_b16 v[14:15], v11 offset:6144\n"
"s_waitcnt lgkmcnt(0)\n"
"s_add_u32 m0, s66, 4096\n"
"s_add_u32 s64, s67, 688128\n"
"buffer_load_dwordx4 v4, s[40:43], s64 offen offset:0 lds\n"
"buffer_load_dwordx4 v5, s[40:43], s64 offen offset:1024 lds\n"
"buffer_load_dwordx4 v6, s[40:43], s64 offen offset:2048 lds\n"
"buffer_load_dwordx4 v7, s[40:43], s64 offen offset:3072 lds\n"
"ds_write_b128 v121, v[12:15] offset:19456\n"
"s_waitcnt vmcnt(4)\n"
"ds_read_b64_tr_b16 a[160:161], v8 offset:0\n"
"ds_read_b64_tr_b16 a[162:163], v8 offset:2048\n"
"ds_read_b64_tr_b16 a[164:165], v9 offset:0\n"
"ds_read_b64_tr_b16 a[166:167], v9 offset:2048\n"
"ds_read_b64_tr_b16 v[208:209], v10 offset:0\n"
"ds_read_b64_tr_b16 v[210:211], v10 offset:2048\n"
"ds_read_b64_tr_b16 v[12:13], v11 offset:0\n"
"ds_read_b64_tr_b16 v[14:15], v11 offset:2048\n"
"s_waitcnt lgkmcnt(0)\n"
"s_add_u32 m0, s66, 0\n"
"s_add_u32 s64, s67, 720896\n"
"buffer_load_dwordx4 v4, s[40:43], s64 offen offset:0 lds\n"
"buffer_load_dwordx4 v5, s[40:43], s64 offen offset:1024 lds\n"
"buffer_load_dwordx4 v6, s[40:43], s64 offen offset:2048 lds\n"
"buffer_load_dwordx4 v7, s[40:43], s64 offen offset:3072 lds\n"
"ds_write_b128 v121, v[12:15] offset:20480\n"
"s_waitcnt vmcnt(4)\n"
"ds_read_b64_tr_b16 a[168:169], v8 offset:4096\n"
"ds_read_b64_tr_b16 a[170:171], v8 offset:6144\n"
"ds_read_b64_tr_b16 a[172:173], v9 offset:4096\n"
"ds_read_b64_tr_b16 a[174:175], v9 offset:6144\n"
"ds_read_b64_tr_b16 v[212:213], v10 offset:4096\n"
"ds_read_b64_tr_b16 v[214:215], v10 offset:6144\n"
"ds_read_b64_tr_b16 v[12:13], v11 offset:4096\n"
"ds_read_b64_tr_b16 v[14:15], v11 offset:6144\n"
"s_waitcnt lgkmcnt(0)\n"
"s_add_u32 m0, s66, 4096\n"
"s_add_u32 s64, s67, 753664\n"
"buffer_load_dwordx4 v4, s[40:43], s64 offen offset:0 lds\n"
"buffer_load_dwordx4 v5, s[40:43], s64 offen offset:1024 lds\n"
"buffer_load_dwordx4 v6, s[40:43], s64 offen offset:2048 lds\n"
"buffer_load_dwordx4 v7, s[40:43], s64 offen offset:3072 lds\n"
"ds_write_b128 v121, v[12:15] offset:21504\n"
"s_waitcnt vmcnt(4)\n"
"ds_read_b64_tr_b16 a[176:177], v8 offset:0\n"
"ds_read_b64_tr_b16 a[178:179], v8 offset:2048\n"
"ds_read_b64_tr_b16 a[180:181], v9 offset:0\n"
"ds_read_b64_tr_b16 a[182:183], v9 offset:2048\n"
"ds_read_b64_tr_b16 v[216:217], v10 offset:0\n"
"ds_read_b64_tr_b16 v[218:219], v10 offset:2048\n"
"ds_read_b64_tr_b16 v[12:13], v11 offset:0\n"
"ds_read_b64_tr_b16 v[14:15], v11 offset:2048\n"
"s_waitcnt lgkmcnt(0)\n"
"s_add_u32 m0, s66, 0\n"
"s_add_u32 s64, s67, 786432\n"
"buffer_load_dwordx4 v4, s[40:43], s64 offen offset:0 lds\n"
"buffer_load_dwordx4 v5, s[40:43], s64 offen offset:1024 lds\n"
"buffer_load_dwordx4 v6, s[40:43], s64 offen offset:2048 lds\n"
"buffer_load_dwordx4 v7, s[40:43], s64 offen offset:3072 lds\n"
"ds_write_b128 v121, v[12:15] offset:22528\n"
"s_waitcnt vmcnt(4)\n"
"ds_read_b64_tr_b16 a[184:185], v8 offset:4096\n"
"ds_read_b64_tr_b16 a[186:187], v8 offset:6144\n"
"ds_read_b64_tr_b16 a[188:189], v9 offset:4096\n"
"ds_read_b64_tr_b16 a[190:191], v9 offset:6144\n"
"ds_read_b64_tr_b16 v[220:221], v10 offset:4096\n"
"ds_read_b64_tr_b16 v[222:223], v10 offset:6144\n"
"ds_read_b64_tr_b16 v[12:13], v11 offset:4096\n"
"ds_read_b64_tr_b16 v[14:15], v11 offset:6144\n"
"s_waitcnt lgkmcnt(0)\n"
"s_add_u32 m0, s66, 4096\n"
"s_add_u32 s64, s67, 819200\n"
"buffer_load_dwordx4 v4, s[40:43], s64 offen offset:0 lds\n"
"buffer_load_dwordx4 v5, s[40:43], s64 offen offset:1024 lds\n"
"buffer_load_dwordx4 v6, s[40:43], s64 offen offset:2048 lds\n"
"buffer_load_dwordx4 v7, s[40:43], s64 offen offset:3072 lds\n"
"ds_write_b128 v121, v[12:15] offset:23552\n"
"s_waitcnt vmcnt(4)\n"
"ds_read_b64_tr_b16 a[192:193], v8 offset:0\n"
"ds_read_b64_tr_b16 a[194:195], v8 offset:2048\n"
"ds_read_b64_tr_b16 a[196:197], v9 offset:0\n"
"ds_read_b64_tr_b16 a[198:199], v9 offset:2048\n"
"ds_read_b64_tr_b16 v[224:225], v10 offset:0\n"
"ds_read_b64_tr_b16 v[226:227], v10 offset:2048\n"
"ds_read_b64_tr_b16 v[12:13], v11 offset:0\n"
"ds_read_b64_tr_b16 v[14:15], v11 offset:2048\n"
"s_waitcnt lgkmcnt(0)\n"
"s_add_u32 m0, s66, 0\n"
"s_add_u32 s64, s67, 851968\n"
"buffer_load_dwordx4 v4, s[40:43], s64 offen offset:0 lds\n"
"buffer_load_dwordx4 v5, s[40:43], s64 offen offset:1024 lds\n"
"buffer_load_dwordx4 v6, s[40:43], s64 offen offset:2048 lds\n"
"buffer_load_dwordx4 v7, s[40:43], s64 offen offset:3072 lds\n"
"ds_write_b128 v121, v[12:15] offset:24576\n"
"s_waitcnt vmcnt(4)\n"
"ds_read_b64_tr_b16 a[200:201], v8 offset:4096\n"
"ds_read_b64_tr_b16 a[202:203], v8 offset:6144\n"
"ds_read_b64_tr_b16 a[204:205], v9 offset:4096\n"
"ds_read_b64_tr_b16 a[206:207], v9 offset:6144\n"
"ds_read_b64_tr_b16 v[228:229], v10 offset:4096\n"
"ds_read_b64_tr_b16 v[230:231], v10 offset:6144\n"
"ds_read_b64_tr_b16 v[12:13], v11 offset:4096\n"
"ds_read_b64_tr_b16 v[14:15], v11 offset:6144\n"
"s_waitcnt lgkmcnt(0)\n"
"s_add_u32 m0, s66, 4096\n"
"s_add_u32 s64, s67, 884736\n"
"buffer_load_dwordx4 v4, s[40:43], s64 offen offset:0 lds\n"
"buffer_load_dwordx4 v5, s[40:43], s64 offen offset:1024 lds\n"
"buffer_load_dwordx4 v6, s[40:43], s64 offen offset:2048 lds\n"
"buffer_load_dwordx4 v7, s[40:43], s64 offen offset:3072 lds\n"
"ds_write_b128 v121, v[12:15] offset:25600\n"
"s_waitcnt vmcnt(4)\n"
"ds_read_b64_tr_b16 a[208:209], v8 offset:0\n"
"ds_read_b64_tr_b16 a[210:211], v8 offset:2048\n"
"ds_read_b64_tr_b16 a[212:213], v9 offset:0\n"
"ds_read_b64_tr_b16 a[214:215], v9 offset:2048\n"
"ds_read_b64_tr_b16 v[232:233], v10 offset:0\n"
"ds_read_b64_tr_b16 v[234:235], v10 offset:2048\n"
"ds_read_b64_tr_b16 v[12:13], v11 offset:0\n"
"ds_read_b64_tr_b16 v[14:15], v11 offset:2048\n"
"s_waitcnt lgkmcnt(0)\n"
"s_add_u32 m0, s66, 0\n"
"s_add_u32 s64, s67, 917504\n"
"buffer_load_dwordx4 v4, s[40:43], s64 offen offset:0 lds\n"
"buffer_load_dwordx4 v5, s[40:43], s64 offen offset:1024 lds\n"
"buffer_load_dwordx4 v6, s[40:43], s64 offen offset:2048 lds\n"
"buffer_load_dwordx4 v7, s[40:43], s64 offen offset:3072 lds\n"
"ds_write_b128 v121, v[12:15] offset:26624\n"
"s_waitcnt vmcnt(4)\n"
"ds_read_b64_tr_b16 a[216:217], v8 offset:4096\n"
"ds_read_b64_tr_b16 a[218:219], v8 offset:6144\n"
"ds_read_b64_tr_b16 a[220:221], v9 offset:4096\n"
"ds_read_b64_tr_b16 a[222:223], v9 offset:6144\n"
"ds_read_b64_tr_b16 v[236:237], v10 offset:4096\n"
"ds_read_b64_tr_b16 v[238:239], v10 offset:6144\n"
"ds_read_b64_tr_b16 v[12:13], v11 offset:4096\n"
"ds_read_b64_tr_b16 v[14:15], v11 offset:6144\n"
"s_waitcnt lgkmcnt(0)\n"
"s_add_u32 m0, s66, 4096\n"
"s_add_u32 s64, s67, 950272\n"
"buffer_load_dwordx4 v4, s[40:43], s64 offen offset:0 lds\n"
"buffer_load_dwordx4 v5, s[40:43], s64 offen offset:1024 lds\n"
"buffer_load_dwordx4 v6, s[40:43], s64 offen offset:2048 lds\n"
"buffer_load_dwordx4 v7, s[40:43], s64 offen offset:3072 lds\n"
"ds_write_b128 v121, v[12:15] offset:27648\n"
"s_waitcnt vmcnt(4)\n"
"ds_read_b64_tr_b16 a[224:225], v8 offset:0\n"
"ds_read_b64_tr_b16 a[226:227], v8 offset:2048\n"
"ds_read_b64_tr_b16 a[228:229], v9 offset:0\n"
"ds_read_b64_tr_b16 a[230:231], v9 offset:2048\n"
"ds_read_b64_tr_b16 v[240:241], v10 offset:0\n"
"ds_read_b64_tr_b16 v[242:243], v10 offset:2048\n"
"ds_read_b64_tr_b16 v[12:13], v11 offset:0\n"
"ds_read_b64_tr_b16 v[14:15], v11 offset:2048\n"
"s_waitcnt lgkmcnt(0)\n"
"s_add_u32 m0, s66, 0\n"
"s_add_u32 s64, s67, 983040\n"
"buffer_load_dwordx4 v4, s[40:43], s64 offen offset:0 lds\n"
"buffer_load_dwordx4 v5, s[40:43], s64 offen offset:1024 lds\n"
"buffer_load_dwordx4 v6, s[40:43], s64 offen offset:2048 lds\n"
"buffer_load_dwordx4 v7, s[40:43], s64 offen offset:3072 lds\n"
"ds_write_b128 v121, v[12:15] offset:28672\n"
"s_waitcnt vmcnt(4)\n"
"ds_read_b64_tr_b16 a[232:233], v8 offset:4096\n"
"ds_read_b64_tr_b16 a[234:235], v8 offset:6144\n"
"ds_read_b64_tr_b16 a[236:237], v9 offset:4096\n"
"ds_read_b64_tr_b16 a[238:239], v9 offset:6144\n"
"ds_read_b64_tr_b16 v[244:245], v10 offset:4096\n"
"ds_read_b64_tr_b16 v[246:247], v10 offset:6144\n"
"ds_read_b64_tr_b16 v[12:13], v11 offset:4096\n"
"ds_read_b64_tr_b16 v[14:15], v11 offset:6144\n"
"s_waitcnt lgkmcnt(0)\n"
"s_add_u32 m0, s66, 4096\n"
"s_add_u32 s64, s67, 1015808\n"
"buffer_load_dwordx4 v4, s[40:43], s64 offen offset:0 lds\n"
"buffer_load_dwordx4 v5, s[40:43], s64 offen offset:1024 lds\n"
"buffer_load_dwordx4 v6, s[40:43], s64 offen offset:2048 lds\n"
"buffer_load_dwordx4 v7, s[40:43], s64 offen offset:3072 lds\n"
"ds_write_b128 v121, v[12:15] offset:29696\n"
"s_waitcnt vmcnt(4)\n"
"ds_read_b64_tr_b16 a[240:241], v8 offset:0\n"
"ds_read_b64_tr_b16 a[242:243], v8 offset:2048\n"
"ds_read_b64_tr_b16 a[244:245], v9 offset:0\n"
"ds_read_b64_tr_b16 a[246:247], v9 offset:2048\n"
"ds_read_b64_tr_b16 v[248:249], v10 offset:0\n"
"ds_read_b64_tr_b16 v[250:251], v10 offset:2048\n"
"ds_read_b64_tr_b16 v[12:13], v11 offset:0\n"
"ds_read_b64_tr_b16 v[14:15], v11 offset:2048\n"
"s_waitcnt lgkmcnt(0)\n"
"ds_write_b128 v121, v[12:15] offset:30720\n"
"s_waitcnt vmcnt(0)\n"
"ds_read_b64_tr_b16 a[248:249], v8 offset:4096\n"
"ds_read_b64_tr_b16 a[250:251], v8 offset:6144\n"
"ds_read_b64_tr_b16 a[252:253], v9 offset:4096\n"
"ds_read_b64_tr_b16 a[254:255], v9 offset:6144\n"
"ds_read_b64_tr_b16 v[252:253], v10 offset:4096\n"
"ds_read_b64_tr_b16 v[254:255], v10 offset:6144\n"
"ds_read_b64_tr_b16 v[12:13], v11 offset:4096\n"
"ds_read_b64_tr_b16 v[14:15], v11 offset:6144\n"
"s_waitcnt lgkmcnt(0)\n"
"ds_write_b128 v121, v[12:15] offset:31744\n"
"v_lshlrev_b32 v100, 3, v119\n"
"s_mov_b32 s74, 3146752\n"
"buffer_load_dwordx2 v[96:97], v100, s[60:63], s74 offen sc1\n"
"buffer_load_dwordx2 v[98:99], v100, s[60:63], s74 offen offset:1024 sc1\n"
"s_waitcnt vmcnt(0)\n"
"s_mov_b32 s78, 16384\n"
".Law89_%=:\n"
"v_xor_b32 v96, s34, v96\n"
"v_xor_b32 v97, s35, v97\n"
"v_and_or_b32 v96, v97, -4, v96\n"
"v_cmp_ne_u32 vcc, 0, v96\n"
"v_cmp_ne_u64 s[64:65], s[34:35], v[98:99]\n"
"s_nop 0\n"
"s_or_b64 vcc, vcc, s[64:65]\n"
"s_cbranch_vccz .Laok90_%=\n"
"s_sub_u32 s78, s78, 1\n"
"s_cbranch_scc1 .Lfail5_%=\n"
"s_sleep 8\n"
"buffer_load_dwordx2 v[96:97], v100, s[60:63], s74 offen sc1\n"
"buffer_load_dwordx2 v[98:99], v100, s[60:63], s74 offen offset:1024 sc1\n"
"s_waitcnt vmcnt(0)\n"
"s_branch .Law89_%=\n"
".Laok90_%=:\n"
"s_mov_b32 s77, 0\n"
"v_cmp_lt_u32 vcc, 1, v97\n"
"v_and_b32 v97, 1, v97\n"
"s_and_b32 s64, vcc_lo, 0xf00ff00f\n"
"s_and_b32 s65, vcc_hi, 0xf00ff00f\n"
"s_bcnt1_i32_b64 s64, s[64:65]\n"
"s_cmp_ge_u32 s64, 16\n"
"s_addc_u32 s77, s77, s77\n"
"s_and_b32 s64, vcc_lo, 0xff00ff0\n"
"s_and_b32 s65, vcc_hi, 0xff00ff0\n"
"s_bcnt1_i32_b64 s64, s[64:65]\n"
"s_cmp_ge_u32 s64, 16\n"
"s_addc_u32 s77, s77, s77\n"
"v_cmp_ne_u32 vcc, 0, v97\n"
"s_nop 0\n"
"s_and_b32 s64, vcc_lo, 0xffffffff\n"
"s_and_b32 s65, vcc_hi, 0xffffffff\n"
"s_bcnt1_i32_b64 s64, s[64:65]\n"
"s_cmp_ge_u32 s64, 32\n"
"s_addc_u32 s77, s77, s77\n"
"v_lshrrev_b32 v0, 1, v119\n"
"v_lshlrev_b32 v0, 7, v0\n"
"v_and_b32 v1, 1, v119\n"
"v_lshl_add_u32 v0, v1, 4, v0\n"
"s_lshl_b32 s64, s37, 10\n"
"v_add_u32 v0, s64, v0\n"
"s_lshl_b32 s64, s38, 7\n"
"s_lshl_b32 s65, s37, 4\n"
"s_add_u32 s64, s64, s65\n"
"s_add_u32 s64, s64, 6144\n"
"v_mov_b32 v1, s64\n"
"v_cmp_gt_u32 vcc, 16, v119\n"
"s_nop 1\n"
"v_cndmask_b32 v115, v1, v0, vcc\n"
"s_lshl_b32 s9, s37, 16\n"
"s_add_u32 s9, s9, 8388608\n"
"v_and_b32 v0, 15, v119\n"
"v_lshrrev_b32 v1, 4, v119\n"
"v_xor_b32 v2, v0, v1\n"
"v_lshlrev_b32 v2, 4, v2\n"
"v_lshl_add_u32 v2, v1, 8, v2\n"
"s_lshl_b32 s65, s37, 12\n"
"v_add_u32 v122, s65, v2\n"
"s_lshl_b32 s65, s37, 2\n"
"v_add_u32 v1, s65, v1\n"
"v_xor_b32 v2, v0, v1\n"
"v_lshlrev_b32 v2, 4, v2\n"
"v_lshl_add_u32 v117, v0, 8, v2\n"
"v_lshlrev_b32 v1, 10, v1\n"
"v_lshl_add_u32 v1, v0, 3, v1\n"
"s_lshl_b32 s65, s38, 7\n"
"v_add_u32 v116, s65, v1\n"
"s_lshl_b32 s64, s38, 2\n"
"s_add_u32 s64, s64, s37\n"
"s_add_u32 s64, s64, 192\n"
"s_mov_b32 s8, s39\n"
"s_waitcnt lgkmcnt(0)\n"
"s_barrier\n"
"v_xor_b32 v123, 64, v122\n"
"v_xor_b32 v124, 128, v122\n"
"v_xor_b32 v125, 192, v122\n"
"s_lshr_b32 s74, s64, 3\n"
"s_lshl_b32 s74, s74, 13\n"
"s_lshl_b32 s14, s64, 10\n"
"s_add_u32 s14, s14, s74\n"
"s_add_u32 s14, s14, 8388608\n"
".Ldtop91_%=:\n"
"s_lshr_b32 s65, s8, 1\n"
"s_lshl_b32 s12, s65, 1\n"
"s_and_b32 s74, s77, 1\n"
"s_add_u32 s12, s12, s74\n"
"s_lshl_b32 s12, s12, 13\n"
"s_add_u32 s12, s12, 6291456\n"
"s_lshr_b32 s67, s8, 2\n"
"s_xnor_b32 s67, s67, s77\n"
"s_and_b32 s67, s67, 1\n"
"s_lshl_b32 s67, s67, 13\n"
"s_lshl_b32 s74, s8, 19\n"
"s_add_u32 s67, s67, s74\n"
"s_and_b32 s64, s8, 1\n"
"s_lshl_b32 s64, s64, 3\n"
"s_add_u32 s12, s12, s64\n"
"s_add_u32 s68, s67, s14\n"
"s_add_u32 s66, s67, s9\n"
"s_add_u32 s67, s66, 524288\n"
"s_add_u32 s69, s68, 524288\n"
"s_lshl_b32 s13, s8, 14\n"
"s_mov_b32 s78, 16384\n"
".Ldw92_%=:\n"
"buffer_load_dwordx2 v[112:113], v115, s[60:63], s12 offen sc1\n"
"s_waitcnt vmcnt(0)\n"
"v_cmp_ne_u64 vcc, s[34:35], v[112:113]\n"
"s_cbranch_vccz .Ldwok93_%=\n"
"s_sub_u32 s78, s78, 1\n"
"s_cbranch_scc1 .Lfail5_%=\n"
"s_sleep 1\n"
"s_branch .Ldw92_%=\n"
".Ldwok93_%=:\n"
"buffer_load_dwordx4 v[0:3], v114, s[60:63], s66 offen offset:0 sc1\n"
"buffer_load_dwordx4 v[4:7], v114, s[60:63], s66 offen offset:1024 sc1\n"
"buffer_load_dwordx4 v[8:11], v114, s[60:63], s66 offen offset:2048 sc1\n"
"buffer_load_dwordx4 v[12:15], v114, s[60:63], s66 offen offset:3072 sc1\n"
"s_add_u32 s66, s66, 4096\n"
"buffer_load_dwordx4 v[16:19], v114, s[60:63], s66 offen offset:0 sc1\n"
"buffer_load_dwordx4 v[20:23], v114, s[60:63], s66 offen offset:1024 sc1\n"
"buffer_load_dwordx4 v[24:27], v114, s[60:63], s66 offen offset:2048 sc1\n"
"buffer_load_dwordx4 v[28:31], v114, s[60:63], s66 offen offset:3072 sc1\n"
"s_add_u32 s66, s66, 12288\n"
"buffer_load_dwordx4 v[32:35], v114, s[60:63], s66 offen offset:0 sc1\n"
"buffer_load_dwordx4 v[36:39], v114, s[60:63], s66 offen offset:1024 sc1\n"
"buffer_load_dwordx4 v[40:43], v114, s[60:63], s66 offen offset:2048 sc1\n"
"buffer_load_dwordx4 v[44:47], v114, s[60:63], s66 offen offset:3072 sc1\n"
"s_add_u32 s66, s66, 4096\n"
"buffer_load_dwordx4 v[48:51], v114, s[60:63], s66 offen offset:0 sc1\n"
"buffer_load_dwordx4 v[52:55], v114, s[60:63], s66 offen offset:1024 sc1\n"
"buffer_load_dwordx4 v[56:59], v114, s[60:63], s66 offen offset:2048 sc1\n"
"buffer_load_dwordx4 v[60:63], v114, s[60:63], s66 offen offset:3072 sc1\n"
"s_add_u32 s66, s66, 12288\n"
"buffer_load_dwordx4 v[64:67], v114, s[60:63], s66 offen offset:0 sc1\n"
"buffer_load_dwordx4 v[68:71], v114, s[60:63], s66 offen offset:1024 sc1\n"
"buffer_load_dwordx4 v[72:75], v114, s[60:63], s66 offen offset:2048 sc1\n"
"buffer_load_dwordx4 v[76:79], v114, s[60:63], s66 offen offset:3072 sc1\n"
"buffer_load_dwordx4 v[108:111], v114, s[60:63], s68 offen sc1\n"
"ds_read_b128 v[96:99], v121 offset:0\n"
"ds_read_b128 v[100:103], v121 offset:1024\n"
"s_waitcnt vmcnt(20)\n"
"v_mfma_f32_16x16x32_bf16 v[80:83], a[0:3], v[0:3], 0\n"
"ds_read_b128 v[104:107], v121 offset:2048\n"
"v_mfma_f32_16x16x32_bf16 v[84:87], a[4:7], v[0:3], 0\n"
"v_mfma_f32_16x16x32_bf16 v[88:91], v[128:131], v[0:3], 0\n"
"s_waitcnt lgkmcnt(2)\n"
"v_mfma_f32_16x16x32_bf16 v[92:95], v[96:99], v[0:3], 0\n"
"s_add_u32 s66, s66, 4096\n"
"buffer_load_dwordx4 v[0:3], v114, s[60:63], s66 offen offset:0 sc1\n"
"s_waitcnt vmcnt(20)\n"
"v_mfma_f32_16x16x32_bf16 v[80:83], a[8:11], v[4:7], v[80:83]\n"
"ds_read_b128 v[96:99], v121 offset:3072\n"
"v_mfma_f32_16x16x32_bf16 v[84:87], a[12:15], v[4:7], v[84:87]\n"
"v_mfma_f32_16x16x32_bf16 v[88:91], v[132:135], v[4:7], v[88:91]\n"
"s_waitcnt lgkmcnt(2)\n"
"v_mfma_f32_16x16x32_bf16 v[92:95], v[100:103], v[4:7], v[92:95]\n"
"buffer_load_dwordx4 v[4:7], v114, s[60:63], s66 offen offset:1024 sc1\n"
"s_waitcnt vmcnt(20)\n"
"v_mfma_f32_16x16x32_bf16 v[80:83], a[16:19], v[8:11], v[80:83]\n"
"ds_read_b128 v[100:103], v121 offset:4096\n"
"v_mfma_f32_16x16x32_bf16 v[84:87], a[20:23], v[8:11], v[84:87]\n"
"v_mfma_f32_16x16x32_bf16 v[88:91], v[136:139], v[8:11], v[88:91]\n"
"s_waitcnt lgkmcnt(2)\n"
"v_mfma_f32_16x16x32_bf16 v[92:95], v[104:107], v[8:11], v[92:95]\n"
"buffer_load_dwordx4 v[8:11], v114, s[60:63], s66 offen offset:2048 sc1\n"
"s_waitcnt vmcnt(20)\n"
"v_mfma_f32_16x16x32_bf16 v[80:83], a[24:27], v[12:15], v[80:83]\n"
"ds_read_b128 v[104:107], v121 offset:5120\n"
"v_mfma_f32_16x16x32_bf16 v[84:87], a[28:31], v[12:15], v[84:87]\n"
"v_mfma_f32_16x16x32_bf16 v[88:91], v[140:143], v[12:15], v[88:91]\n"
"s_waitcnt lgkmcnt(2)\n"
"v_mfma_f32_16x16x32_bf16 v[92:95], v[96:99], v[12:15], v[92:95]\n"
"buffer_load_dwordx4 v[12:15], v114, s[60:63], s66 offen offset:3072 sc1\n"
"s_waitcnt vmcnt(20)\n"
"v_mfma_f32_16x16x32_bf16 v[80:83], a[32:35], v[16:19], v[80:83]\n"
"ds_read_b128 v[96:99], v121 offset:6144\n"
"v_mfma_f32_16x16x32_bf16 v[84:87], a[36:39], v[16:19], v[84:87]\n"
"v_mfma_f32_16x16x32_bf16 v[88:91], v[144:147], v[16:19], v[88:91]\n"
"s_waitcnt lgkmcnt(2)\n"
"v_mfma_f32_16x16x32_bf16 v[92:95], v[100:103], v[16:19], v[92:95]\n"
"s_add_u32 s66, s66, 12288\n"
"buffer_load_dwordx4 v[16:19], v114, s[60:63], s66 offen offset:0 sc1\n"
"s_waitcnt vmcnt(20)\n"
"v_mfma_f32_16x16x32_bf16 v[80:83], a[40:43], v[20:23], v[80:83]\n"
"ds_read_b128 v[100:103], v121 offset:7168\n"
"v_mfma_f32_16x16x32_bf16 v[84:87], a[44:47], v[20:23], v[84:87]\n"
"v_mfma_f32_16x16x32_bf16 v[88:91], v[148:151], v[20:23], v[88:91]\n"
"s_waitcnt lgkmcnt(2)\n"
"v_mfma_f32_16x16x32_bf16 v[92:95], v[104:107], v[20:23], v[92:95]\n"
"buffer_load_dwordx4 v[20:23], v114, s[60:63], s66 offen offset:1024 sc1\n"
"s_waitcnt vmcnt(20)\n"
"v_mfma_f32_16x16x32_bf16 v[80:83], a[48:51], v[24:27], v[80:83]\n"
"ds_read_b128 v[104:107], v121 offset:8192\n"
"v_mfma_f32_16x16x32_bf16 v[84:87], a[52:55], v[24:27], v[84:87]\n"
"v_mfma_f32_16x16x32_bf16 v[88:91], v[152:155], v[24:27], v[88:91]\n"
"s_waitcnt lgkmcnt(2)\n"
"v_mfma_f32_16x16x32_bf16 v[92:95], v[96:99], v[24:27], v[92:95]\n"
"buffer_load_dwordx4 v[24:27], v114, s[60:63], s66 offen offset:2048 sc1\n"
"s_waitcnt vmcnt(20)\n"
"v_mfma_f32_16x16x32_bf16 v[80:83], a[56:59], v[28:31], v[80:83]\n"
"ds_read_b128 v[96:99], v121 offset:9216\n"
"v_mfma_f32_16x16x32_bf16 v[84:87], a[60:63], v[28:31], v[84:87]\n"
"v_mfma_f32_16x16x32_bf16 v[88:91], v[156:159], v[28:31], v[88:91]\n"
"s_waitcnt lgkmcnt(2)\n"
"v_mfma_f32_16x16x32_bf16 v[92:95], v[100:103], v[28:31], v[92:95]\n"
"buffer_load_dwordx4 v[28:31], v114, s[60:63], s66 offen offset:3072 sc1\n"
"s_waitcnt vmcnt(20)\n"
"v_mfma_f32_16x16x32_bf16 v[80:83], a[64:67], v[32:35], v[80:83]\n"
"ds_read_b128 v[100:103], v121 offset:10240\n"
"v_mfma_f32_16x16x32_bf16 v[84:87], a[68:71], v[32:35], v[84:87]\n"
"v_mfma_f32_16x16x32_bf16 v[88:91], v[160:163], v[32:35], v[88:91]\n"
"s_waitcnt lgkmcnt(2)\n"
"v_mfma_f32_16x16x32_bf16 v[92:95], v[104:107], v[32:35], v[92:95]\n"
"s_add_u32 s66, s66, 4096\n"
"buffer_load_dwordx4 v[32:35], v114, s[60:63], s66 offen offset:0 sc1\n"
"s_waitcnt vmcnt(20)\n"
"v_mfma_f32_16x16x32_bf16 v[80:83], a[72:75], v[36:39], v[80:83]\n"
"ds_read_b128 v[104:107], v121 offset:11264\n"
"v_mfma_f32_16x16x32_bf16 v[84:87], a[76:79], v[36:39], v[84:87]\n"
"v_mfma_f32_16x16x32_bf16 v[88:91], v[164:167], v[36:39], v[88:91]\n"
"s_waitcnt lgkmcnt(2)\n"
"v_mfma_f32_16x16x32_bf16 v[92:95], v[96:99], v[36:39], v[92:95]\n"
"buffer_load_dwordx4 v[36:39], v114, s[60:63], s66 offen offset:1024 sc1\n"
"s_waitcnt vmcnt(20)\n"
"v_mfma_f32_16x16x32_bf16 v[80:83], a[80:83], v[40:43], v[80:83]\n"
"ds_read_b128 v[96:99], v121 offset:12288\n"
"v_mfma_f32_16x16x32_bf16 v[84:87], a[84:87], v[40:43], v[84:87]\n"
"v_mfma_f32_16x16x32_bf16 v[88:91], v[168:171], v[40:43], v[88:91]\n"
"s_waitcnt lgkmcnt(2)\n"
"v_mfma_f32_16x16x32_bf16 v[92:95], v[100:103], v[40:43], v[92:95]\n"
"buffer_load_dwordx4 v[40:43], v114, s[60:63], s66 offen offset:2048 sc1\n"
"s_waitcnt vmcnt(20)\n"
"v_mfma_f32_16x16x32_bf16 v[80:83], a[88:91], v[44:47], v[80:83]\n"
"ds_read_b128 v[100:103], v121 offset:13312\n"
"v_mfma_f32_16x16x32_bf16 v[84:87], a[92:95], v[44:47], v[84:87]\n"
"v_mfma_f32_16x16x32_bf16 v[88:91], v[172:175], v[44:47], v[88:91]\n"
"s_waitcnt lgkmcnt(2)\n"
"v_mfma_f32_16x16x32_bf16 v[92:95], v[104:107], v[44:47], v[92:95]\n"
"buffer_load_dwordx4 v[44:47], v114, s[60:63], s66 offen offset:3072 sc1\n"
"s_waitcnt vmcnt(20)\n"
"v_mfma_f32_16x16x32_bf16 v[80:83], a[96:99], v[48:51], v[80:83]\n"
"ds_read_b128 v[104:107], v121 offset:14336\n"
"v_mfma_f32_16x16x32_bf16 v[84:87], a[100:103], v[48:51], v[84:87]\n"
"v_mfma_f32_16x16x32_bf16 v[88:91], v[176:179], v[48:51], v[88:91]\n"
"s_waitcnt lgkmcnt(2)\n"
"v_mfma_f32_16x16x32_bf16 v[92:95], v[96:99], v[48:51], v[92:95]\n"
"s_waitcnt vmcnt(19)\n"
"v_mfma_f32_16x16x32_bf16 v[80:83], a[104:107], v[52:55], v[80:83]\n"
"ds_read_b128 v[96:99], v121 offset:15360\n"
"v_mfma_f32_16x16x32_bf16 v[84:87], a[108:111], v[52:55], v[84:87]\n"
"v_mfma_f32_16x16x32_bf16 v[88:91], v[180:183], v[52:55], v[88:91]\n"
"s_waitcnt lgkmcnt(2)\n"
"v_mfma_f32_16x16x32_bf16 v[92:95], v[100:103], v[52:55], v[92:95]\n"
"s_waitcnt vmcnt(18)\n"
"v_mfma_f32_16x16x32_bf16 v[80:83], a[112:115], v[56:59], v[80:83]\n"
"ds_read_b128 v[100:103], v121 offset:16384\n"
"v_mfma_f32_16x16x32_bf16 v[84:87], a[116:119], v[56:59], v[84:87]\n"
"v_mfma_f32_16x16x32_bf16 v[88:91], v[184:187], v[56:59], v[88:91]\n"
"s_waitcnt lgkmcnt(2)\n"
"v_mfma_f32_16x16x32_bf16 v[92:95], v[104:107], v[56:59], v[92:95]\n"
"s_waitcnt vmcnt(17)\n"
"v_mfma_f32_16x16x32_bf16 v[80:83], a[120:123], v[60:63], v[80:83]\n"
"ds_read_b128 v[104:107], v121 offset:17408\n"
"v_mfma_f32_16x16x32_bf16 v[84:87], a[124:127], v[60:63], v[84:87]\n"
"v_mfma_f32_16x16x32_bf16 v[88:91], v[188:191], v[60:63], v[88:91]\n"
"s_waitcnt lgkmcnt(2)\n"
"v_mfma_f32_16x16x32_bf16 v[92:95], v[96:99], v[60:63], v[92:95]\n"
"s_waitcnt vmcnt(16)\n"
"v_mfma_f32_16x16x32_bf16 v[80:83], a[128:131], v[64:67], v[80:83]\n"
"ds_read_b128 v[96:99], v121 offset:18432\n"
"v_mfma_f32_16x16x32_bf16 v[84:87], a[132:135], v[64:67], v[84:87]\n"
"v_mfma_f32_16x16x32_bf16 v[88:91], v[192:195], v[64:67], v[88:91]\n"
"s_waitcnt lgkmcnt(2)\n"
"v_mfma_f32_16x16x32_bf16 v[92:95], v[100:103], v[64:67], v[92:95]\n"
"s_waitcnt vmcnt(15)\n"
"v_mfma_f32_16x16x32_bf16 v[80:83], a[136:139], v[68:71], v[80:83]\n"
"ds_read_b128 v[100:103], v121 offset:19456\n"
"v_mfma_f32_16x16x32_bf16 v[84:87], a[140:143], v[68:71], v[84:87]\n"
"v_mfma_f32_16x16x32_bf16 v[88:91], v[196:199], v[68:71], v[88:91]\n"
"s_waitcnt lgkmcnt(2)\n"
"v_mfma_f32_16x16x32_bf16 v[92:95], v[104:107], v[68:71], v[92:95]\n"
"s_waitcnt vmcnt(14)\n"
"v_mfma_f32_16x16x32_bf16 v[80:83], a[144:147], v[72:75], v[80:83]\n"
"ds_read_b128 v[104:107], v121 offset:20480\n"
"v_mfma_f32_16x16x32_bf16 v[84:87], a[148:151], v[72:75], v[84:87]\n"
"v_mfma_f32_16x16x32_bf16 v[88:91], v[200:203], v[72:75], v[88:91]\n"
"s_waitcnt lgkmcnt(2)\n"
"v_mfma_f32_16x16x32_bf16 v[92:95], v[96:99], v[72:75], v[92:95]\n"
"s_waitcnt vmcnt(13)\n"
"v_mfma_f32_16x16x32_bf16 v[80:83], a[152:155], v[76:79], v[80:83]\n"
"ds_read_b128 v[96:99], v121 offset:21504\n"
"v_mfma_f32_16x16x32_bf16 v[84:87], a[156:159], v[76:79], v[84:87]\n"
"v_mfma_f32_16x16x32_bf16 v[88:91], v[204:207], v[76:79], v[88:91]\n"
"s_waitcnt lgkmcnt(2)\n"
"v_mfma_f32_16x16x32_bf16 v[92:95], v[100:103], v[76:79], v[92:95]\n"
"s_waitcnt vmcnt(11)\n"
"v_mfma_f32_16x16x32_bf16 v[80:83], a[160:163], v[0:3], v[80:83]\n"
"ds_read_b128 v[100:103], v121 offset:22528\n"
"v_mfma_f32_16x16x32_bf16 v[84:87], a[164:167], v[0:3], v[84:87]\n"
"v_mfma_f32_16x16x32_bf16 v[88:91], v[208:211], v[0:3], v[88:91]\n"
"s_waitcnt lgkmcnt(2)\n"
"v_mfma_f32_16x16x32_bf16 v[92:95], v[104:107], v[0:3], v[92:95]\n"
"s_waitcnt vmcnt(10)\n"
"v_mfma_f32_16x16x32_bf16 v[80:83], a[168:171], v[4:7], v[80:83]\n"
"ds_read_b128 v[104:107], v121 offset:23552\n"
"v_mfma_f32_16x16x32_bf16 v[84:87], a[172:175], v[4:7], v[84:87]\n"
"v_mfma_f32_16x16x32_bf16 v[88:91], v[212:215], v[4:7], v[88:91]\n"
"s_waitcnt lgkmcnt(2)\n"
"v_mfma_f32_16x16x32_bf16 v[92:95], v[96:99], v[4:7], v[92:95]\n"
"s_waitcnt vmcnt(9)\n"
"v_mfma_f32_16x16x32_bf16 v[80:83], a[176:179], v[8:11], v[80:83]\n"
"ds_read_b128 v[96:99], v121 offset:24576\n"
"v_mfma_f32_16x16x32_bf16 v[84:87], a[180:183], v[8:11], v[84:87]\n"
"v_mfma_f32_16x16x32_bf16 v[88:91], v[216:219], v[8:11], v[88:91]\n"
"s_waitcnt lgkmcnt(2)\n"
"v_mfma_f32_16x16x32_bf16 v[92:95], v[100:103], v[8:11], v[92:95]\n"
"s_waitcnt vmcnt(8)\n"
"v_mfma_f32_16x16x32_bf16 v[80:83], a[184:187], v[12:15], v[80:83]\n"
"ds_read_b128 v[100:103], v121 offset:25600\n"
"v_mfma_f32_16x16x32_bf16 v[84:87], a[188:191], v[12:15], v[84:87]\n"
"v_mfma_f32_16x16x32_bf16 v[88:91], v[220:223], v[12:15], v[88:91]\n"
"s_waitcnt lgkmcnt(2)\n"
"v_mfma_f32_16x16x32_bf16 v[92:95], v[104:107], v[12:15], v[92:95]\n"
"s_waitcnt vmcnt(7)\n"
"v_mfma_f32_16x16x32_bf16 v[80:83], a[192:195], v[16:19], v[80:83]\n"
"ds_read_b128 v[104:107], v121 offset:26624\n"
"v_mfma_f32_16x16x32_bf16 v[84:87], a[196:199], v[16:19], v[84:87]\n"
"v_mfma_f32_16x16x32_bf16 v[88:91], v[224:227], v[16:19], v[88:91]\n"
"s_waitcnt lgkmcnt(2)\n"
"v_mfma_f32_16x16x32_bf16 v[92:95], v[96:99], v[16:19], v[92:95]\n"
"s_waitcnt vmcnt(6)\n"
"v_mfma_f32_16x16x32_bf16 v[80:83], a[200:203], v[20:23], v[80:83]\n"
"ds_read_b128 v[96:99], v121 offset:27648\n"
"v_mfma_f32_16x16x32_bf16 v[84:87], a[204:207], v[20:23], v[84:87]\n"
"v_mfma_f32_16x16x32_bf16 v[88:91], v[228:231], v[20:23], v[88:91]\n"
"s_waitcnt lgkmcnt(2)\n"
"v_mfma_f32_16x16x32_bf16 v[92:95], v[100:103], v[20:23], v[92:95]\n"
"s_waitcnt vmcnt(5)\n"
"v_mfma_f32_16x16x32_bf16 v[80:83], a[208:211], v[24:27], v[80:83]\n"
"ds_read_b128 v[100:103], v121 offset:28672\n"
"v_mfma_f32_16x16x32_bf16 v[84:87], a[212:215], v[24:27], v[84:87]\n"
"v_mfma_f32_16x16x32_bf16 v[88:91], v[232:235], v[24:27], v[88:91]\n"
"s_waitcnt lgkmcnt(2)\n"
"v_mfma_f32_16x16x32_bf16 v[92:95], v[104:107], v[24:27], v[92:95]\n"
"s_waitcnt vmcnt(4)\n"
"v_mfma_f32_16x16x32_bf16 v[80:83], a[216:219], v[28:31], v[80:83]\n"
"ds_read_b128 v[104:107], v121 offset:29696\n"
"v_mfma_f32_16x16x32_bf16 v[84:87], a[220:223], v[28:31], v[84:87]\n"
"v_mfma_f32_16x16x32_bf16 v[88:91], v[236:239], v[28:31], v[88:91]\n"
"s_waitcnt lgkmcnt(2)\n"
"v_mfma_f32_16x16x32_bf16 v[92:95], v[96:99], v[28:31], v[92:95]\n"
"s_waitcnt vmcnt(3)\n"
"v_mfma_f32_16x16x32_bf16 v[80:83], a[224:227], v[32:35], v[80:83]\n"
"ds_read_b128 v[96:99], v121 offset:30720\n"
"v_mfma_f32_16x16x32_bf16 v[84:87], a[228:231], v[32:35], v[84:87]\n"
"v_mfma_f32_16x16x32_bf16 v[88:91], v[240:243], v[32:35], v[88:91]\n"
"s_waitcnt lgkmcnt(2)\n"
"v_mfma_f32_16x16x32_bf16 v[92:95], v[100:103], v[32:35], v[92:95]\n"
"s_waitcnt vmcnt(2)\n"
"v_mfma_f32_16x16x32_bf16 v[80:83], a[232:235], v[36:39], v[80:83]\n"
"ds_read_b128 v[100:103], v121 offset:31744\n"
"v_mfma_f32_16x16x32_bf16 v[84:87], a[236:239], v[36:39], v[84:87]\n"
"v_mfma_f32_16x16x32_bf16 v[88:91], v[244:247], v[36:39], v[88:91]\n"
"s_waitcnt lgkmcnt(2)\n"
"v_mfma_f32_16x16x32_bf16 v[92:95], v[104:107], v[36:39], v[92:95]\n"
"s_waitcnt vmcnt(1)\n"
"v_mfma_f32_16x16x32_bf16 v[80:83], a[240:243], v[40:43], v[80:83]\n"
"v_mfma_f32_16x16x32_bf16 v[84:87], a[244:247], v[40:43], v[84:87]\n"
"v_mfma_f32_16x16x32_bf16 v[88:91], v[248:251], v[40:43], v[88:91]\n"
"s_waitcnt lgkmcnt(1)\n"
"v_mfma_f32_16x16x32_bf16 v[92:95], v[96:99], v[40:43], v[92:95]\n"
"s_waitcnt vmcnt(0)\n"
"v_mfma_f32_16x16x32_bf16 v[80:83], a[248:251], v[44:47], v[80:83]\n"
"v_mfma_f32_16x16x32_bf16 v[84:87], a[252:255], v[44:47], v[84:87]\n"
"v_mfma_f32_16x16x32_bf16 v[88:91], v[252:255], v[44:47], v[88:91]\n"
"s_waitcnt lgkmcnt(0)\n"
"v_mfma_f32_16x16x32_bf16 v[92:95], v[100:103], v[44:47], v[92:95]\n"
"ds_write_b128 v122, v[80:83] offset:0\n"
"ds_write_b128 v123, v[84:87] offset:1024\n"
"s_nop 2\n"
"ds_write_b128 v124, v[88:91] offset:2048\n"
"s_nop 1\n"
"ds_write_b128 v125, v[92:95] offset:3072\n"
"v_xor_b32 v122, 16384, v122\n"
"v_xor_b32 v123, 16384, v123\n"
"v_xor_b32 v124, 16384, v124\n"
"v_xor_b32 v125, 16384, v125\n"
"s_add_u32 s8, s8, 4\n"
"s_waitcnt lgkmcnt(0)\n"
"s_barrier\n"
"ds_read_b128 v[0:3], v117 offset:0\n"
"ds_read_b128 v[4:7], v117 offset:4096\n"
"ds_read_b128 v[8:11], v117 offset:8192\n"
"ds_read_b128 v[12:15], v117 offset:12288\n"
"v_xor_b32 v117, 16384, v117\n"
"s_waitcnt lgkmcnt(3)\n"
"v_add_f32 v108, v108, v0\n"
"v_add_f32 v109, v109, v1\n"
"v_add_f32 v110, v110, v2\n"
"v_add_f32 v111, v111, v3\n"
"s_waitcnt lgkmcnt(2)\n"
"v_add_f32 v108, v108, v4\n"
"v_add_f32 v109, v109, v5\n"
"v_add_f32 v110, v110, v6\n"
"v_add_f32 v111, v111, v7\n"
"s_waitcnt lgkmcnt(1)\n"
"v_add_f32 v108, v108, v8\n"
"v_add_f32 v109, v109, v9\n"
"v_add_f32 v110, v110, v10\n"
"v_add_f32 v111, v111, v11\n"
"s_waitcnt lgkmcnt(0)\n"
"v_add_f32 v108, v108, v12\n"
"v_add_f32 v109, v109, v13\n"
"v_add_f32 v110, v110, v14\n"
"v_add_f32 v111, v111, v15\n"
"v_cvt_pk_bf16_f32 v16, v108, v109\n"
"v_cvt_pk_bf16_f32 v17, v110, v111\n"
"buffer_store_dwordx2 v[16:17], v116, s[48:51], s13 offen sc1\n"
"s_lshr_b32 s64, s8, 2\n"
"s_cmp_lg_u32 s64, 2\n"
"s_cbranch_scc1 .Ldnc94_%=\n"
"s_cmp_lg_u32 s37, 3\n"
"s_cbranch_scc1 .Lci95_%=\n"
"v_mov_b32 v112, 1\n"
"s_mov_b32 s64, 3170304\n"
"s_mov_b64 exec, 1\n"
"buffer_atomic_add v112, off, s[60:63], s64 sc0\n"
"s_waitcnt vmcnt(0)\n"
"v_readfirstlane_b32 s64, v112\n"
"s_mov_b64 exec, -1\n"
"s_cmp_lg_u32 s64, 47\n"
"s_cbranch_scc1 .Lci95_%=\n"
"s_mov_b32 s64, 3145728\n"
"v_mov_b32 v112, s75\n"
"s_mov_b64 exec, 1\n"
"buffer_store_dword v112, off, s[60:63], s64 sc1\n"
"s_mov_b64 exec, -1\n"
".Lci95_%=:\n"
".Ldnc94_%=:\n"
"s_cmp_lt_u32 s8, 128\n"
"s_cbranch_scc1 .Ldtop91_%=\n"
"s_endpgm\n"
".Lx768_%=:\n"
"s_cmp_ge_u32 s39, 2\n"
"s_cbranch_scc1 .Lup8_%=\n"
"s_mov_b32 s40, s24\n"
"s_and_b32 s41, s25, 0xffff\n"
"s_mov_b32 s42, 0x1c0000\n"
"s_mov_b32 s43, 0x20000\n"
"s_mov_b32 s48, s8\n"
"s_and_b32 s49, s9, 0xffff\n"
"s_mov_b32 s50, 0x200000\n"
"s_mov_b32 s51, 0x20000\n"
"s_sleep 32\n"
"s_sleep 32\n"
"s_sleep 32\n"
"s_sleep 32\n"
"v_lshlrev_b32 v114, 4, v119\n"
"v_lshrrev_b32 v0, 3, v119\n"
"v_lshlrev_b32 v1, 10, v0\n"
"v_lshrrev_b32 v2, 4, v119\n"
"v_lshlrev_b32 v2, 1, v2\n"
"v_and_b32 v3, 7, v119\n"
"v_xor_b32 v2, v2, v3\n"
"v_lshl_add_u32 v1, v2, 4, v1\n"
"s_lshl_b32 s64, s38, 7\n"
"v_add_u32 v4, s64, v1\n"
"s_lshl_b32 s66, s37, 13\n"
"v_lshrrev_b32 v1, 2, v119\n"
"v_lshlrev_b32 v1, 7, v1\n"
"v_and_b32 v0, 3, v0\n"
"v_lshl_add_u32 v1, v0, 5, v1\n"
"v_and_b32 v0, 3, v119\n"
"v_lshl_add_u32 v1, v0, 3, v1\n"
"v_add_u32 v8, s66, v1\n"
"s_mul_i32 s67, s37, 458752\n"
"s_lshl_b32 s64, s37, 15\n"
"s_add_u32 s64, s64, 32768\n"
"v_add_u32 v121, s64, v114\n"
"v_add_u32 v5, 7168, v4\n"
"v_xor_b32 v9, 32, v8\n"
"v_add_u32 v6, 14336, v4\n"
"v_xor_b32 v10, 64, v8\n"
"v_add_u32 v7, 21504, v4\n"
"v_xor_b32 v11, 96, v8\n"
"s_add_u32 m0, s66, 0\n"
"s_add_u32 s64, s67, 0\n"
"buffer_load_dwordx4 v4, s[40:43], s64 offen offset:0 lds\n"
"buffer_load_dwordx4 v5, s[40:43], s64 offen offset:1024 lds\n"
"buffer_load_dwordx4 v6, s[40:43], s64 offen offset:2048 lds\n"
"buffer_load_dwordx4 v7, s[40:43], s64 offen offset:3072 lds\n"
"s_add_u32 m0, s66, 4096\n"
"s_add_u32 s64, s67, 32768\n"
"buffer_load_dwordx4 v4, s[40:43], s64 offen offset:0 lds\n"
"buffer_load_dwordx4 v5, s[40:43], s64 offen offset:1024 lds\n"
"buffer_load_dwordx4 v6, s[40:43], s64 offen offset:2048 lds\n"
"buffer_load_dwordx4 v7, s[40:43], s64 offen offset:3072 lds\n"
"s_waitcnt vmcnt(4)\n"
"ds_read_b64_tr_b16 a[0:1], v8 offset:0\n"
"ds_read_b64_tr_b16 a[2:3], v8 offset:2048\n"
"ds_read_b64_tr_b16 a[4:5], v9 offset:0\n"
"ds_read_b64_tr_b16 a[6:7], v9 offset:2048\n"
"ds_read_b64_tr_b16 a[8:9], v10 offset:0\n"
"ds_read_b64_tr_b16 a[10:11], v10 offset:2048\n"
"ds_read_b64_tr_b16 a[12:13], v11 offset:0\n"
"ds_read_b64_tr_b16 a[14:15], v11 offset:2048\n"
"s_waitcnt lgkmcnt(0)\n"
"s_add_u32 m0, s66, 0\n"
"s_add_u32 s64, s67, 65536\n"
"buffer_load_dwordx4 v4, s[40:43], s64 offen offset:0 lds\n"
"buffer_load_dwordx4 v5, s[40:43], s64 offen offset:1024 lds\n"
"buffer_load_dwordx4 v6, s[40:43], s64 offen offset:2048 lds\n"
"buffer_load_dwordx4 v7, s[40:43], s64 offen offset:3072 lds\n"
"s_waitcnt vmcnt(4)\n"
"ds_read_b64_tr_b16 a[16:17], v8 offset:4096\n"
"ds_read_b64_tr_b16 a[18:19], v8 offset:6144\n"
"ds_read_b64_tr_b16 a[20:21], v9 offset:4096\n"
"ds_read_b64_tr_b16 a[22:23], v9 offset:6144\n"
"ds_read_b64_tr_b16 a[24:25], v10 offset:4096\n"
"ds_read_b64_tr_b16 a[26:27], v10 offset:6144\n"
"ds_read_b64_tr_b16 a[28:29], v11 offset:4096\n"
"ds_read_b64_tr_b16 a[30:31], v11 offset:6144\n"
"s_waitcnt lgkmcnt(0)\n"
"s_add_u32 m0, s66, 4096\n"
"s_add_u32 s64, s67, 98304\n"
"buffer_load_dwordx4 v4, s[40:43], s64 offen offset:0 lds\n"
"buffer_load_dwordx4 v5, s[40:43], s64 offen offset:1024 lds\n"
"buffer_load_dwordx4 v6, s[40:43], s64 offen offset:2048 lds\n"
"buffer_load_dwordx4 v7, s[40:43], s64 offen offset:3072 lds\n"
"s_waitcnt vmcnt(4)\n"
"ds_read_b64_tr_b16 a[32:33], v8 offset:0\n"
"ds_read_b64_tr_b16 a[34:35], v8 offset:2048\n"
"ds_read_b64_tr_b16 a[36:37], v9 offset:0\n"
"ds_read_b64_tr_b16 a[38:39], v9 offset:2048\n"
"ds_read_b64_tr_b16 a[40:41], v10 offset:0\n"
"ds_read_b64_tr_b16 a[42:43], v10 offset:2048\n"
"ds_read_b64_tr_b16 a[44:45], v11 offset:0\n"
"ds_read_b64_tr_b16 a[46:47], v11 offset:2048\n"
"s_waitcnt lgkmcnt(0)\n"
"s_add_u32 m0, s66, 0\n"
"s_add_u32 s64, s67, 131072\n"
"buffer_load_dwordx4 v4, s[40:43], s64 offen offset:0 lds\n"
"buffer_load_dwordx4 v5, s[40:43], s64 offen offset:1024 lds\n"
"buffer_load_dwordx4 v6, s[40:43], s64 offen offset:2048 lds\n"
"buffer_load_dwordx4 v7, s[40:43], s64 offen offset:3072 lds\n"
"s_waitcnt vmcnt(4)\n"
"ds_read_b64_tr_b16 a[48:49], v8 offset:4096\n"
"ds_read_b64_tr_b16 a[50:51], v8 offset:6144\n"
"ds_read_b64_tr_b16 a[52:53], v9 offset:4096\n"
"ds_read_b64_tr_b16 a[54:55], v9 offset:6144\n"
"ds_read_b64_tr_b16 a[56:57], v10 offset:4096\n"
"ds_read_b64_tr_b16 a[58:59], v10 offset:6144\n"
"ds_read_b64_tr_b16 a[60:61], v11 offset:4096\n"
"ds_read_b64_tr_b16 a[62:63], v11 offset:6144\n"
"s_waitcnt lgkmcnt(0)\n"
"s_add_u32 m0, s66, 4096\n"
"s_add_u32 s64, s67, 163840\n"
"buffer_load_dwordx4 v4, s[40:43], s64 offen offset:0 lds\n"
"buffer_load_dwordx4 v5, s[40:43], s64 offen offset:1024 lds\n"
"buffer_load_dwordx4 v6, s[40:43], s64 offen offset:2048 lds\n"
"buffer_load_dwordx4 v7, s[40:43], s64 offen offset:3072 lds\n"
"s_waitcnt vmcnt(4)\n"
"ds_read_b64_tr_b16 a[64:65], v8 offset:0\n"
"ds_read_b64_tr_b16 a[66:67], v8 offset:2048\n"
"ds_read_b64_tr_b16 a[68:69], v9 offset:0\n"
"ds_read_b64_tr_b16 a[70:71], v9 offset:2048\n"
"ds_read_b64_tr_b16 a[72:73], v10 offset:0\n"
"ds_read_b64_tr_b16 a[74:75], v10 offset:2048\n"
"ds_read_b64_tr_b16 a[76:77], v11 offset:0\n"
"ds_read_b64_tr_b16 a[78:79], v11 offset:2048\n"
"s_waitcnt lgkmcnt(0)\n"
"s_add_u32 m0, s66, 0\n"
"s_add_u32 s64, s67, 196608\n"
"buffer_load_dwordx4 v4, s[40:43], s64 offen offset:0 lds\n"
"buffer_load_dwordx4 v5, s[40:43], s64 offen offset:1024 lds\n"
"buffer_load_dwordx4 v6, s[40:43], s64 offen offset:2048 lds\n"
"buffer_load_dwordx4 v7, s[40:43], s64 offen offset:3072 lds\n"
"s_waitcnt vmcnt(4)\n"
"ds_read_b64_tr_b16 a[80:81], v8 offset:4096\n"
"ds_read_b64_tr_b16 a[82:83], v8 offset:6144\n"
"ds_read_b64_tr_b16 a[84:85], v9 offset:4096\n"
"ds_read_b64_tr_b16 a[86:87], v9 offset:6144\n"
"ds_read_b64_tr_b16 a[88:89], v10 offset:4096\n"
"ds_read_b64_tr_b16 a[90:91], v10 offset:6144\n"
"ds_read_b64_tr_b16 a[92:93], v11 offset:4096\n"
"ds_read_b64_tr_b16 a[94:95], v11 offset:6144\n"
"s_waitcnt lgkmcnt(0)\n"
"s_add_u32 m0, s66, 4096\n"
"s_add_u32 s64, s67, 229376\n"
"buffer_load_dwordx4 v4, s[40:43], s64 offen offset:0 lds\n"
"buffer_load_dwordx4 v5, s[40:43], s64 offen offset:1024 lds\n"
"buffer_load_dwordx4 v6, s[40:43], s64 offen offset:2048 lds\n"
"buffer_load_dwordx4 v7, s[40:43], s64 offen offset:3072 lds\n"
"s_waitcnt vmcnt(4)\n"
"ds_read_b64_tr_b16 a[96:97], v8 offset:0\n"
"ds_read_b64_tr_b16 a[98:99], v8 offset:2048\n"
"ds_read_b64_tr_b16 a[100:101], v9 offset:0\n"
"ds_read_b64_tr_b16 a[102:103], v9 offset:2048\n"
"ds_read_b64_tr_b16 a[104:105], v10 offset:0\n"
"ds_read_b64_tr_b16 a[106:107], v10 offset:2048\n"
"ds_read_b64_tr_b16 a[108:109], v11 offset:0\n"
"ds_read_b64_tr_b16 a[110:111], v11 offset:2048\n"
"s_waitcnt lgkmcnt(0)\n"
"s_add_u32 m0, s66, 0\n"
"s_add_u32 s64, s67, 262144\n"
"buffer_load_dwordx4 v4, s[40:43], s64 offen offset:0 lds\n"
"buffer_load_dwordx4 v5, s[40:43], s64 offen offset:1024 lds\n"
"buffer_load_dwordx4 v6, s[40:43], s64 offen offset:2048 lds\n"
"buffer_load_dwordx4 v7, s[40:43], s64 offen offset:3072 lds\n"
"s_waitcnt vmcnt(4)\n"
"ds_read_b64_tr_b16 a[112:113], v8 offset:4096\n"
"ds_read_b64_tr_b16 a[114:115], v8 offset:6144\n"
"ds_read_b64_tr_b16 a[116:117], v9 offset:4096\n"
"ds_read_b64_tr_b16 a[118:119], v9 offset:6144\n"
"ds_read_b64_tr_b16 a[120:121], v10 offset:4096\n"
"ds_read_b64_tr_b16 a[122:123], v10 offset:6144\n"
"ds_read_b64_tr_b16 a[124:125], v11 offset:4096\n"
"ds_read_b64_tr_b16 a[126:127], v11 offset:6144\n"
"s_waitcnt lgkmcnt(0)\n"
"s_add_u32 m0, s66, 4096\n"
"s_add_u32 s64, s67, 294912\n"
"buffer_load_dwordx4 v4, s[40:43], s64 offen offset:0 lds\n"
"buffer_load_dwordx4 v5, s[40:43], s64 offen offset:1024 lds\n"
"buffer_load_dwordx4 v6, s[40:43], s64 offen offset:2048 lds\n"
"buffer_load_dwordx4 v7, s[40:43], s64 offen offset:3072 lds\n"
"s_waitcnt vmcnt(4)\n"
"ds_read_b64_tr_b16 a[128:129], v8 offset:0\n"
"ds_read_b64_tr_b16 a[130:131], v8 offset:2048\n"
"ds_read_b64_tr_b16 a[132:133], v9 offset:0\n"
"ds_read_b64_tr_b16 a[134:135], v9 offset:2048\n"
"ds_read_b64_tr_b16 a[136:137], v10 offset:0\n"
"ds_read_b64_tr_b16 a[138:139], v10 offset:2048\n"
"ds_read_b64_tr_b16 a[140:141], v11 offset:0\n"
"ds_read_b64_tr_b16 a[142:143], v11 offset:2048\n"
"s_waitcnt lgkmcnt(0)\n"
"s_add_u32 m0, s66, 0\n"
"s_add_u32 s64, s67, 327680\n"
"buffer_load_dwordx4 v4, s[40:43], s64 offen offset:0 lds\n"
"buffer_load_dwordx4 v5, s[40:43], s64 offen offset:1024 lds\n"
"buffer_load_dwordx4 v6, s[40:43], s64 offen offset:2048 lds\n"
"buffer_load_dwordx4 v7, s[40:43], s64 offen offset:3072 lds\n"
"s_waitcnt vmcnt(4)\n"
"ds_read_b64_tr_b16 a[144:145], v8 offset:4096\n"
"ds_read_b64_tr_b16 a[146:147], v8 offset:6144\n"
"ds_read_b64_tr_b16 a[148:149], v9 offset:4096\n"
"ds_read_b64_tr_b16 a[150:151], v9 offset:6144\n"
"ds_read_b64_tr_b16 a[152:153], v10 offset:4096\n"
"ds_read_b64_tr_b16 a[154:155], v10 offset:6144\n"
"ds_read_b64_tr_b16 a[156:157], v11 offset:4096\n"
"ds_read_b64_tr_b16 a[158:159], v11 offset:6144\n"
"s_waitcnt lgkmcnt(0)\n"
"s_add_u32 m0, s66, 4096\n"
"s_add_u32 s64, s67, 360448\n"
"buffer_load_dwordx4 v4, s[40:43], s64 offen offset:0 lds\n"
"buffer_load_dwordx4 v5, s[40:43], s64 offen offset:1024 lds\n"
"buffer_load_dwordx4 v6, s[40:43], s64 offen offset:2048 lds\n"
"buffer_load_dwordx4 v7, s[40:43], s64 offen offset:3072 lds\n"
"s_waitcnt vmcnt(4)\n"
"ds_read_b64_tr_b16 a[160:161], v8 offset:0\n"
"ds_read_b64_tr_b16 a[162:163], v8 offset:2048\n"
"ds_read_b64_tr_b16 a[164:165], v9 offset:0\n"
"ds_read_b64_tr_b16 a[166:167], v9 offset:2048\n"
"ds_read_b64_tr_b16 a[168:169], v10 offset:0\n"
"ds_read_b64_tr_b16 a[170:171], v10 offset:2048\n"
"ds_read_b64_tr_b16 a[172:173], v11 offset:0\n"
"ds_read_b64_tr_b16 a[174:175], v11 offset:2048\n"
"s_waitcnt lgkmcnt(0)\n"
"s_add_u32 m0, s66, 0\n"
"s_add_u32 s64, s67, 393216\n"
"buffer_load_dwordx4 v4, s[40:43], s64 offen offset:0 lds\n"
"buffer_load_dwordx4 v5, s[40:43], s64 offen offset:1024 lds\n"
"buffer_load_dwordx4 v6, s[40:43], s64 offen offset:2048 lds\n"
"buffer_load_dwordx4 v7, s[40:43], s64 offen offset:3072 lds\n"
"s_waitcnt vmcnt(4)\n"
"ds_read_b64_tr_b16 a[176:177], v8 offset:4096\n"
"ds_read_b64_tr_b16 a[178:179], v8 offset:6144\n"
"ds_read_b64_tr_b16 a[180:181], v9 offset:4096\n"
"ds_read_b64_tr_b16 a[182:183], v9 offset:6144\n"
"ds_read_b64_tr_b16 a[184:185], v10 offset:4096\n"
"ds_read_b64_tr_b16 a[186:187], v10 offset:6144\n"
"ds_read_b64_tr_b16 a[188:189], v11 offset:4096\n"
"ds_read_b64_tr_b16 a[190:191], v11 offset:6144\n"
"s_waitcnt lgkmcnt(0)\n"
"s_add_u32 m0, s66, 4096\n"
"s_add_u32 s64, s67, 425984\n"
"buffer_load_dwordx4 v4, s[40:43], s64 offen offset:0 lds\n"
"buffer_load_dwordx4 v5, s[40:43], s64 offen offset:1024 lds\n"
"buffer_load_dwordx4 v6, s[40:43], s64 offen offset:2048 lds\n"
"buffer_load_dwordx4 v7, s[40:43], s64 offen offset:3072 lds\n"
"s_waitcnt vmcnt(4)\n"
"ds_read_b64_tr_b16 a[192:193], v8 offset:0\n"
"ds_read_b64_tr_b16 a[194:195], v8 offset:2048\n"
"ds_read_b64_tr_b16 a[196:197], v9 offset:0\n"
"ds_read_b64_tr_b16 a[198:199], v9 offset:2048\n"
"ds_read_b64_tr_b16 a[200:201], v10 offset:0\n"
"ds_read_b64_tr_b16 a[202:203], v10 offset:2048\n"
"ds_read_b64_tr_b16 a[204:205], v11 offset:0\n"
"ds_read_b64_tr_b16 a[206:207], v11 offset:2048\n"
"s_waitcnt lgkmcnt(0)\n"
"s_waitcnt vmcnt(0)\n"
"ds_read_b64_tr_b16 a[208:209], v8 offset:4096\n"
"ds_read_b64_tr_b16 a[210:211], v8 offset:6144\n"
"ds_read_b64_tr_b16 a[212:213], v9 offset:4096\n"
"ds_read_b64_tr_b16 a[214:215], v9 offset:6144\n"
"ds_read_b64_tr_b16 a[216:217], v10 offset:4096\n"
"ds_read_b64_tr_b16 a[218:219], v10 offset:6144\n"
"ds_read_b64_tr_b16 a[220:221], v11 offset:4096\n"
"ds_read_b64_tr_b16 a[222:223], v11 offset:6144\n"
"s_waitcnt lgkmcnt(0)\n"
"v_lshlrev_b32 v100, 3, v119\n"
"s_mov_b32 s74, 3146752\n"
"buffer_load_dwordx2 v[96:97], v100, s[60:63], s74 offen sc1\n"
"buffer_load_dwordx2 v[98:99], v100, s[60:63], s74 offen offset:1024 sc1\n"
"s_waitcnt vmcnt(0)\n"
"s_mov_b32 s78, 16384\n"
".Law96_%=:\n"
"v_xor_b32 v96, s34, v96\n"
"v_xor_b32 v97, s35, v97\n"
"v_and_or_b32 v96, v97, -4, v96\n"
"v_cmp_ne_u32 vcc, 0, v96\n"
"v_cmp_ne_u64 s[64:65], s[34:35], v[98:99]\n"
"s_nop 0\n"
"s_or_b64 vcc, vcc, s[64:65]\n"
"s_cbranch_vccz .Laok97_%=\n"
"s_sub_u32 s78, s78, 1\n"
"s_cbranch_scc1 .Lfail5_%=\n"
"s_sleep 8\n"
"buffer_load_dwordx2 v[96:97], v100, s[60:63], s74 offen sc1\n"
"buffer_load_dwordx2 v[98:99], v100, s[60:63], s74 offen offset:1024 sc1\n"
"s_waitcnt vmcnt(0)\n"
"s_branch .Law96_%=\n"
".Laok97_%=:\n"
"s_mov_b32 s77, 0\n"
"v_cmp_lt_u32 vcc, 1, v97\n"
"v_and_b32 v97, 1, v97\n"
"s_and_b32 s64, vcc_lo, 0xf00ff00f\n"
"s_and_b32 s65, vcc_hi, 0xf00ff00f\n"
"s_bcnt1_i32_b64 s64, s[64:65]\n"
"s_cmp_ge_u32 s64, 16\n"
"s_addc_u32 s77, s77, s77\n"
"s_and_b32 s64, vcc_lo, 0xff00ff0\n"
"s_and_b32 s65, vcc_hi, 0xff00ff0\n"
"s_bcnt1_i32_b64 s64, s[64:65]\n"
"s_cmp_ge_u32 s64, 16\n"
"s_addc_u32 s77, s77, s77\n"
"v_cmp_ne_u32 vcc, 0, v97\n"
"s_nop 0\n"
"s_and_b32 s64, vcc_lo, 0xffffffff\n"
"s_and_b32 s65, vcc_hi, 0xffffffff\n"
"s_bcnt1_i32_b64 s64, s[64:65]\n"
"s_cmp_ge_u32 s64, 32\n"
"s_addc_u32 s77, s77, s77\n"
"s_cmp_lg_u32 s37, 3\n"
"s_cbranch_scc1 .Lci98_%=\n"
"v_mov_b32 v96, 1\n"
"s_mov_b32 s64, 3170304\n"
"s_mov_b64 exec, 1\n"
"buffer_atomic_add v96, off, s[60:63], s64 sc0\n"
"s_waitcnt vmcnt(0)\n"
"v_readfirstlane_b32 s64, v96\n"
"s_mov_b64 exec, -1\n"
"s_cmp_lg_u32 s64, 47\n"
"s_cbranch_scc1 .Lci98_%=\n"
"s_mov_b32 s64, 3145728\n"
"v_mov_b32 v96, s75\n"
"s_mov_b64 exec, 1\n"
"buffer_store_dword v96, off, s[60:63], s64 sc1\n"
"s_mov_b64 exec, -1\n"
".Lci98_%=:\n"
"v_min_u32 v0, 13, v119\n"
"s_mul_i32 s64, s37, 14\n"
"v_add_u32 v0, s64, v0\n"
"v_lshrrev_b32 v1, 2, v0\n"
"v_lshlrev_b32 v1, 7, v1\n"
"v_and_b32 v0, 3, v0\n"
"v_lshl_add_u32 v0, v0, 3, v1\n"
"v_add_u32 v115, 4096, v0\n"
"s_lshl_b32 s15, s38, 7\n"
"s_lshl_b32 s65, s37, 4\n"
"s_add_u32 s15, s15, s65\n"
"s_add_u32 s15, s15, 6144\n"
"s_lshl_b32 s9, s37, 15\n"
"s_add_u32 s9, s9, 8650752\n"
"v_and_b32 v0, 15, v119\n"
"v_lshrrev_b32 v1, 4, v119\n"
"v_xor_b32 v2, v0, v1\n"
"v_lshlrev_b32 v2, 4, v2\n"
"v_lshl_add_u32 v2, v1, 8, v2\n"
"s_lshl_b32 s65, s37, 12\n"
"v_add_u32 v122, s65, v2\n"
"s_lshl_b32 s65, s37, 2\n"
"v_add_u32 v1, s65, v1\n"
"v_xor_b32 v2, v0, v1\n"
"v_lshlrev_b32 v2, 4, v2\n"
"v_lshl_add_u32 v117, v0, 8, v2\n"
"v_lshlrev_b32 v1, 10, v1\n"
"v_lshl_add_u32 v1, v0, 3, v1\n"
"s_lshl_b32 s65, s38, 7\n"
"v_add_u32 v116, s65, v1\n"
"s_lshl_b32 s64, s38, 2\n"
"s_add_u32 s64, s64, s37\n"
"s_add_u32 s64, s64, 192\n"
"s_mov_b32 s8, s39\n"
"s_waitcnt lgkmcnt(0)\n"
"s_barrier\n"
"v_xor_b32 v123, 64, v122\n"
"v_xor_b32 v124, 128, v122\n"
"v_xor_b32 v125, 192, v122\n"
"s_lshr_b32 s74, s64, 3\n"
"s_lshl_b32 s74, s74, 13\n"
"s_lshl_b32 s14, s64, 10\n"
"s_add_u32 s14, s14, s74\n"
"s_add_u32 s14, s14, 8388608\n"
"s_lshl_b32 s8, s8, 1\n"
"s_mov_b32 s70, 0x6000000\n"
".Ldtop99_%=:\n"
"s_lshr_b32 s65, s8, 1\n"
"s_lshl_b32 s12, s65, 1\n"
"s_and_b32 s74, s77, 1\n"
"s_add_u32 s12, s12, s74\n"
"s_lshl_b32 s12, s12, 13\n"
"s_add_u32 s12, s12, 6291456\n"
"s_lshr_b32 s67, s8, 2\n"
"s_xnor_b32 s67, s67, s77\n"
"s_and_b32 s67, s67, 1\n"
"s_lshl_b32 s67, s67, 13\n"
"s_lshl_b32 s74, s8, 19\n"
"s_add_u32 s67, s67, s74\n"
"s_and_b32 s64, s8, 1\n"
"s_lshl_b32 s64, s64, 3\n"
"s_add_u32 s12, s12, s64\n"
"s_add_u32 s68, s67, s14\n"
"s_add_u32 s66, s67, s9\n"
"s_add_u32 s67, s66, 524288\n"
"s_add_u32 s69, s68, 524288\n"
"s_lshl_b32 s13, s8, 14\n"
"s_mov_b32 s78, 16384\n"
".Ldw100_%=:\n"
"buffer_load_dwordx2 v[112:113], v115, s[60:63], s12 offen sc1\n"
"s_waitcnt vmcnt(0)\n"
"v_mov_b32 v168, s34\n"
"v_mov_b32 v169, s35\n"
"v_mov_b32 v170, s34\n"
"v_mov_b32 v171, s35\n"
"s_mov_b64 exec, 1\n"
"buffer_store_dwordx4 v[168:171], off, s[60:63], s70 sc1\n"
"s_mov_b64 exec, -1\n"
"s_mov_b32 s70, 0x6000000\n"
"v_cmp_ne_u64 vcc, s[34:35], v[112:113]\n"
"s_cbranch_vccz .Ldwok101_%=\n"
"s_sub_u32 s78, s78, 1\n"
"s_cbranch_scc1 .Lfail5_%=\n"
"s_sleep 8\n"
"s_branch .Ldw100_%=\n"
".Ldwok101_%=:\n"
"buffer_load_dwordx4 v[0:3], v114, s[60:63], s66 offen offset:0 sc1\n"
"buffer_load_dwordx4 v[4:7], v114, s[60:63], s66 offen offset:1024 sc1\n"
"buffer_load_dwordx4 v[8:11], v114, s[60:63], s66 offen offset:2048 sc1\n"
"buffer_load_dwordx4 v[12:15], v114, s[60:63], s66 offen offset:3072 sc1\n"
"s_add_u32 s66, s66, 4096\n"
"buffer_load_dwordx4 v[16:19], v114, s[60:63], s66 offen offset:0 sc1\n"
"buffer_load_dwordx4 v[20:23], v114, s[60:63], s66 offen offset:1024 sc1\n"
"buffer_load_dwordx4 v[24:27], v114, s[60:63], s66 offen offset:2048 sc1\n"
"buffer_load_dwordx4 v[28:31], v114, s[60:63], s66 offen offset:3072 sc1\n"
"s_add_u32 s66, s66, 12288\n"
"buffer_load_dwordx4 v[32:35], v114, s[60:63], s66 offen offset:0 sc1\n"
"buffer_load_dwordx4 v[36:39], v114, s[60:63], s66 offen offset:1024 sc1\n"
"buffer_load_dwordx4 v[40:43], v114, s[60:63], s66 offen offset:2048 sc1\n"
"buffer_load_dwordx4 v[44:47], v114, s[60:63], s66 offen offset:3072 sc1\n"
"s_add_u32 s66, s66, 4096\n"
"buffer_load_dwordx4 v[48:51], v114, s[60:63], s66 offen offset:0 sc1\n"
"buffer_load_dwordx4 v[52:55], v114, s[60:63], s66 offen offset:1024 sc1\n"
"buffer_load_dwordx4 v[56:59], v114, s[60:63], s67 offen offset:0 sc1\n"
"buffer_load_dwordx4 v[60:63], v114, s[60:63], s67 offen offset:1024 sc1\n"
"buffer_load_dwordx4 v[64:67], v114, s[60:63], s67 offen offset:2048 sc1\n"
"buffer_load_dwordx4 v[68:71], v114, s[60:63], s67 offen offset:3072 sc1\n"
"s_add_u32 s67, s67, 4096\n"
"buffer_load_dwordx4 v[72:75], v114, s[60:63], s67 offen offset:0 sc1\n"
"buffer_load_dwordx4 v[76:79], v114, s[60:63], s67 offen offset:1024 sc1\n"
"buffer_load_dwordx2 v[160:161], v116, s[48:51], s13 offen\n"
"s_add_u32 s13, s13, 16384\n"
"buffer_load_dwordx2 v[164:165], v116, s[48:51], s13 offen\n"
"s_waitcnt vmcnt(21)\n"
"v_mfma_f32_16x16x32_bf16 v[128:131], a[0:3], v[0:3], 0\n"
"v_mfma_f32_16x16x32_bf16 v[132:135], a[4:7], v[0:3], 0\n"
"v_mfma_f32_16x16x32_bf16 v[136:139], a[8:11], v[0:3], 0\n"
"v_mfma_f32_16x16x32_bf16 v[140:143], a[12:15], v[0:3], 0\n"
"buffer_load_dwordx4 v[0:3], v114, s[60:63], s67 offen offset:2048 sc1\n"
"s_waitcnt vmcnt(21)\n"
"v_mfma_f32_16x16x32_bf16 v[128:131], a[16:19], v[4:7], v[128:131]\n"
"v_mfma_f32_16x16x32_bf16 v[132:135], a[20:23], v[4:7], v[132:135]\n"
"v_mfma_f32_16x16x32_bf16 v[136:139], a[24:27], v[4:7], v[136:139]\n"
"v_mfma_f32_16x16x32_bf16 v[140:143], a[28:31], v[4:7], v[140:143]\n"
"buffer_load_dwordx4 v[4:7], v114, s[60:63], s67 offen offset:3072 sc1\n"
"s_waitcnt vmcnt(21)\n"
"v_mfma_f32_16x16x32_bf16 v[128:131], a[32:35], v[8:11], v[128:131]\n"
"v_mfma_f32_16x16x32_bf16 v[132:135], a[36:39], v[8:11], v[132:135]\n"
"v_mfma_f32_16x16x32_bf16 v[136:139], a[40:43], v[8:11], v[136:139]\n"
"v_mfma_f32_16x16x32_bf16 v[140:143], a[44:47], v[8:11], v[140:143]\n"
"s_add_u32 s67, s67, 12288\n"
"buffer_load_dwordx4 v[8:11], v114, s[60:63], s67 offen offset:0 sc1\n"
"s_waitcnt vmcnt(21)\n"
"v_mfma_f32_16x16x32_bf16 v[128:131], a[48:51], v[12:15], v[128:131]\n"
"v_mfma_f32_16x16x32_bf16 v[132:135], a[52:55], v[12:15], v[132:135]\n"
"v_mfma_f32_16x16x32_bf16 v[136:139], a[56:59], v[12:15], v[136:139]\n"
"v_mfma_f32_16x16x32_bf16 v[140:143], a[60:63], v[12:15], v[140:143]\n"
"buffer_load_dwordx4 v[12:15], v114, s[60:63], s67 offen offset:1024 sc1\n"
"s_waitcnt vmcnt(21)\n"
"v_mfma_f32_16x16x32_bf16 v[128:131], a[64:67], v[16:19], v[128:131]\n"
"v_mfma_f32_16x16x32_bf16 v[132:135], a[68:71], v[16:19], v[132:135]\n"
"v_mfma_f32_16x16x32_bf16 v[136:139], a[72:75], v[16:19], v[136:139]\n"
"v_mfma_f32_16x16x32_bf16 v[140:143], a[76:79], v[16:19], v[140:143]\n"
"buffer_load_dwordx4 v[16:19], v114, s[60:63], s67 offen offset:2048 sc1\n"
"s_waitcnt vmcnt(21)\n"
"v_mfma_f32_16x16x32_bf16 v[128:131], a[80:83], v[20:23], v[128:131]\n"
"v_mfma_f32_16x16x32_bf16 v[132:135], a[84:87], v[20:23], v[132:135]\n"
"v_mfma_f32_16x16x32_bf16 v[136:139], a[88:91], v[20:23], v[136:139]\n"
"v_mfma_f32_16x16x32_bf16 v[140:143], a[92:95], v[20:23], v[140:143]\n"
"buffer_load_dwordx4 v[20:23], v114, s[60:63], s67 offen offset:3072 sc1\n"
"s_waitcnt vmcnt(21)\n"
"v_mfma_f32_16x16x32_bf16 v[128:131], a[96:99], v[24:27], v[128:131]\n"
"v_mfma_f32_16x16x32_bf16 v[132:135], a[100:103], v[24:27], v[132:135]\n"
"v_mfma_f32_16x16x32_bf16 v[136:139], a[104:107], v[24:27], v[136:139]\n"
"v_mfma_f32_16x16x32_bf16 v[140:143], a[108:111], v[24:27], v[140:143]\n"
"s_add_u32 s67, s67, 4096\n"
"buffer_load_dwordx4 v[24:27], v114, s[60:63], s67 offen offset:0 sc1\n"
"s_waitcnt vmcnt(21)\n"
"v_mfma_f32_16x16x32_bf16 v[128:131], a[112:115], v[28:31], v[128:131]\n"
"v_mfma_f32_16x16x32_bf16 v[132:135], a[116:119], v[28:31], v[132:135]\n"
"v_mfma_f32_16x16x32_bf16 v[136:139], a[120:123], v[28:31], v[136:139]\n"
"v_mfma_f32_16x16x32_bf16 v[140:143], a[124:127], v[28:31], v[140:143]\n"
"buffer_load_dwordx4 v[28:31], v114, s[60:63], s67 offen offset:1024 sc1\n"
"s_waitcnt vmcnt(21)\n"
"v_mfma_f32_16x16x32_bf16 v[128:131], a[128:131], v[32:35], v[128:131]\n"
"v_mfma_f32_16x16x32_bf16 v[132:135], a[132:135], v[32:35], v[132:135]\n"
"v_mfma_f32_16x16x32_bf16 v[136:139], a[136:139], v[32:35], v[136:139]\n"
"v_mfma_f32_16x16x32_bf16 v[140:143], a[140:143], v[32:35], v[140:143]\n"
"s_waitcnt vmcnt(20)\n"
"v_mfma_f32_16x16x32_bf16 v[128:131], a[144:147], v[36:39], v[128:131]\n"
"v_mfma_f32_16x16x32_bf16 v[132:135], a[148:151], v[36:39], v[132:135]\n"
"v_mfma_f32_16x16x32_bf16 v[136:139], a[152:155], v[36:39], v[136:139]\n"
"v_mfma_f32_16x16x32_bf16 v[140:143], a[156:159], v[36:39], v[140:143]\n"
"s_waitcnt vmcnt(19)\n"
"v_mfma_f32_16x16x32_bf16 v[128:131], a[160:163], v[40:43], v[128:131]\n"
"v_mfma_f32_16x16x32_bf16 v[132:135], a[164:167], v[40:43], v[132:135]\n"
"v_mfma_f32_16x16x32_bf16 v[136:139], a[168:171], v[40:43], v[136:139]\n"
"v_mfma_f32_16x16x32_bf16 v[140:143], a[172:175], v[40:43], v[140:143]\n"
"s_waitcnt vmcnt(18)\n"
"v_mfma_f32_16x16x32_bf16 v[128:131], a[176:179], v[44:47], v[128:131]\n"
"v_mfma_f32_16x16x32_bf16 v[132:135], a[180:183], v[44:47], v[132:135]\n"
"v_mfma_f32_16x16x32_bf16 v[136:139], a[184:187], v[44:47], v[136:139]\n"
"v_mfma_f32_16x16x32_bf16 v[140:143], a[188:191], v[44:47], v[140:143]\n"
"s_waitcnt vmcnt(17)\n"
"v_mfma_f32_16x16x32_bf16 v[128:131], a[192:195], v[48:51], v[128:131]\n"
"v_mfma_f32_16x16x32_bf16 v[132:135], a[196:199], v[48:51], v[132:135]\n"
"v_mfma_f32_16x16x32_bf16 v[136:139], a[200:203], v[48:51], v[136:139]\n"
"v_mfma_f32_16x16x32_bf16 v[140:143], a[204:207], v[48:51], v[140:143]\n"
"s_waitcnt vmcnt(16)\n"
"v_mfma_f32_16x16x32_bf16 v[128:131], a[208:211], v[52:55], v[128:131]\n"
"v_mfma_f32_16x16x32_bf16 v[132:135], a[212:215], v[52:55], v[132:135]\n"
"v_mfma_f32_16x16x32_bf16 v[136:139], a[216:219], v[52:55], v[136:139]\n"
"v_mfma_f32_16x16x32_bf16 v[140:143], a[220:223], v[52:55], v[140:143]\n"
"s_waitcnt vmcnt(15)\n"
"v_mfma_f32_16x16x32_bf16 v[144:147], a[0:3], v[56:59], 0\n"
"v_mfma_f32_16x16x32_bf16 v[148:151], a[4:7], v[56:59], 0\n"
"v_mfma_f32_16x16x32_bf16 v[152:155], a[8:11], v[56:59], 0\n"
"v_mfma_f32_16x16x32_bf16 v[156:159], a[12:15], v[56:59], 0\n"
"s_waitcnt vmcnt(14)\n"
"v_mfma_f32_16x16x32_bf16 v[144:147], a[16:19], v[60:63], v[144:147]\n"
"ds_write_b128 v122, v[128:131] offset:0\n"
"v_mfma_f32_16x16x32_bf16 v[148:151], a[20:23], v[60:63], v[148:151]\n"
"v_mfma_f32_16x16x32_bf16 v[152:155], a[24:27], v[60:63], v[152:155]\n"
"v_mfma_f32_16x16x32_bf16 v[156:159], a[28:31], v[60:63], v[156:159]\n"
"s_waitcnt vmcnt(13)\n"
"v_mfma_f32_16x16x32_bf16 v[144:147], a[32:35], v[64:67], v[144:147]\n"
"ds_write_b128 v123, v[132:135] offset:1024\n"
"v_mfma_f32_16x16x32_bf16 v[148:151], a[36:39], v[64:67], v[148:151]\n"
"v_mfma_f32_16x16x32_bf16 v[152:155], a[40:43], v[64:67], v[152:155]\n"
"v_mfma_f32_16x16x32_bf16 v[156:159], a[44:47], v[64:67], v[156:159]\n"
"s_waitcnt vmcnt(12)\n"
"v_mfma_f32_16x16x32_bf16 v[144:147], a[48:51], v[68:71], v[144:147]\n"
"ds_write_b128 v124, v[136:139] offset:2048\n"
"v_mfma_f32_16x16x32_bf16 v[148:151], a[52:55], v[68:71], v[148:151]\n"
"v_mfma_f32_16x16x32_bf16 v[152:155], a[56:59], v[68:71], v[152:155]\n"
"v_mfma_f32_16x16x32_bf16 v[156:159], a[60:63], v[68:71], v[156:159]\n"
"s_waitcnt vmcnt(11)\n"
"v_mfma_f32_16x16x32_bf16 v[144:147], a[64:67], v[72:75], v[144:147]\n"
"ds_write_b128 v125, v[140:143] offset:3072\n"
"v_mfma_f32_16x16x32_bf16 v[148:151], a[68:71], v[72:75], v[148:151]\n"
"v_mfma_f32_16x16x32_bf16 v[152:155], a[72:75], v[72:75], v[152:155]\n"
"v_mfma_f32_16x16x32_bf16 v[156:159], a[76:79], v[72:75], v[156:159]\n"
"s_waitcnt vmcnt(10)\n"
"v_mfma_f32_16x16x32_bf16 v[144:147], a[80:83], v[76:79], v[144:147]\n"
"v_mfma_f32_16x16x32_bf16 v[148:151], a[84:87], v[76:79], v[148:151]\n"
"v_mfma_f32_16x16x32_bf16 v[152:155], a[88:91], v[76:79], v[152:155]\n"
"v_mfma_f32_16x16x32_bf16 v[156:159], a[92:95], v[76:79], v[156:159]\n"
"s_waitcnt vmcnt(7)\n"
"v_mfma_f32_16x16x32_bf16 v[144:147], a[96:99], v[0:3], v[144:147]\n"
"v_mfma_f32_16x16x32_bf16 v[148:151], a[100:103], v[0:3], v[148:151]\n"
"v_mfma_f32_16x16x32_bf16 v[152:155], a[104:107], v[0:3], v[152:155]\n"
"v_mfma_f32_16x16x32_bf16 v[156:159], a[108:111], v[0:3], v[156:159]\n"
"s_waitcnt vmcnt(6)\n"
"v_mfma_f32_16x16x32_bf16 v[144:147], a[112:115], v[4:7], v[144:147]\n"
"v_mfma_f32_16x16x32_bf16 v[148:151], a[116:119], v[4:7], v[148:151]\n"
"v_mfma_f32_16x16x32_bf16 v[152:155], a[120:123], v[4:7], v[152:155]\n"
"v_mfma_f32_16x16x32_bf16 v[156:159], a[124:127], v[4:7], v[156:159]\n"
"s_waitcnt vmcnt(5)\n"
"v_mfma_f32_16x16x32_bf16 v[144:147], a[128:131], v[8:11], v[144:147]\n"
"v_mfma_f32_16x16x32_bf16 v[148:151], a[132:135], v[8:11], v[148:151]\n"
"v_mfma_f32_16x16x32_bf16 v[152:155], a[136:139], v[8:11], v[152:155]\n"
"v_mfma_f32_16x16x32_bf16 v[156:159], a[140:143], v[8:11], v[156:159]\n"
"s_waitcnt vmcnt(4)\n"
"v_mfma_f32_16x16x32_bf16 v[144:147], a[144:147], v[12:15], v[144:147]\n"
"v_mfma_f32_16x16x32_bf16 v[148:151], a[148:151], v[12:15], v[148:151]\n"
"v_mfma_f32_16x16x32_bf16 v[152:155], a[152:155], v[12:15], v[152:155]\n"
"v_mfma_f32_16x16x32_bf16 v[156:159], a[156:159], v[12:15], v[156:159]\n"
"s_waitcnt vmcnt(3)\n"
"v_mfma_f32_16x16x32_bf16 v[144:147], a[160:163], v[16:19], v[144:147]\n"
"v_mfma_f32_16x16x32_bf16 v[148:151], a[164:167], v[16:19], v[148:151]\n"
"v_mfma_f32_16x16x32_bf16 v[152:155], a[168:171], v[16:19], v[152:155]\n"
"v_mfma_f32_16x16x32_bf16 v[156:159], a[172:175], v[16:19], v[156:159]\n"
"s_waitcnt vmcnt(2)\n"
"v_mfma_f32_16x16x32_bf16 v[144:147], a[176:179], v[20:23], v[144:147]\n"
"v_mfma_f32_16x16x32_bf16 v[148:151], a[180:183], v[20:23], v[148:151]\n"
"v_mfma_f32_16x16x32_bf16 v[152:155], a[184:187], v[20:23], v[152:155]\n"
"v_mfma_f32_16x16x32_bf16 v[156:159], a[188:191], v[20:23], v[156:159]\n"
"s_waitcnt vmcnt(1)\n"
"v_mfma_f32_16x16x32_bf16 v[144:147], a[192:195], v[24:27], v[144:147]\n"
"v_mfma_f32_16x16x32_bf16 v[148:151], a[196:199], v[24:27], v[148:151]\n"
"v_mfma_f32_16x16x32_bf16 v[152:155], a[200:203], v[24:27], v[152:155]\n"
"v_mfma_f32_16x16x32_bf16 v[156:159], a[204:207], v[24:27], v[156:159]\n"
"s_waitcnt vmcnt(0)\n"
"v_mfma_f32_16x16x32_bf16 v[144:147], a[208:211], v[28:31], v[144:147]\n"
"v_mfma_f32_16x16x32_bf16 v[148:151], a[212:215], v[28:31], v[148:151]\n"
"v_mfma_f32_16x16x32_bf16 v[152:155], a[216:219], v[28:31], v[152:155]\n"
"v_mfma_f32_16x16x32_bf16 v[156:159], a[220:223], v[28:31], v[156:159]\n"
"ds_write_b128 v122, v[144:147] offset:32768\n"
"ds_write_b128 v123, v[148:151] offset:33792\n"
"s_nop 2\n"
"ds_write_b128 v124, v[152:155] offset:34816\n"
"s_nop 1\n"
"ds_write_b128 v125, v[156:159] offset:35840\n"
"v_xor_b32 v122, 16384, v122\n"
"v_xor_b32 v123, 16384, v123\n"
"v_xor_b32 v124, 16384, v124\n"
"v_xor_b32 v125, 16384, v125\n"
"s_add_u32 s8, s8, 4\n"
"v_and_b32 v163, 0xffff0000, v161\n"
"v_lshlrev_b32 v162, 16, v161\n"
"v_and_b32 v161, 0xffff0000, v160\n"
"v_lshlrev_b32 v160, 16, v160\n"
"v_and_b32 v167, 0xffff0000, v165\n"
"v_lshlrev_b32 v166, 16, v165\n"
"v_and_b32 v165, 0xffff0000, v164\n"
"v_lshlrev_b32 v164, 16, v164\n"
"s_waitcnt lgkmcnt(0)\n"
"s_barrier\n"
"ds_read_b128 v[0:3], v117 offset:0\n"
"ds_read_b128 v[4:7], v117 offset:4096\n"
"ds_read_b128 v[8:11], v117 offset:8192\n"
"ds_read_b128 v[12:15], v117 offset:12288\n"
"ds_read_b128 v[16:19], v117 offset:32768\n"
"ds_read_b128 v[20:23], v117 offset:36864\n"
"ds_read_b128 v[24:27], v117 offset:40960\n"
"ds_read_b128 v[28:31], v117 offset:45056\n"
"v_xor_b32 v117, 16384, v117\n"
"s_waitcnt lgkmcnt(7)\n"
"v_add_f32 v160, v160, v0\n"
"v_add_f32 v161, v161, v1\n"
"v_add_f32 v162, v162, v2\n"
"v_add_f32 v163, v163, v3\n"
"s_waitcnt lgkmcnt(6)\n"
"v_add_f32 v160, v160, v4\n"
"v_add_f32 v161, v161, v5\n"
"v_add_f32 v162, v162, v6\n"
"v_add_f32 v163, v163, v7\n"
"s_waitcnt lgkmcnt(5)\n"
"v_add_f32 v160, v160, v8\n"
"v_add_f32 v161, v161, v9\n"
"v_add_f32 v162, v162, v10\n"
"v_add_f32 v163, v163, v11\n"
"s_waitcnt lgkmcnt(4)\n"
"v_add_f32 v160, v160, v12\n"
"v_add_f32 v161, v161, v13\n"
"v_add_f32 v162, v162, v14\n"
"v_add_f32 v163, v163, v15\n"
"buffer_store_dwordx4 v[160:163], v114, s[60:63], s68 offen sc1\n"
"s_waitcnt lgkmcnt(3)\n"
"v_add_f32 v164, v164, v16\n"
"v_add_f32 v165, v165, v17\n"
"v_add_f32 v166, v166, v18\n"
"v_add_f32 v167, v167, v19\n"
"s_waitcnt lgkmcnt(2)\n"
"v_add_f32 v164, v164, v20\n"
"v_add_f32 v165, v165, v21\n"
"v_add_f32 v166, v166, v22\n"
"v_add_f32 v167, v167, v23\n"
"s_waitcnt lgkmcnt(1)\n"
"v_add_f32 v164, v164, v24\n"
"v_add_f32 v165, v165, v25\n"
"v_add_f32 v166, v166, v26\n"
"v_add_f32 v167, v167, v27\n"
"s_waitcnt lgkmcnt(0)\n"
"v_add_f32 v164, v164, v28\n"
"v_add_f32 v165, v165, v29\n"
"v_add_f32 v166, v166, v30\n"
"v_add_f32 v167, v167, v31\n"
"buffer_store_dwordx4 v[164:167], v114, s[60:63], s69 offen sc1\n"
"s_add_u32 s70, s12, s15\n"
"s_cmp_lt_u32 s8, 128\n"
"s_cbranch_scc1 .Ldtop99_%=\n"
"s_waitcnt vmcnt(0)\n"
"v_mov_b32 v168, s34\n"
"v_mov_b32 v169, s35\n"
"v_mov_b32 v170, s34\n"
"v_mov_b32 v171, s35\n"
"s_mov_b64 exec, 1\n"
"buffer_store_dwordx4 v[168:171], off, s[60:63], s70 sc1\n"
"s_mov_b64 exec, -1\n"
"s_mov_b32 s70, 0x6000000\n"
"s_endpgm\n"
".Lup8_%=:\n"
"s_and_b32 s38, s65, 15\n"
"s_cmp_lt_u32 s38, 14\n"
"s_cbranch_scc1 .Lgo102_%=\n"
"s_endpgm\n"
".Lgo102_%=:\n"
"s_mov_b32 s40, s20\n"
"s_and_b32 s41, s21, 0xffff\n"
"s_mov_b32 s42, 0x1c0000\n"
"s_mov_b32 s43, 0x20000\n"
"s_mov_b32 s44, s22\n"
"s_and_b32 s45, s23, 0xffff\n"
"s_mov_b32 s46, 0x1c0000\n"
"s_mov_b32 s47, 0x20000\n"
"v_lshlrev_b32 v80, 4, v119\n"
"s_mov_b32 s9, 0\n"
"s_mov_b32 s8, 0\n"
"v_mov_b32 v81, v80\n"
"s_mov_b32 s12, 0xc0135761\n"
"v_mov_b32 v82, 0xbdd2d3e8\n"
"s_lshl_b32 s64, s38, 2\n"
"s_add_u32 s64, s64, s37\n"
"s_lshl_b32 s15, s38, 4\n"
"s_add_u32 s15, s15, s37\n"
"s_lshl_b32 s15, s15, 3\n"
"s_add_u32 s15, s15, 4096\n"
"s_mul_i32 s65, s64, 37\n"
"s_lshr_b32 s65, s65, 9\n"
"s_lshl_b32 s65, s65, 1\n"
"s_add_u32 s64, s64, s65\n"
"s_add_u32 s64, s64, 128\n"
"s_lshl_b32 s67, s38, 8\n"
"s_lshr_b32 s74, s64, 3\n"
"s_lshl_b32 s74, s74, 13\n"
"s_lshl_b32 s13, s64, 10\n"
"s_add_u32 s13, s13, s74\n"
"s_add_u32 s13, s13, 8388608\n"
"s_sleep 100\n"
"v_lshrrev_b32 v108, 4, v119\n"
"v_and_b32 v109, 1, v108\n"
"s_lshl_b32 s73, s37, 1\n"
"v_or_b32 v109, s73, v109\n"
"v_lshlrev_b32 v109, 1, v109\n"
"v_and_b32 v110, 15, v119\n"
"v_xor_b32 v109, v109, v110\n"
"v_lshlrev_b32 v109, 4, v109\n"
"s_lshl_b32 s73, s37, 2\n"
"v_add_u32 v108, s73, v108\n"
"v_mul_u32_u24 v108, 3584, v108\n"
"v_add3_u32 v108, v108, v109, s67\n"
"s_lshl_b32 s73, s37, 12\n"
"s_add_u32 s73, s73, 32768\n"
"v_add_u32 v109, 56320, v108\n"
"v_add_u32 v110, 112640, v108\n"
"v_add_u32 v111, 168960, v108\n"
"s_add_u32 m0, s73, 0\n"
"s_mov_b32 s72, 0\n"
"buffer_load_dwordx4 v108, s[40:43], s72 offen offset:0 lds\n"
"buffer_load_dwordx4 v109, s[40:43], s72 offen offset:1024 lds\n"
"buffer_load_dwordx4 v110, s[40:43], s72 offen offset:2048 lds\n"
"buffer_load_dwordx4 v111, s[40:43], s72 offen offset:3072 lds\n"
"s_add_u32 m0, s73, 16384\n"
"s_mov_b32 s72, 229376\n"
"buffer_load_dwordx4 v108, s[40:43], s72 offen offset:0 lds\n"
"buffer_load_dwordx4 v109, s[40:43], s72 offen offset:1024 lds\n"
"buffer_load_dwordx4 v110, s[40:43], s72 offen offset:2048 lds\n"
"buffer_load_dwordx4 v111, s[40:43], s72 offen offset:3072 lds\n"
"s_add_u32 m0, s73, 32768\n"
"s_mov_b32 s72, 458752\n"
"buffer_load_dwordx4 v108, s[40:43], s72 offen offset:0 lds\n"
"buffer_load_dwordx4 v109, s[40:43], s72 offen offset:1024 lds\n"
"buffer_load_dwordx4 v110, s[40:43], s72 offen offset:2048 lds\n"
"buffer_load_dwordx4 v111, s[40:43], s72 offen offset:3072 lds\n"
"s_add_u32 m0, s73, 49152\n"
"s_mov_b32 s72, 688128\n"
"buffer_load_dwordx4 v108, s[40:43], s72 offen offset:0 lds\n"
"buffer_load_dwordx4 v109, s[40:43], s72 offen offset:1024 lds\n"
"buffer_load_dwordx4 v110, s[40:43], s72 offen offset:2048 lds\n"
"buffer_load_dwordx4 v111, s[40:43], s72 offen offset:3072 lds\n"
"s_add_u32 m0, s73, 65536\n"
"s_mov_b32 s72, 917504\n"
"buffer_load_dwordx4 v108, s[40:43], s72 offen offset:0 lds\n"
"buffer_load_dwordx4 v109, s[40:43], s72 offen offset:1024 lds\n"
"buffer_load_dwordx4 v110, s[40:43], s72 offen offset:2048 lds\n"
"buffer_load_dwordx4 v111, s[40:43], s72 offen offset:3072 lds\n"
"s_add_u32 m0, s73, 81920\n"
"s_mov_b32 s72, 1146880\n"
"buffer_load_dwordx4 v108, s[40:43], s72 offen offset:0 lds\n"
"buffer_load_dwordx4 v109, s[40:43], s72 offen offset:1024 lds\n"
"buffer_load_dwordx4 v110, s[40:43], s72 offen offset:2048 lds\n"
"buffer_load_dwordx4 v111, s[40:43], s72 offen offset:3072 lds\n"
"s_add_u32 m0, s73, 98304\n"
"s_mov_b32 s72, 1376256\n"
"buffer_load_dwordx4 v108, s[40:43], s72 offen offset:0 lds\n"
"buffer_load_dwordx4 v109, s[40:43], s72 offen offset:1024 lds\n"
"buffer_load_dwordx4 v110, s[40:43], s72 offen offset:2048 lds\n"
"buffer_load_dwordx4 v111, s[40:43], s72 offen offset:3072 lds\n"
"v_bfe_u32 v112, v119, 4, 1\n"
"v_bfe_u32 v113, v119, 3, 1\n"
"v_lshl_add_u32 v112, v113, 1, v112\n"
"v_bfe_u32 v113, v119, 2, 1\n"
"v_lshl_add_u32 v114, v112, 1, v113\n"
"v_lshlrev_b32 v112, 12, v112\n"
"v_lshl_add_u32 v112, v113, 8, v112\n"
"v_bfe_u32 v113, v119, 5, 1\n"
"v_lshl_add_u32 v112, v113, 10, v112\n"
"v_and_b32 v113, 3, v119\n"
"v_lshl_add_u32 v112, v113, 3, v112\n"
"s_lshl_b32 s72, s37, 1\n"
"v_xor_b32 v114, s72, v114\n"
"v_lshl_add_u32 v112, v114, 5, v112\n"
"v_add_u32 v112, 32768, v112\n"
"v_xor_b32 v113, 32, v112\n"
"v_add_u32 v114, 0x10000, v112\n"
"v_add_u32 v115, 0x10000, v113\n"
"s_waitcnt vmcnt(24) lgkmcnt(0)\n"
"s_barrier\n"
"s_add_u32 m0, s73, 114688\n"
"s_mov_b32 s72, 1605632\n"
"buffer_load_dwordx4 v108, s[40:43], s72 offen offset:0 lds\n"
"buffer_load_dwordx4 v109, s[40:43], s72 offen offset:1024 lds\n"
"buffer_load_dwordx4 v110, s[40:43], s72 offen offset:2048 lds\n"
"buffer_load_dwordx4 v111, s[40:43], s72 offen offset:3072 lds\n"
"ds_read_b64_tr_b16 a[0:1], v112 offset:0\n"
"ds_read_b64_tr_b16 a[2:3], v112 offset:512\n"
"ds_read_b64_tr_b16 a[4:5], v112 offset:2048\n"
"ds_read_b64_tr_b16 a[6:7], v112 offset:2560\n"
"ds_read_b64_tr_b16 a[8:9], v113 offset:0\n"
"ds_read_b64_tr_b16 a[10:11], v113 offset:512\n"
"ds_read_b64_tr_b16 a[12:13], v113 offset:2048\n"
"ds_read_b64_tr_b16 a[14:15], v113 offset:2560\n"
"s_waitcnt vmcnt(24) lgkmcnt(0)\n"
"s_barrier\n"
"s_add_u32 m0, s73, 0\n"
"s_mov_b32 s72, 0\n"
"buffer_load_dwordx4 v108, s[44:47], s72 offen offset:0 lds\n"
"buffer_load_dwordx4 v109, s[44:47], s72 offen offset:1024 lds\n"
"buffer_load_dwordx4 v110, s[44:47], s72 offen offset:2048 lds\n"
"buffer_load_dwordx4 v111, s[44:47], s72 offen offset:3072 lds\n"
"ds_read_b64_tr_b16 a[16:17], v112 offset:16384\n"
"ds_read_b64_tr_b16 a[18:19], v112 offset:16896\n"
"ds_read_b64_tr_b16 a[20:21], v112 offset:18432\n"
"ds_read_b64_tr_b16 a[22:23], v112 offset:18944\n"
"ds_read_b64_tr_b16 a[24:25], v113 offset:16384\n"
"ds_read_b64_tr_b16 a[26:27], v113 offset:16896\n"
"ds_read_b64_tr_b16 a[28:29], v113 offset:18432\n"
"ds_read_b64_tr_b16 a[30:31], v113 offset:18944\n"
"s_waitcnt vmcnt(24) lgkmcnt(0)\n"
"s_barrier\n"
"s_add_u32 m0, s73, 16384\n"
"s_mov_b32 s72, 229376\n"
"buffer_load_dwordx4 v108, s[44:47], s72 offen offset:0 lds\n"
"buffer_load_dwordx4 v109, s[44:47], s72 offen offset:1024 lds\n"
"buffer_load_dwordx4 v110, s[44:47], s72 offen offset:2048 lds\n"
"buffer_load_dwordx4 v111, s[44:47], s72 offen offset:3072 lds\n"
"ds_read_b64_tr_b16 a[32:33], v112 offset:32768\n"
"ds_read_b64_tr_b16 a[34:35], v112 offset:33280\n"
"ds_read_b64_tr_b16 a[36:37], v112 offset:34816\n"
"ds_read_b64_tr_b16 a[38:39], v112 offset:35328\n"
"ds_read_b64_tr_b16 a[40:41], v113 offset:32768\n"
"ds_read_b64_tr_b16 a[42:43], v113 offset:33280\n"
"ds_read_b64_tr_b16 a[44:45], v113 offset:34816\n"
"ds_read_b64_tr_b16 a[46:47], v113 offset:35328\n"
"s_waitcnt vmcnt(24) lgkmcnt(0)\n"
"s_barrier\n"
"s_add_u32 m0, s73, 32768\n"
"s_mov_b32 s72, 458752\n"
"buffer_load_dwordx4 v108, s[44:47], s72 offen offset:0 lds\n"
"buffer_load_dwordx4 v109, s[44:47], s72 offen offset:1024 lds\n"
"buffer_load_dwordx4 v110, s[44:47], s72 offen offset:2048 lds\n"
"buffer_load_dwordx4 v111, s[44:47], s72 offen offset:3072 lds\n"
"ds_read_b64_tr_b16 a[48:49], v112 offset:49152\n"
"ds_read_b64_tr_b16 a[50:51], v112 offset:49664\n"
"ds_read_b64_tr_b16 a[52:53], v112 offset:51200\n"
"ds_read_b64_tr_b16 a[54:55], v112 offset:51712\n"
"ds_read_b64_tr_b16 a[56:57], v113 offset:49152\n"
"ds_read_b64_tr_b16 a[58:59], v113 offset:49664\n"
"ds_read_b64_tr_b16 a[60:61], v113 offset:51200\n"
"ds_read_b64_tr_b16 a[62:63], v113 offset:51712\n"
"s_waitcnt vmcnt(24) lgkmcnt(0)\n"
"s_barrier\n"
"s_add_u32 m0, s73, 49152\n"
"s_mov_b32 s72, 688128\n"
"buffer_load_dwordx4 v108, s[44:47], s72 offen offset:0 lds\n"
"buffer_load_dwordx4 v109, s[44:47], s72 offen offset:1024 lds\n"
"buffer_load_dwordx4 v110, s[44:47], s72 offen offset:2048 lds\n"
"buffer_load_dwordx4 v111, s[44:47], s72 offen offset:3072 lds\n"
"ds_read_b64_tr_b16 a[64:65], v114 offset:0\n"
"ds_read_b64_tr_b16 a[66:67], v114 offset:512\n"
"ds_read_b64_tr_b16 a[68:69], v114 offset:2048\n"
"ds_read_b64_tr_b16 a[70:71], v114 offset:2560\n"
"ds_read_b64_tr_b16 a[72:73], v115 offset:0\n"
"ds_read_b64_tr_b16 a[74:75], v115 offset:512\n"
"ds_read_b64_tr_b16 a[76:77], v115 offset:2048\n"
"ds_read_b64_tr_b16 a[78:79], v115 offset:2560\n"
"s_waitcnt vmcnt(24) lgkmcnt(0)\n"
"s_barrier\n"
"s_add_u32 m0, s73, 65536\n"
"s_mov_b32 s72, 917504\n"
"buffer_load_dwordx4 v108, s[44:47], s72 offen offset:0 lds\n"
"buffer_load_dwordx4 v109, s[44:47], s72 offen offset:1024 lds\n"
"buffer_load_dwordx4 v110, s[44:47], s72 offen offset:2048 lds\n"
"buffer_load_dwordx4 v111, s[44:47], s72 offen offset:3072 lds\n"
"ds_read_b64_tr_b16 a[80:81], v114 offset:16384\n"
"ds_read_b64_tr_b16 a[82:83], v114 offset:16896\n"
"ds_read_b64_tr_b16 a[84:85], v114 offset:18432\n"
"ds_read_b64_tr_b16 a[86:87], v114 offset:18944\n"
"ds_read_b64_tr_b16 a[88:89], v115 offset:16384\n"
"ds_read_b64_tr_b16 a[90:91], v115 offset:16896\n"
"ds_read_b64_tr_b16 a[92:93], v115 offset:18432\n"
"ds_read_b64_tr_b16 a[94:95], v115 offset:18944\n"
"s_waitcnt vmcnt(24) lgkmcnt(0)\n"
"s_barrier\n"
"s_add_u32 m0, s73, 81920\n"
"s_mov_b32 s72, 1146880\n"
"buffer_load_dwordx4 v108, s[44:47], s72 offen offset:0 lds\n"
"buffer_load_dwordx4 v109, s[44:47], s72 offen offset:1024 lds\n"
"buffer_load_dwordx4 v110, s[44:47], s72 offen offset:2048 lds\n"
"buffer_load_dwordx4 v111, s[44:47], s72 offen offset:3072 lds\n"
"ds_read_b64_tr_b16 a[96:97], v114 offset:32768\n"
"ds_read_b64_tr_b16 a[98:99], v114 offset:33280\n"
"ds_read_b64_tr_b16 a[100:101], v114 offset:34816\n"
"ds_read_b64_tr_b16 a[102:103], v114 offset:35328\n"
"ds_read_b64_tr_b16 a[104:105], v115 offset:32768\n"
"ds_read_b64_tr_b16 a[106:107], v115 offset:33280\n"
"ds_read_b64_tr_b16 a[108:109], v115 offset:34816\n"
"ds_read_b64_tr_b16 a[110:111], v115 offset:35328\n"
"v_lshlrev_b32 v100, 3, v119\n"
"s_mov_b32 s74, 3146752\n"
"buffer_load_dwordx2 v[96:97], v100, s[60:63], s74 offen sc1\n"
"buffer_load_dwordx2 v[98:99], v100, s[60:63], s74 offen offset:1024 sc1\n"
"s_waitcnt vmcnt(26) lgkmcnt(0)\n"
"s_barrier\n"
"s_add_u32 m0, s73, 98304\n"
"s_mov_b32 s72, 1376256\n"
"buffer_load_dwordx4 v108, s[44:47], s72 offen offset:0 lds\n"
"buffer_load_dwordx4 v109, s[44:47], s72 offen offset:1024 lds\n"
"buffer_load_dwordx4 v110, s[44:47], s72 offen offset:2048 lds\n"
"buffer_load_dwordx4 v111, s[44:47], s72 offen offset:3072 lds\n"
"ds_read_b64_tr_b16 a[112:113], v114 offset:49152\n"
"ds_read_b64_tr_b16 a[114:115], v114 offset:49664\n"
"ds_read_b64_tr_b16 a[116:117], v114 offset:51200\n"
"ds_read_b64_tr_b16 a[118:119], v114 offset:51712\n"
"ds_read_b64_tr_b16 a[120:121], v115 offset:49152\n"
"ds_read_b64_tr_b16 a[122:123], v115 offset:49664\n"
"ds_read_b64_tr_b16 a[124:125], v115 offset:51200\n"
"ds_read_b64_tr_b16 a[126:127], v115 offset:51712\n"
"s_waitcnt vmcnt(26) lgkmcnt(0)\n"
"s_barrier\n"
"s_add_u32 m0, s73, 114688\n"
"s_mov_b32 s72, 1605632\n"
"buffer_load_dwordx4 v108, s[44:47], s72 offen offset:0 lds\n"
"buffer_load_dwordx4 v109, s[44:47], s72 offen offset:1024 lds\n"
"buffer_load_dwordx4 v110, s[44:47], s72 offen offset:2048 lds\n"
"buffer_load_dwordx4 v111, s[44:47], s72 offen offset:3072 lds\n"
"ds_read_b64_tr_b16 a[128:129], v112 offset:0\n"
"ds_read_b64_tr_b16 a[130:131], v112 offset:512\n"
"ds_read_b64_tr_b16 a[132:133], v112 offset:2048\n"
"ds_read_b64_tr_b16 a[134:135], v112 offset:2560\n"
"ds_read_b64_tr_b16 a[136:137], v113 offset:0\n"
"ds_read_b64_tr_b16 a[138:139], v113 offset:512\n"
"ds_read_b64_tr_b16 a[140:141], v113 offset:2048\n"
"ds_read_b64_tr_b16 a[142:143], v113 offset:2560\n"
"s_waitcnt vmcnt(26) lgkmcnt(0)\n"
"s_barrier\n"
"ds_read_b64_tr_b16 a[144:145], v112 offset:16384\n"
"ds_read_b64_tr_b16 a[146:147], v112 offset:16896\n"
"ds_read_b64_tr_b16 a[148:149], v112 offset:18432\n"
"ds_read_b64_tr_b16 a[150:151], v112 offset:18944\n"
"ds_read_b64_tr_b16 a[152:153], v113 offset:16384\n"
"ds_read_b64_tr_b16 a[154:155], v113 offset:16896\n"
"ds_read_b64_tr_b16 a[156:157], v113 offset:18432\n"
"ds_read_b64_tr_b16 a[158:159], v113 offset:18944\n"
"s_waitcnt vmcnt(22) lgkmcnt(0)\n"
"s_barrier\n"
"ds_read_b64_tr_b16 a[160:161], v112 offset:32768\n"
"ds_read_b64_tr_b16 a[162:163], v112 offset:33280\n"
"ds_read_b64_tr_b16 a[164:165], v112 offset:34816\n"
"ds_read_b64_tr_b16 a[166:167], v112 offset:35328\n"
"ds_read_b64_tr_b16 a[168:169], v113 offset:32768\n"
"ds_read_b64_tr_b16 a[170:171], v113 offset:33280\n"
"ds_read_b64_tr_b16 a[172:173], v113 offset:34816\n"
"ds_read_b64_tr_b16 a[174:175], v113 offset:35328\n"
"s_waitcnt vmcnt(18) lgkmcnt(0)\n"
"s_barrier\n"
"ds_read_b64_tr_b16 a[176:177], v112 offset:49152\n"
"ds_read_b64_tr_b16 a[178:179], v112 offset:49664\n"
"ds_read_b64_tr_b16 a[180:181], v112 offset:51200\n"
"ds_read_b64_tr_b16 a[182:183], v112 offset:51712\n"
"ds_read_b64_tr_b16 a[184:185], v113 offset:49152\n"
"ds_read_b64_tr_b16 a[186:187], v113 offset:49664\n"
"ds_read_b64_tr_b16 a[188:189], v113 offset:51200\n"
"ds_read_b64_tr_b16 a[190:191], v113 offset:51712\n"
"s_waitcnt vmcnt(14) lgkmcnt(0)\n"
"s_barrier\n"
"ds_read_b64_tr_b16 a[192:193], v114 offset:0\n"
"ds_read_b64_tr_b16 a[194:195], v114 offset:512\n"
"ds_read_b64_tr_b16 a[196:197], v114 offset:2048\n"
"ds_read_b64_tr_b16 a[198:199], v114 offset:2560\n"
"ds_read_b64_tr_b16 a[200:201], v115 offset:0\n"
"ds_read_b64_tr_b16 a[202:203], v115 offset:512\n"
"ds_read_b64_tr_b16 a[204:205], v115 offset:2048\n"
"ds_read_b64_tr_b16 a[206:207], v115 offset:2560\n"
"s_waitcnt vmcnt(10) lgkmcnt(0)\n"
"s_barrier\n"
"ds_read_b64_tr_b16 a[208:209], v114 offset:16384\n"
"ds_read_b64_tr_b16 a[210:211], v114 offset:16896\n"
"ds_read_b64_tr_b16 a[212:213], v114 offset:18432\n"
"ds_read_b64_tr_b16 a[214:215], v114 offset:18944\n"
"ds_read_b64_tr_b16 a[216:217], v115 offset:16384\n"
"ds_read_b64_tr_b16 a[218:219], v115 offset:16896\n"
"ds_read_b64_tr_b16 a[220:221], v115 offset:18432\n"
"ds_read_b64_tr_b16 a[222:223], v115 offset:18944\n"
"s_waitcnt vmcnt(8)\n"
"s_mov_b32 s78, 16384\n"
".Law103_%=:\n"
"v_xor_b32 v96, s34, v96\n"
"v_xor_b32 v97, s35, v97\n"
"v_and_or_b32 v96, v97, -4, v96\n"
"v_cmp_ne_u32 vcc, 0, v96\n"
"v_cmp_ne_u64 s[64:65], s[34:35], v[98:99]\n"
"s_nop 0\n"
"s_or_b64 vcc, vcc, s[64:65]\n"
"s_cbranch_vccz .Laok104_%=\n"
"s_sub_u32 s78, s78, 1\n"
"s_cbranch_scc1 .Lfail5_%=\n"
"s_sleep 8\n"
"buffer_load_dwordx2 v[96:97], v100, s[60:63], s74 offen sc1\n"
"buffer_load_dwordx2 v[98:99], v100, s[60:63], s74 offen offset:1024 sc1\n"
"s_waitcnt vmcnt(0)\n"
"s_branch .Law103_%=\n"
".Laok104_%=:\n"
"s_mov_b32 s77, 0\n"
"v_cmp_lt_u32 vcc, 1, v97\n"
"v_and_b32 v97, 1, v97\n"
"s_and_b32 s64, vcc_lo, 0xf00ff00f\n"
"s_and_b32 s65, vcc_hi, 0xf00ff00f\n"
"s_bcnt1_i32_b64 s64, s[64:65]\n"
"s_cmp_ge_u32 s64, 16\n"
"s_addc_u32 s77, s77, s77\n"
"s_and_b32 s64, vcc_lo, 0xff00ff0\n"
"s_and_b32 s65, vcc_hi, 0xff00ff0\n"
"s_bcnt1_i32_b64 s64, s[64:65]\n"
"s_cmp_ge_u32 s64, 16\n"
"s_addc_u32 s77, s77, s77\n"
"v_cmp_ne_u32 vcc, 0, v97\n"
"s_nop 0\n"
"s_and_b32 s64, vcc_lo, 0xffffffff\n"
"s_and_b32 s65, vcc_hi, 0xffffffff\n"
"s_bcnt1_i32_b64 s64, s[64:65]\n"
"s_cmp_ge_u32 s64, 32\n"
"s_addc_u32 s77, s77, s77\n"
"s_lshl_b32 s71, s8, 15\n"
"s_lshl_b32 s65, s37, 13\n"
"s_add_u32 s71, s71, s65\n"
"s_add_u32 s71, s71, 4194304\n"
"s_add_u32 m0, s65, s9\n"
"s_nop 0\n"
"buffer_load_dwordx4 v80, s[60:63], s71 offen offset:0 sc1 lds\n"
"buffer_load_dwordx4 v80, s[60:63], s71 offen offset:1024 sc1 lds\n"
"buffer_load_dwordx4 v80, s[60:63], s71 offen offset:2048 sc1 lds\n"
"buffer_load_dwordx4 v80, s[60:63], s71 offen offset:3072 sc1 lds\n"
"s_add_u32 m0, m0, 4096\n"
"s_add_u32 s71, s71, 4096\n"
"buffer_load_dwordx4 v80, s[60:63], s71 offen offset:0 sc1 lds\n"
"buffer_load_dwordx4 v80, s[60:63], s71 offen offset:1024 sc1 lds\n"
"buffer_load_dwordx4 v80, s[60:63], s71 offen offset:2048 sc1 lds\n"
"buffer_load_dwordx4 v80, s[60:63], s71 offen offset:3072 sc1 lds\n"
"s_waitcnt vmcnt(12) lgkmcnt(0)\n"
"s_barrier\n"
"ds_read_b64_tr_b16 a[224:225], v114 offset:32768\n"
"ds_read_b64_tr_b16 a[226:227], v114 offset:33280\n"
"ds_read_b64_tr_b16 a[228:229], v114 offset:34816\n"
"ds_read_b64_tr_b16 a[230:231], v114 offset:35328\n"
"ds_read_b64_tr_b16 a[232:233], v115 offset:32768\n"
"ds_read_b64_tr_b16 a[234:235], v115 offset:33280\n"
"ds_read_b64_tr_b16 a[236:237], v115 offset:34816\n"
"ds_read_b64_tr_b16 a[238:239], v115 offset:35328\n"
"s_waitcnt vmcnt(8) lgkmcnt(0)\n"
"s_barrier\n"
"ds_read_b64_tr_b16 a[240:241], v114 offset:49152\n"
"ds_read_b64_tr_b16 a[242:243], v114 offset:49664\n"
"ds_read_b64_tr_b16 a[244:245], v114 offset:51200\n"
"ds_read_b64_tr_b16 a[246:247], v114 offset:51712\n"
"ds_read_b64_tr_b16 a[248:249], v115 offset:49152\n"
"ds_read_b64_tr_b16 a[250:251], v115 offset:49664\n"
"ds_read_b64_tr_b16 a[252:253], v115 offset:51200\n"
"ds_read_b64_tr_b16 a[254:255], v115 offset:51712\n"
"s_waitcnt lgkmcnt(0)\n"
"s_barrier\n"
"s_lshl_b32 s11, s8, 1\n"
"s_and_b32 s74, s77, 1\n"
"s_add_u32 s11, s11, s74\n"
"s_lshl_b32 s11, s11, 13\n"
"s_add_u32 s11, s11, 6291456\n"
"s_add_u32 s11, s11, s15\n"
"s_add_u32 s10, s8, 1\n"
"s_min_u32 s10, s10, 63\n"
"s_xor_b32 s9, s9, 32768\n"
"s_waitcnt vmcnt(0) lgkmcnt(0)\n"
"s_barrier\n"
"ds_read_b128 v[64:67], v81 offset:0\n"
"ds_read_b128 v[68:71], v81 offset:16384\n"
"s_waitcnt lgkmcnt(0)\n"
"v_mfma_f32_16x16x32_bf16 v[0:3], a[0:3], v[64:67], 0\n"
"ds_read_b128 v[72:75], v81 offset:1024\n"
"v_mfma_f32_16x16x32_bf16 v[8:11], a[0:3], v[68:71], 0\n"
"ds_read_b128 v[76:79], v81 offset:17408\n"
"v_mfma_f32_16x16x32_bf16 v[4:7], a[8:11], v[64:67], 0\n"
"s_lshl_b32 s71, s10, 15\n"
"s_lshl_b32 s65, s37, 13\n"
"s_add_u32 s71, s71, s65\n"
"s_add_u32 s71, s71, 4194304\n"
"s_add_u32 m0, s65, s9\n"
"v_mfma_f32_16x16x32_bf16 v[12:15], a[8:11], v[68:71], 0\n"
"v_mfma_f32_16x16x32_bf16 v[16:19], a[128:131], v[64:67], 0\n"
"v_mfma_f32_16x16x32_bf16 v[24:27], a[128:131], v[68:71], 0\n"
"buffer_load_dwordx4 v80, s[60:63], s71 offen offset:0 sc1 lds\n"
"v_mfma_f32_16x16x32_bf16 v[20:23], a[136:139], v[64:67], 0\n"
"v_mfma_f32_16x16x32_bf16 v[28:31], a[136:139], v[68:71], 0\n"
"s_waitcnt lgkmcnt(0)\n"
"v_mfma_f32_16x16x32_bf16 v[0:3], a[4:7], v[72:75], v[0:3]\n"
"ds_read_b128 v[64:67], v81 offset:2048\n"
"v_mfma_f32_16x16x32_bf16 v[8:11], a[4:7], v[76:79], v[8:11]\n"
"ds_read_b128 v[68:71], v81 offset:18432\n"
"buffer_load_dwordx4 v80, s[60:63], s71 offen offset:1024 sc1 lds\n"
"v_mfma_f32_16x16x32_bf16 v[4:7], a[12:15], v[72:75], v[4:7]\n"
"v_mfma_f32_16x16x32_bf16 v[12:15], a[12:15], v[76:79], v[12:15]\n"
"v_mfma_f32_16x16x32_bf16 v[16:19], a[132:135], v[72:75], v[16:19]\n"
"v_mfma_f32_16x16x32_bf16 v[24:27], a[132:135], v[76:79], v[24:27]\n"
"buffer_load_dwordx4 v80, s[60:63], s71 offen offset:2048 sc1 lds\n"
"v_mfma_f32_16x16x32_bf16 v[20:23], a[140:143], v[72:75], v[20:23]\n"
"v_mfma_f32_16x16x32_bf16 v[28:31], a[140:143], v[76:79], v[28:31]\n"
"s_waitcnt lgkmcnt(0)\n"
"v_mfma_f32_16x16x32_bf16 v[0:3], a[16:19], v[64:67], v[0:3]\n"
"ds_read_b128 v[72:75], v81 offset:3072\n"
"v_mfma_f32_16x16x32_bf16 v[8:11], a[16:19], v[68:71], v[8:11]\n"
"ds_read_b128 v[76:79], v81 offset:19456\n"
"buffer_load_dwordx4 v80, s[60:63], s71 offen offset:3072 sc1 lds\n"
"v_mfma_f32_16x16x32_bf16 v[4:7], a[24:27], v[64:67], v[4:7]\n"
"v_mfma_f32_16x16x32_bf16 v[12:15], a[24:27], v[68:71], v[12:15]\n"
"v_mfma_f32_16x16x32_bf16 v[16:19], a[144:147], v[64:67], v[16:19]\n"
"v_mfma_f32_16x16x32_bf16 v[24:27], a[144:147], v[68:71], v[24:27]\n"
"s_add_u32 m0, m0, 4096\n"
"s_add_u32 s71, s71, 4096\n"
"buffer_load_dwordx4 v80, s[60:63], s71 offen offset:0 sc1 lds\n"
"v_mfma_f32_16x16x32_bf16 v[20:23], a[152:155], v[64:67], v[20:23]\n"
"v_mfma_f32_16x16x32_bf16 v[28:31], a[152:155], v[68:71], v[28:31]\n"
"s_waitcnt lgkmcnt(0)\n"
"v_mfma_f32_16x16x32_bf16 v[0:3], a[20:23], v[72:75], v[0:3]\n"
"ds_read_b128 v[64:67], v81 offset:4096\n"
"v_mfma_f32_16x16x32_bf16 v[8:11], a[20:23], v[76:79], v[8:11]\n"
"ds_read_b128 v[68:71], v81 offset:20480\n"
"buffer_load_dwordx4 v80, s[60:63], s71 offen offset:1024 sc1 lds\n"
"v_mfma_f32_16x16x32_bf16 v[4:7], a[28:31], v[72:75], v[4:7]\n"
"v_mfma_f32_16x16x32_bf16 v[12:15], a[28:31], v[76:79], v[12:15]\n"
"v_mfma_f32_16x16x32_bf16 v[16:19], a[148:151], v[72:75], v[16:19]\n"
"v_mfma_f32_16x16x32_bf16 v[24:27], a[148:151], v[76:79], v[24:27]\n"
"buffer_load_dwordx4 v80, s[60:63], s71 offen offset:2048 sc1 lds\n"
"v_mfma_f32_16x16x32_bf16 v[20:23], a[156:159], v[72:75], v[20:23]\n"
"v_mfma_f32_16x16x32_bf16 v[28:31], a[156:159], v[76:79], v[28:31]\n"
"s_waitcnt lgkmcnt(0)\n"
"v_mfma_f32_16x16x32_bf16 v[0:3], a[32:35], v[64:67], v[0:3]\n"
"ds_read_b128 v[72:75], v81 offset:5120\n"
"v_mfma_f32_16x16x32_bf16 v[8:11], a[32:35], v[68:71], v[8:11]\n"
"ds_read_b128 v[76:79], v81 offset:21504\n"
"buffer_load_dwordx4 v80, s[60:63], s71 offen offset:3072 sc1 lds\n"
"v_mfma_f32_16x16x32_bf16 v[4:7], a[40:43], v[64:67], v[4:7]\n"
"v_mfma_f32_16x16x32_bf16 v[12:15], a[40:43], v[68:71], v[12:15]\n"
"v_mfma_f32_16x16x32_bf16 v[16:19], a[160:163], v[64:67], v[16:19]\n"
"v_mfma_f32_16x16x32_bf16 v[24:27], a[160:163], v[68:71], v[24:27]\n"
"v_mfma_f32_16x16x32_bf16 v[20:23], a[168:171], v[64:67], v[20:23]\n"
"v_mfma_f32_16x16x32_bf16 v[28:31], a[168:171], v[68:71], v[28:31]\n"
"s_waitcnt lgkmcnt(0)\n"
"v_mfma_f32_16x16x32_bf16 v[0:3], a[36:39], v[72:75], v[0:3]\n"
"ds_read_b128 v[64:67], v81 offset:6144\n"
"v_mfma_f32_16x16x32_bf16 v[8:11], a[36:39], v[76:79], v[8:11]\n"
"ds_read_b128 v[68:71], v81 offset:22528\n"
"v_mfma_f32_16x16x32_bf16 v[4:7], a[44:47], v[72:75], v[4:7]\n"
"v_mfma_f32_16x16x32_bf16 v[12:15], a[44:47], v[76:79], v[12:15]\n"
"v_mfma_f32_16x16x32_bf16 v[16:19], a[164:167], v[72:75], v[16:19]\n"
"v_mfma_f32_16x16x32_bf16 v[24:27], a[164:167], v[76:79], v[24:27]\n"
"v_mfma_f32_16x16x32_bf16 v[20:23], a[172:175], v[72:75], v[20:23]\n"
"v_mfma_f32_16x16x32_bf16 v[28:31], a[172:175], v[76:79], v[28:31]\n"
"s_waitcnt lgkmcnt(0)\n"
"v_mfma_f32_16x16x32_bf16 v[0:3], a[48:51], v[64:67], v[0:3]\n"
"ds_read_b128 v[72:75], v81 offset:7168\n"
"v_mfma_f32_16x16x32_bf16 v[8:11], a[48:51], v[68:71], v[8:11]\n"
"ds_read_b128 v[76:79], v81 offset:23552\n"
"v_mfma_f32_16x16x32_bf16 v[4:7], a[56:59], v[64:67], v[4:7]\n"
"v_mfma_f32_16x16x32_bf16 v[12:15], a[56:59], v[68:71], v[12:15]\n"
"v_mfma_f32_16x16x32_bf16 v[16:19], a[176:179], v[64:67], v[16:19]\n"
"v_mfma_f32_16x16x32_bf16 v[24:27], a[176:179], v[68:71], v[24:27]\n"
"v_mfma_f32_16x16x32_bf16 v[20:23], a[184:187], v[64:67], v[20:23]\n"
"v_mfma_f32_16x16x32_bf16 v[28:31], a[184:187], v[68:71], v[28:31]\n"
"s_waitcnt lgkmcnt(0)\n"
"v_mfma_f32_16x16x32_bf16 v[0:3], a[52:55], v[72:75], v[0:3]\n"
"ds_read_b128 v[64:67], v81 offset:8192\n"
"v_mfma_f32_16x16x32_bf16 v[8:11], a[52:55], v[76:79], v[8:11]\n"
"ds_read_b128 v[68:71], v81 offset:24576\n"
"v_mfma_f32_16x16x32_bf16 v[4:7], a[60:63], v[72:75], v[4:7]\n"
"v_mfma_f32_16x16x32_bf16 v[12:15], a[60:63], v[76:79], v[12:15]\n"
"v_mfma_f32_16x16x32_bf16 v[16:19], a[180:183], v[72:75], v[16:19]\n"
"v_mfma_f32_16x16x32_bf16 v[24:27], a[180:183], v[76:79], v[24:27]\n"
"v_mfma_f32_16x16x32_bf16 v[20:23], a[188:191], v[72:75], v[20:23]\n"
"v_mfma_f32_16x16x32_bf16 v[28:31], a[188:191], v[76:79], v[28:31]\n"
"s_waitcnt lgkmcnt(0)\n"
"v_mfma_f32_16x16x32_bf16 v[0:3], a[64:67], v[64:67], v[0:3]\n"
"ds_read_b128 v[72:75], v81 offset:9216\n"
"v_mfma_f32_16x16x32_bf16 v[8:11], a[64:67], v[68:71], v[8:11]\n"
"ds_read_b128 v[76:79], v81 offset:25600\n"
"v_mfma_f32_16x16x32_bf16 v[4:7], a[72:75], v[64:67], v[4:7]\n"
"v_mfma_f32_16x16x32_bf16 v[12:15], a[72:75], v[68:71], v[12:15]\n"
"v_mfma_f32_16x16x32_bf16 v[16:19], a[192:195], v[64:67], v[16:19]\n"
"v_mfma_f32_16x16x32_bf16 v[24:27], a[192:195], v[68:71], v[24:27]\n"
"v_mfma_f32_16x16x32_bf16 v[20:23], a[200:203], v[64:67], v[20:23]\n"
"v_mfma_f32_16x16x32_bf16 v[28:31], a[200:203], v[68:71], v[28:31]\n"
"s_waitcnt lgkmcnt(0)\n"
"v_mfma_f32_16x16x32_bf16 v[0:3], a[68:71], v[72:75], v[0:3]\n"
"ds_read_b128 v[64:67], v81 offset:10240\n"
"v_mfma_f32_16x16x32_bf16 v[8:11], a[68:71], v[76:79], v[8:11]\n"
"ds_read_b128 v[68:71], v81 offset:26624\n"
"v_mfma_f32_16x16x32_bf16 v[4:7], a[76:79], v[72:75], v[4:7]\n"
"v_mfma_f32_16x16x32_bf16 v[12:15], a[76:79], v[76:79], v[12:15]\n"
"v_mfma_f32_16x16x32_bf16 v[16:19], a[196:199], v[72:75], v[16:19]\n"
"v_mfma_f32_16x16x32_bf16 v[24:27], a[196:199], v[76:79], v[24:27]\n"
"v_mfma_f32_16x16x32_bf16 v[20:23], a[204:207], v[72:75], v[20:23]\n"
"v_mfma_f32_16x16x32_bf16 v[28:31], a[204:207], v[76:79], v[28:31]\n"
"s_waitcnt lgkmcnt(0)\n"
"v_mfma_f32_16x16x32_bf16 v[0:3], a[80:83], v[64:67], v[0:3]\n"
"ds_read_b128 v[72:75], v81 offset:11264\n"
"v_mfma_f32_16x16x32_bf16 v[8:11], a[80:83], v[68:71], v[8:11]\n"
"ds_read_b128 v[76:79], v81 offset:27648\n"
"v_mfma_f32_16x16x32_bf16 v[4:7], a[88:91], v[64:67], v[4:7]\n"
"v_mfma_f32_16x16x32_bf16 v[12:15], a[88:91], v[68:71], v[12:15]\n"
"v_mfma_f32_16x16x32_bf16 v[16:19], a[208:211], v[64:67], v[16:19]\n"
"v_mfma_f32_16x16x32_bf16 v[24:27], a[208:211], v[68:71], v[24:27]\n"
"v_mfma_f32_16x16x32_bf16 v[20:23], a[216:219], v[64:67], v[20:23]\n"
"v_mfma_f32_16x16x32_bf16 v[28:31], a[216:219], v[68:71], v[28:31]\n"
"s_waitcnt lgkmcnt(0)\n"
"v_mfma_f32_16x16x32_bf16 v[0:3], a[84:87], v[72:75], v[0:3]\n"
"ds_read_b128 v[64:67], v81 offset:12288\n"
"v_mfma_f32_16x16x32_bf16 v[8:11], a[84:87], v[76:79], v[8:11]\n"
"ds_read_b128 v[68:71], v81 offset:28672\n"
"v_mfma_f32_16x16x32_bf16 v[4:7], a[92:95], v[72:75], v[4:7]\n"
"v_mfma_f32_16x16x32_bf16 v[12:15], a[92:95], v[76:79], v[12:15]\n"
"v_mfma_f32_16x16x32_bf16 v[16:19], a[212:215], v[72:75], v[16:19]\n"
"v_mfma_f32_16x16x32_bf16 v[24:27], a[212:215], v[76:79], v[24:27]\n"
"v_mfma_f32_16x16x32_bf16 v[20:23], a[220:223], v[72:75], v[20:23]\n"
"v_mfma_f32_16x16x32_bf16 v[28:31], a[220:223], v[76:79], v[28:31]\n"
"s_waitcnt lgkmcnt(0)\n"
"v_mfma_f32_16x16x32_bf16 v[0:3], a[96:99], v[64:67], v[0:3]\n"
"ds_read_b128 v[72:75], v81 offset:13312\n"
"v_mfma_f32_16x16x32_bf16 v[8:11], a[96:99], v[68:71], v[8:11]\n"
"ds_read_b128 v[76:79], v81 offset:29696\n"
"v_mfma_f32_16x16x32_bf16 v[4:7], a[104:107], v[64:67], v[4:7]\n"
"v_mfma_f32_16x16x32_bf16 v[12:15], a[104:107], v[68:71], v[12:15]\n"
"v_mfma_f32_16x16x32_bf16 v[16:19], a[224:227], v[64:67], v[16:19]\n"
"v_mfma_f32_16x16x32_bf16 v[24:27], a[224:227], v[68:71], v[24:27]\n"
"v_mfma_f32_16x16x32_bf16 v[20:23], a[232:235], v[64:67], v[20:23]\n"
"v_mfma_f32_16x16x32_bf16 v[28:31], a[232:235], v[68:71], v[28:31]\n"
"s_waitcnt lgkmcnt(0)\n"
"v_mfma_f32_16x16x32_bf16 v[0:3], a[100:103], v[72:75], v[0:3]\n"
"ds_read_b128 v[64:67], v81 offset:14336\n"
"v_mfma_f32_16x16x32_bf16 v[8:11], a[100:103], v[76:79], v[8:11]\n"
"ds_read_b128 v[68:71], v81 offset:30720\n"
"v_mfma_f32_16x16x32_bf16 v[4:7], a[108:111], v[72:75], v[4:7]\n"
"v_mfma_f32_16x16x32_bf16 v[12:15], a[108:111], v[76:79], v[12:15]\n"
"v_mfma_f32_16x16x32_bf16 v[16:19], a[228:231], v[72:75], v[16:19]\n"
"v_mfma_f32_16x16x32_bf16 v[24:27], a[228:231], v[76:79], v[24:27]\n"
"v_mfma_f32_16x16x32_bf16 v[20:23], a[236:239], v[72:75], v[20:23]\n"
"v_mfma_f32_16x16x32_bf16 v[28:31], a[236:239], v[76:79], v[28:31]\n"
"s_waitcnt lgkmcnt(0)\n"
"v_mfma_f32_16x16x32_bf16 v[0:3], a[112:115], v[64:67], v[0:3]\n"
"ds_read_b128 v[72:75], v81 offset:15360\n"
"v_mfma_f32_16x16x32_bf16 v[8:11], a[112:115], v[68:71], v[8:11]\n"
"ds_read_b128 v[76:79], v81 offset:31744\n"
"v_mfma_f32_16x16x32_bf16 v[4:7], a[120:123], v[64:67], v[4:7]\n"
"v_mfma_f32_16x16x32_bf16 v[12:15], a[120:123], v[68:71], v[12:15]\n"
"v_mfma_f32_16x16x32_bf16 v[16:19], a[240:243], v[64:67], v[16:19]\n"
"v_mfma_f32_16x16x32_bf16 v[24:27], a[240:243], v[68:71], v[24:27]\n"
"v_mfma_f32_16x16x32_bf16 v[20:23], a[248:251], v[64:67], v[20:23]\n"
"v_mfma_f32_16x16x32_bf16 v[28:31], a[248:251], v[68:71], v[28:31]\n"
"s_waitcnt lgkmcnt(0)\n"
"v_mfma_f32_16x16x32_bf16 v[0:3], a[116:119], v[72:75], v[0:3]\n"
"v_mfma_f32_16x16x32_bf16 v[8:11], a[116:119], v[76:79], v[8:11]\n"
"v_mfma_f32_16x16x32_bf16 v[4:7], a[124:127], v[72:75], v[4:7]\n"
"v_mfma_f32_16x16x32_bf16 v[12:15], a[124:127], v[76:79], v[12:15]\n"
"v_mfma_f32_16x16x32_bf16 v[16:19], a[244:247], v[72:75], v[16:19]\n"
"v_mfma_f32_16x16x32_bf16 v[24:27], a[244:247], v[76:79], v[24:27]\n"
"v_mfma_f32_16x16x32_bf16 v[20:23], a[252:255], v[72:75], v[20:23]\n"
"v_mfma_f32_16x16x32_bf16 v[28:31], a[252:255], v[76:79], v[28:31]\n"
"s_lshr_b32 s14, s8, 1\n"
"s_xnor_b32 s14, s14, s77\n"
"s_and_b32 s14, s14, 1\n"
"s_lshl_b32 s14, s14, 13\n"
"s_lshl_b32 s74, s8, 20\n"
"s_add_u32 s14, s14, s74\n"
"s_add_u32 s14, s14, s13\n"
"v_xor_b32 v81, 32768, v81\n"
"s_add_u32 s8, s8, 1\n"
"s_add_u32 s10, s8, 1\n"
"s_min_u32 s10, s10, 63\n"
"s_xor_b32 s9, s9, 32768\n"
"s_waitcnt vmcnt(0) lgkmcnt(0)\n"
"s_barrier\n"
"ds_read_b128 v[64:67], v81 offset:0\n"
"ds_read_b128 v[68:71], v81 offset:16384\n"
"s_waitcnt lgkmcnt(0)\n"
"v_mfma_f32_16x16x32_bf16 v[32:35], a[0:3], v[64:67], 0\n"
"ds_read_b128 v[72:75], v81 offset:1024\n"
"v_mfma_f32_16x16x32_bf16 v[40:43], a[0:3], v[68:71], 0\n"
"ds_read_b128 v[76:79], v81 offset:17408\n"
"v_mfma_f32_16x16x32_bf16 v[36:39], a[8:11], v[64:67], 0\n"
"s_lshl_b32 s71, s10, 15\n"
"s_lshl_b32 s65, s37, 13\n"
"s_add_u32 s71, s71, s65\n"
"s_add_u32 s71, s71, 4194304\n"
"s_add_u32 m0, s65, s9\n"
"v_mfma_f32_16x16x32_bf16 v[44:47], a[8:11], v[68:71], 0\n"
"v_mfma_f32_16x16x32_bf16 v[48:51], a[128:131], v[64:67], 0\n"
"v_mfma_f32_16x16x32_bf16 v[56:59], a[128:131], v[68:71], 0\n"
"buffer_load_dwordx4 v80, s[60:63], s71 offen offset:0 sc1 lds\n"
"v_mul_f32 v84, v0, v0\n"
"v_mfma_f32_16x16x32_bf16 v[52:55], a[136:139], v[64:67], 0\n"
"v_mul_f32 v85, v1, v1\n"
"v_mul_f32 v86, v2, v2\n"
"v_mfma_f32_16x16x32_bf16 v[60:63], a[136:139], v[68:71], 0\n"
"s_waitcnt lgkmcnt(0)\n"
"v_mul_f32 v87, v3, v3\n"
"v_mul_f32 v88, v4, v4\n"
"v_mfma_f32_16x16x32_bf16 v[32:35], a[4:7], v[72:75], v[32:35]\n"
"ds_read_b128 v[64:67], v81 offset:2048\n"
"v_mul_f32 v89, v5, v5\n"
"v_mfma_f32_16x16x32_bf16 v[40:43], a[4:7], v[76:79], v[40:43]\n"
"ds_read_b128 v[68:71], v81 offset:18432\n"
"buffer_load_dwordx4 v80, s[60:63], s71 offen offset:1024 sc1 lds\n"
"v_mfma_f32_16x16x32_bf16 v[36:39], a[12:15], v[72:75], v[36:39]\n"
"v_mul_f32 v90, v6, v6\n"
"v_mul_f32 v91, v7, v7\n"
"v_mfma_f32_16x16x32_bf16 v[44:47], a[12:15], v[76:79], v[44:47]\n"
"v_fma_f32 v84, v84, v82, s12\n"
"v_fma_f32 v85, v85, v82, s12\n"
"v_mfma_f32_16x16x32_bf16 v[48:51], a[132:135], v[72:75], v[48:51]\n"
"v_fma_f32 v86, v86, v82, s12\n"
"v_fma_f32 v87, v87, v82, s12\n"
"v_mfma_f32_16x16x32_bf16 v[56:59], a[132:135], v[76:79], v[56:59]\n"
"buffer_load_dwordx4 v80, s[60:63], s71 offen offset:2048 sc1 lds\n"
"v_fma_f32 v88, v88, v82, s12\n"
"v_mfma_f32_16x16x32_bf16 v[52:55], a[140:143], v[72:75], v[52:55]\n"
"v_fma_f32 v89, v89, v82, s12\n"
"v_fma_f32 v90, v90, v82, s12\n"
"v_mfma_f32_16x16x32_bf16 v[60:63], a[140:143], v[76:79], v[60:63]\n"
"s_waitcnt lgkmcnt(0)\n"
"v_fma_f32 v91, v91, v82, s12\n"
"v_mul_f32 v84, v84, v0\n"
"v_mfma_f32_16x16x32_bf16 v[32:35], a[16:19], v[64:67], v[32:35]\n"
"ds_read_b128 v[72:75], v81 offset:3072\n"
"v_mul_f32 v85, v85, v1\n"
"v_mfma_f32_16x16x32_bf16 v[40:43], a[16:19], v[68:71], v[40:43]\n"
"ds_read_b128 v[76:79], v81 offset:19456\n"
"buffer_load_dwordx4 v80, s[60:63], s71 offen offset:3072 sc1 lds\n"
"v_mfma_f32_16x16x32_bf16 v[36:39], a[24:27], v[64:67], v[36:39]\n"
"v_mul_f32 v86, v86, v2\n"
"v_mul_f32 v87, v87, v3\n"
"v_mfma_f32_16x16x32_bf16 v[44:47], a[24:27], v[68:71], v[44:47]\n"
"v_mul_f32 v88, v88, v4\n"
"v_mul_f32 v89, v89, v5\n"
"v_mfma_f32_16x16x32_bf16 v[48:51], a[144:147], v[64:67], v[48:51]\n"
"v_mul_f32 v90, v90, v6\n"
"v_mul_f32 v91, v91, v7\n"
"v_mfma_f32_16x16x32_bf16 v[56:59], a[144:147], v[68:71], v[56:59]\n"
"s_add_u32 m0, m0, 4096\n"
"s_add_u32 s71, s71, 4096\n"
"buffer_load_dwordx4 v80, s[60:63], s71 offen offset:0 sc1 lds\n"
"v_mfma_f32_16x16x32_bf16 v[52:55], a[152:155], v[64:67], v[52:55]\n"
"v_exp_f32 v84, v84\n"
"v_mfma_f32_16x16x32_bf16 v[60:63], a[152:155], v[68:71], v[60:63]\n"
"s_waitcnt lgkmcnt(0)\n"
"v_exp_f32 v85, v85\n"
"v_mfma_f32_16x16x32_bf16 v[32:35], a[20:23], v[72:75], v[32:35]\n"
"ds_read_b128 v[64:67], v81 offset:4096\n"
"v_mfma_f32_16x16x32_bf16 v[40:43], a[20:23], v[76:79], v[40:43]\n"
"ds_read_b128 v[68:71], v81 offset:20480\n"
"v_mfma_f32_16x16x32_bf16 v[36:39], a[28:31], v[72:75], v[36:39]\n"
"v_exp_f32 v86, v86\n"
"v_mfma_f32_16x16x32_bf16 v[44:47], a[28:31], v[76:79], v[44:47]\n"
"v_exp_f32 v87, v87\n"
"v_mfma_f32_16x16x32_bf16 v[48:51], a[148:151], v[72:75], v[48:51]\n"
"v_exp_f32 v88, v88\n"
"v_mfma_f32_16x16x32_bf16 v[56:59], a[148:151], v[76:79], v[56:59]\n"
"v_exp_f32 v89, v89\n"
"v_mfma_f32_16x16x32_bf16 v[52:55], a[156:159], v[72:75], v[52:55]\n"
"buffer_load_dwordx4 v80, s[60:63], s71 offen offset:1024 sc1 lds\n"
"v_mfma_f32_16x16x32_bf16 v[60:63], a[156:159], v[76:79], v[60:63]\n"
"s_waitcnt lgkmcnt(0)\n"
"v_exp_f32 v90, v90\n"
"v_mfma_f32_16x16x32_bf16 v[32:35], a[32:35], v[64:67], v[32:35]\n"
"ds_read_b128 v[72:75], v81 offset:5120\n"
"v_mfma_f32_16x16x32_bf16 v[40:43], a[32:35], v[68:71], v[40:43]\n"
"ds_read_b128 v[76:79], v81 offset:21504\n"
"v_mfma_f32_16x16x32_bf16 v[36:39], a[40:43], v[64:67], v[36:39]\n"
"v_exp_f32 v91, v91\n"
"v_mfma_f32_16x16x32_bf16 v[44:47], a[40:43], v[68:71], v[44:47]\n"
"v_mul_f32 v16, v0, v16\n"
"v_mul_f32 v17, v1, v17\n"
"v_mfma_f32_16x16x32_bf16 v[48:51], a[160:163], v[64:67], v[48:51]\n"
"v_mul_f32 v18, v2, v18\n"
"v_mul_f32 v19, v3, v19\n"
"v_mfma_f32_16x16x32_bf16 v[56:59], a[160:163], v[68:71], v[56:59]\n"
"buffer_load_dwordx4 v80, s[60:63], s71 offen offset:2048 sc1 lds\n"
"v_mul_f32 v20, v4, v20\n"
"v_mfma_f32_16x16x32_bf16 v[52:55], a[168:171], v[64:67], v[52:55]\n"
"v_mul_f32 v21, v5, v21\n"
"v_mul_f32 v22, v6, v22\n"
"v_mfma_f32_16x16x32_bf16 v[60:63], a[168:171], v[68:71], v[60:63]\n"
"s_waitcnt lgkmcnt(0)\n"
"v_mul_f32 v23, v7, v23\n"
"v_add_f32 v84, 1.0, v84\n"
"v_mfma_f32_16x16x32_bf16 v[32:35], a[36:39], v[72:75], v[32:35]\n"
"ds_read_b128 v[64:67], v81 offset:6144\n"
"v_add_f32 v85, 1.0, v85\n"
"v_mfma_f32_16x16x32_bf16 v[40:43], a[36:39], v[76:79], v[40:43]\n"
"ds_read_b128 v[68:71], v81 offset:22528\n"
"buffer_load_dwordx4 v80, s[60:63], s71 offen offset:3072 sc1 lds\n"
"v_mfma_f32_16x16x32_bf16 v[36:39], a[44:47], v[72:75], v[36:39]\n"
"v_add_f32 v86, 1.0, v86\n"
"v_add_f32 v87, 1.0, v87\n"
"v_mfma_f32_16x16x32_bf16 v[44:47], a[44:47], v[76:79], v[44:47]\n"
"v_add_f32 v88, 1.0, v88\n"
"v_add_f32 v89, 1.0, v89\n"
"v_mfma_f32_16x16x32_bf16 v[48:51], a[164:167], v[72:75], v[48:51]\n"
"v_add_f32 v90, 1.0, v90\n"
"v_add_f32 v91, 1.0, v91\n"
"v_mfma_f32_16x16x32_bf16 v[56:59], a[164:167], v[76:79], v[56:59]\n"
"v_rcp_f32 v84, v84\n"
"v_mfma_f32_16x16x32_bf16 v[52:55], a[172:175], v[72:75], v[52:55]\n"
"v_rcp_f32 v85, v85\n"
"v_mfma_f32_16x16x32_bf16 v[60:63], a[172:175], v[76:79], v[60:63]\n"
"s_waitcnt lgkmcnt(0)\n"
"v_rcp_f32 v86, v86\n"
"v_mfma_f32_16x16x32_bf16 v[32:35], a[48:51], v[64:67], v[32:35]\n"
"ds_read_b128 v[72:75], v81 offset:7168\n"
"v_mfma_f32_16x16x32_bf16 v[40:43], a[48:51], v[68:71], v[40:43]\n"
"ds_read_b128 v[76:79], v81 offset:23552\n"
"v_mfma_f32_16x16x32_bf16 v[36:39], a[56:59], v[64:67], v[36:39]\n"
"v_rcp_f32 v87, v87\n"
"v_mfma_f32_16x16x32_bf16 v[44:47], a[56:59], v[68:71], v[44:47]\n"
"v_rcp_f32 v88, v88\n"
"v_mfma_f32_16x16x32_bf16 v[48:51], a[176:179], v[64:67], v[48:51]\n"
"v_rcp_f32 v89, v89\n"
"v_mfma_f32_16x16x32_bf16 v[56:59], a[176:179], v[68:71], v[56:59]\n"
"v_rcp_f32 v90, v90\n"
"v_mfma_f32_16x16x32_bf16 v[52:55], a[184:187], v[64:67], v[52:55]\n"
"v_rcp_f32 v91, v91\n"
"v_mfma_f32_16x16x32_bf16 v[60:63], a[184:187], v[68:71], v[60:63]\n"
"s_waitcnt lgkmcnt(0)\n"
"v_mul_f32 v84, v84, v16\n"
"v_mul_f32 v85, v85, v17\n"
"v_mfma_f32_16x16x32_bf16 v[32:35], a[52:55], v[72:75], v[32:35]\n"
"ds_read_b128 v[64:67], v81 offset:8192\n"
"v_mul_f32 v86, v86, v18\n"
"v_mfma_f32_16x16x32_bf16 v[40:43], a[52:55], v[76:79], v[40:43]\n"
"ds_read_b128 v[68:71], v81 offset:24576\n"
"v_mul_f32 v87, v87, v19\n"
"v_mfma_f32_16x16x32_bf16 v[36:39], a[60:63], v[72:75], v[36:39]\n"
"v_mul_f32 v88, v88, v20\n"
"v_mul_f32 v89, v89, v21\n"
"v_mfma_f32_16x16x32_bf16 v[44:47], a[60:63], v[76:79], v[44:47]\n"
"v_mul_f32 v90, v90, v22\n"
"v_mul_f32 v91, v91, v23\n"
"v_mfma_f32_16x16x32_bf16 v[48:51], a[180:183], v[72:75], v[48:51]\n"
"v_cvt_pk_bf16_f32 v100, v84, v85\n"
"v_cvt_pk_bf16_f32 v101, v86, v87\n"
"v_mfma_f32_16x16x32_bf16 v[56:59], a[180:183], v[76:79], v[56:59]\n"
"v_cvt_pk_bf16_f32 v102, v88, v89\n"
"v_cvt_pk_bf16_f32 v103, v90, v91\n"
"v_mfma_f32_16x16x32_bf16 v[52:55], a[188:191], v[72:75], v[52:55]\n"
"buffer_store_dwordx4 v[100:103], v80, s[60:63], s14 offen sc1\n"
"s_add_u32 s14, s14, 524288\n"
"v_mul_f32 v92, v8, v8\n"
"v_mfma_f32_16x16x32_bf16 v[60:63], a[188:191], v[76:79], v[60:63]\n"
"s_waitcnt lgkmcnt(0)\n"
"v_mul_f32 v93, v9, v9\n"
"v_mul_f32 v94, v10, v10\n"
"v_mfma_f32_16x16x32_bf16 v[32:35], a[64:67], v[64:67], v[32:35]\n"
"ds_read_b128 v[72:75], v81 offset:9216\n"
"v_mul_f32 v95, v11, v11\n"
"v_mfma_f32_16x16x32_bf16 v[40:43], a[64:67], v[68:71], v[40:43]\n"
"ds_read_b128 v[76:79], v81 offset:25600\n"
"v_mul_f32 v96, v12, v12\n"
"v_mfma_f32_16x16x32_bf16 v[36:39], a[72:75], v[64:67], v[36:39]\n"
"v_mul_f32 v97, v13, v13\n"
"v_mul_f32 v98, v14, v14\n"
"v_mfma_f32_16x16x32_bf16 v[44:47], a[72:75], v[68:71], v[44:47]\n"
"v_mul_f32 v99, v15, v15\n"
"v_fma_f32 v92, v92, v82, s12\n"
"v_mfma_f32_16x16x32_bf16 v[48:51], a[192:195], v[64:67], v[48:51]\n"
"v_fma_f32 v93, v93, v82, s12\n"
"v_fma_f32 v94, v94, v82, s12\n"
"v_mfma_f32_16x16x32_bf16 v[56:59], a[192:195], v[68:71], v[56:59]\n"
"v_fma_f32 v95, v95, v82, s12\n"
"v_fma_f32 v96, v96, v82, s12\n"
"v_mfma_f32_16x16x32_bf16 v[52:55], a[200:203], v[64:67], v[52:55]\n"
"v_fma_f32 v97, v97, v82, s12\n"
"v_fma_f32 v98, v98, v82, s12\n"
"v_mfma_f32_16x16x32_bf16 v[60:63], a[200:203], v[68:71], v[60:63]\n"
"s_waitcnt lgkmcnt(0)\n"
"v_fma_f32 v99, v99, v82, s12\n"
"v_mul_f32 v92, v92, v8\n"
"v_mfma_f32_16x16x32_bf16 v[32:35], a[68:71], v[72:75], v[32:35]\n"
"ds_read_b128 v[64:67], v81 offset:10240\n"
"v_mul_f32 v93, v93, v9\n"
"v_mfma_f32_16x16x32_bf16 v[40:43], a[68:71], v[76:79], v[40:43]\n"
"ds_read_b128 v[68:71], v81 offset:26624\n"
"v_mul_f32 v94, v94, v10\n"
"v_mfma_f32_16x16x32_bf16 v[36:39], a[76:79], v[72:75], v[36:39]\n"
"v_mul_f32 v95, v95, v11\n"
"v_mul_f32 v96, v96, v12\n"
"v_mfma_f32_16x16x32_bf16 v[44:47], a[76:79], v[76:79], v[44:47]\n"
"v_mul_f32 v97, v97, v13\n"
"v_mul_f32 v98, v98, v14\n"
"v_mfma_f32_16x16x32_bf16 v[48:51], a[196:199], v[72:75], v[48:51]\n"
"v_mul_f32 v99, v99, v15\n"
"v_mfma_f32_16x16x32_bf16 v[56:59], a[196:199], v[76:79], v[56:59]\n"
"v_exp_f32 v92, v92\n"
"v_mfma_f32_16x16x32_bf16 v[52:55], a[204:207], v[72:75], v[52:55]\n"
"v_exp_f32 v93, v93\n"
"v_mfma_f32_16x16x32_bf16 v[60:63], a[204:207], v[76:79], v[60:63]\n"
"s_waitcnt lgkmcnt(0)\n"
"v_exp_f32 v94, v94\n"
"v_mfma_f32_16x16x32_bf16 v[32:35], a[80:83], v[64:67], v[32:35]\n"
"ds_read_b128 v[72:75], v81 offset:11264\n"
"v_mfma_f32_16x16x32_bf16 v[40:43], a[80:83], v[68:71], v[40:43]\n"
"ds_read_b128 v[76:79], v81 offset:27648\n"
"v_mfma_f32_16x16x32_bf16 v[36:39], a[88:91], v[64:67], v[36:39]\n"
"v_exp_f32 v95, v95\n"
"v_mfma_f32_16x16x32_bf16 v[44:47], a[88:91], v[68:71], v[44:47]\n"
"v_exp_f32 v96, v96\n"
"v_mfma_f32_16x16x32_bf16 v[48:51], a[208:211], v[64:67], v[48:51]\n"
"v_exp_f32 v97, v97\n"
"v_mfma_f32_16x16x32_bf16 v[56:59], a[208:211], v[68:71], v[56:59]\n"
"v_exp_f32 v98, v98\n"
"v_mfma_f32_16x16x32_bf16 v[52:55], a[216:219], v[64:67], v[52:55]\n"
"v_exp_f32 v99, v99\n"
"v_mfma_f32_16x16x32_bf16 v[60:63], a[216:219], v[68:71], v[60:63]\n"
"s_waitcnt lgkmcnt(0)\n"
"v_mul_f32 v24, v8, v24\n"
"v_mul_f32 v25, v9, v25\n"
"v_mfma_f32_16x16x32_bf16 v[32:35], a[84:87], v[72:75], v[32:35]\n"
"ds_read_b128 v[64:67], v81 offset:12288\n"
"v_mul_f32 v26, v10, v26\n"
"v_mfma_f32_16x16x32_bf16 v[40:43], a[84:87], v[76:79], v[40:43]\n"
"ds_read_b128 v[68:71], v81 offset:28672\n"
"v_mul_f32 v27, v11, v27\n"
"v_mfma_f32_16x16x32_bf16 v[36:39], a[92:95], v[72:75], v[36:39]\n"
"v_mul_f32 v28, v12, v28\n"
"v_mul_f32 v29, v13, v29\n"
"v_mfma_f32_16x16x32_bf16 v[44:47], a[92:95], v[76:79], v[44:47]\n"
"v_mul_f32 v30, v14, v30\n"
"v_mul_f32 v31, v15, v31\n"
"v_mfma_f32_16x16x32_bf16 v[48:51], a[212:215], v[72:75], v[48:51]\n"
"v_add_f32 v92, 1.0, v92\n"
"v_add_f32 v93, 1.0, v93\n"
"v_mfma_f32_16x16x32_bf16 v[56:59], a[212:215], v[76:79], v[56:59]\n"
"v_add_f32 v94, 1.0, v94\n"
"v_add_f32 v95, 1.0, v95\n"
"v_mfma_f32_16x16x32_bf16 v[52:55], a[220:223], v[72:75], v[52:55]\n"
"v_add_f32 v96, 1.0, v96\n"
"v_add_f32 v97, 1.0, v97\n"
"v_mfma_f32_16x16x32_bf16 v[60:63], a[220:223], v[76:79], v[60:63]\n"
"s_waitcnt lgkmcnt(0)\n"
"v_add_f32 v98, 1.0, v98\n"
"v_add_f32 v99, 1.0, v99\n"
"v_mfma_f32_16x16x32_bf16 v[32:35], a[96:99], v[64:67], v[32:35]\n"
"ds_read_b128 v[72:75], v81 offset:13312\n"
"v_mfma_f32_16x16x32_bf16 v[40:43], a[96:99], v[68:71], v[40:43]\n"
"ds_read_b128 v[76:79], v81 offset:29696\n"
"v_mfma_f32_16x16x32_bf16 v[36:39], a[104:107], v[64:67], v[36:39]\n"
"v_rcp_f32 v92, v92\n"
"v_mfma_f32_16x16x32_bf16 v[44:47], a[104:107], v[68:71], v[44:47]\n"
"v_rcp_f32 v93, v93\n"
"v_mfma_f32_16x16x32_bf16 v[48:51], a[224:227], v[64:67], v[48:51]\n"
"v_rcp_f32 v94, v94\n"
"v_mfma_f32_16x16x32_bf16 v[56:59], a[224:227], v[68:71], v[56:59]\n"
"v_rcp_f32 v95, v95\n"
"v_mfma_f32_16x16x32_bf16 v[52:55], a[232:235], v[64:67], v[52:55]\n"
"v_rcp_f32 v96, v96\n"
"v_mfma_f32_16x16x32_bf16 v[60:63], a[232:235], v[68:71], v[60:63]\n"
"s_waitcnt lgkmcnt(0)\n"
"v_rcp_f32 v97, v97\n"
"v_mfma_f32_16x16x32_bf16 v[32:35], a[100:103], v[72:75], v[32:35]\n"
"ds_read_b128 v[64:67], v81 offset:14336\n"
"v_mfma_f32_16x16x32_bf16 v[40:43], a[100:103], v[76:79], v[40:43]\n"
"ds_read_b128 v[68:71], v81 offset:30720\n"
"v_mfma_f32_16x16x32_bf16 v[36:39], a[108:111], v[72:75], v[36:39]\n"
"v_rcp_f32 v98, v98\n"
"v_mfma_f32_16x16x32_bf16 v[44:47], a[108:111], v[76:79], v[44:47]\n"
"v_rcp_f32 v99, v99\n"
"v_mfma_f32_16x16x32_bf16 v[48:51], a[228:231], v[72:75], v[48:51]\n"
"v_mul_f32 v92, v92, v24\n"
"v_mul_f32 v93, v93, v25\n"
"v_mfma_f32_16x16x32_bf16 v[56:59], a[228:231], v[76:79], v[56:59]\n"
"v_mul_f32 v94, v94, v26\n"
"v_mul_f32 v95, v95, v27\n"
"v_mfma_f32_16x16x32_bf16 v[52:55], a[236:239], v[72:75], v[52:55]\n"
"v_mul_f32 v96, v96, v28\n"
"v_mul_f32 v97, v97, v29\n"
"v_mfma_f32_16x16x32_bf16 v[60:63], a[236:239], v[76:79], v[60:63]\n"
"s_waitcnt lgkmcnt(0)\n"
"v_mul_f32 v98, v98, v30\n"
"v_mul_f32 v99, v99, v31\n"
"v_mfma_f32_16x16x32_bf16 v[32:35], a[112:115], v[64:67], v[32:35]\n"
"ds_read_b128 v[72:75], v81 offset:15360\n"
"v_cvt_pk_bf16_f32 v104, v92, v93\n"
"v_mfma_f32_16x16x32_bf16 v[40:43], a[112:115], v[68:71], v[40:43]\n"
"ds_read_b128 v[76:79], v81 offset:31744\n"
"v_cvt_pk_bf16_f32 v105, v94, v95\n"
"v_mfma_f32_16x16x32_bf16 v[36:39], a[120:123], v[64:67], v[36:39]\n"
"v_cvt_pk_bf16_f32 v106, v96, v97\n"
"v_cvt_pk_bf16_f32 v107, v98, v99\n"
"v_mfma_f32_16x16x32_bf16 v[44:47], a[120:123], v[68:71], v[44:47]\n"
"buffer_store_dwordx4 v[104:107], v80, s[60:63], s14 offen sc1\n"
"s_add_u32 s14, s14, 524288\n"
"v_mfma_f32_16x16x32_bf16 v[48:51], a[240:243], v[64:67], v[48:51]\n"
"v_mfma_f32_16x16x32_bf16 v[56:59], a[240:243], v[68:71], v[56:59]\n"
"v_mfma_f32_16x16x32_bf16 v[52:55], a[248:251], v[64:67], v[52:55]\n"
"v_mfma_f32_16x16x32_bf16 v[60:63], a[248:251], v[68:71], v[60:63]\n"
"s_waitcnt lgkmcnt(0)\n"
"v_mfma_f32_16x16x32_bf16 v[32:35], a[116:119], v[72:75], v[32:35]\n"
"v_mfma_f32_16x16x32_bf16 v[40:43], a[116:119], v[76:79], v[40:43]\n"
"v_mfma_f32_16x16x32_bf16 v[36:39], a[124:127], v[72:75], v[36:39]\n"
"v_mfma_f32_16x16x32_bf16 v[44:47], a[124:127], v[76:79], v[44:47]\n"
"v_mfma_f32_16x16x32_bf16 v[48:51], a[244:247], v[72:75], v[48:51]\n"
"v_mfma_f32_16x16x32_bf16 v[56:59], a[244:247], v[76:79], v[56:59]\n"
"v_mfma_f32_16x16x32_bf16 v[52:55], a[252:255], v[72:75], v[52:55]\n"
"v_mfma_f32_16x16x32_bf16 v[60:63], a[252:255], v[76:79], v[60:63]\n"
"s_lshr_b32 s14, s8, 1\n"
"s_xnor_b32 s14, s14, s77\n"
"s_and_b32 s14, s14, 1\n"
"s_lshl_b32 s14, s14, 13\n"
"s_lshl_b32 s74, s8, 20\n"
"s_add_u32 s14, s14, s74\n"
"s_add_u32 s14, s14, s13\n"
"v_xor_b32 v81, 32768, v81\n"
"s_add_u32 s8, s8, 1\n"
".Lutop105_%=:\n"
"s_add_u32 s10, s8, 1\n"
"s_min_u32 s10, s10, 63\n"
"s_xor_b32 s9, s9, 32768\n"
"s_waitcnt vmcnt(2) lgkmcnt(0)\n"
"s_barrier\n"
"ds_read_b128 v[64:67], v81 offset:0\n"
"ds_read_b128 v[68:71], v81 offset:16384\n"
"s_waitcnt lgkmcnt(0)\n"
"v_mfma_f32_16x16x32_bf16 v[0:3], a[0:3], v[64:67], 0\n"
"ds_read_b128 v[72:75], v81 offset:1024\n"
"v_mfma_f32_16x16x32_bf16 v[8:11], a[0:3], v[68:71], 0\n"
"ds_read_b128 v[76:79], v81 offset:17408\n"
"v_mfma_f32_16x16x32_bf16 v[4:7], a[8:11], v[64:67], 0\n"
"s_lshl_b32 s71, s10, 15\n"
"s_lshl_b32 s65, s37, 13\n"
"s_add_u32 s71, s71, s65\n"
"s_add_u32 s71, s71, 4194304\n"
"s_add_u32 m0, s65, s9\n"
"v_mfma_f32_16x16x32_bf16 v[12:15], a[8:11], v[68:71], 0\n"
"v_mfma_f32_16x16x32_bf16 v[16:19], a[128:131], v[64:67], 0\n"
"v_mfma_f32_16x16x32_bf16 v[24:27], a[128:131], v[68:71], 0\n"
"buffer_load_dwordx4 v80, s[60:63], s71 offen offset:0 sc1 lds\n"
"v_mul_f32 v84, v32, v32\n"
"v_mfma_f32_16x16x32_bf16 v[20:23], a[136:139], v[64:67], 0\n"
"v_mul_f32 v85, v33, v33\n"
"v_mul_f32 v86, v34, v34\n"
"v_mfma_f32_16x16x32_bf16 v[28:31], a[136:139], v[68:71], 0\n"
"s_waitcnt lgkmcnt(0)\n"
"v_mul_f32 v87, v35, v35\n"
"v_mul_f32 v88, v36, v36\n"
"v_mfma_f32_16x16x32_bf16 v[0:3], a[4:7], v[72:75], v[0:3]\n"
"ds_read_b128 v[64:67], v81 offset:2048\n"
"v_mul_f32 v89, v37, v37\n"
"v_mfma_f32_16x16x32_bf16 v[8:11], a[4:7], v[76:79], v[8:11]\n"
"ds_read_b128 v[68:71], v81 offset:18432\n"
"buffer_load_dwordx4 v80, s[60:63], s71 offen offset:1024 sc1 lds\n"
"v_mfma_f32_16x16x32_bf16 v[4:7], a[12:15], v[72:75], v[4:7]\n"
"v_mul_f32 v90, v38, v38\n"
"v_mul_f32 v91, v39, v39\n"
"v_mfma_f32_16x16x32_bf16 v[12:15], a[12:15], v[76:79], v[12:15]\n"
"v_fma_f32 v84, v84, v82, s12\n"
"v_fma_f32 v85, v85, v82, s12\n"
"v_mfma_f32_16x16x32_bf16 v[16:19], a[132:135], v[72:75], v[16:19]\n"
"v_fma_f32 v86, v86, v82, s12\n"
"v_fma_f32 v87, v87, v82, s12\n"
"v_mfma_f32_16x16x32_bf16 v[24:27], a[132:135], v[76:79], v[24:27]\n"
"buffer_load_dwordx4 v80, s[60:63], s71 offen offset:2048 sc1 lds\n"
"v_fma_f32 v88, v88, v82, s12\n"
"v_mfma_f32_16x16x32_bf16 v[20:23], a[140:143], v[72:75], v[20:23]\n"
"v_fma_f32 v89, v89, v82, s12\n"
"v_fma_f32 v90, v90, v82, s12\n"
"v_mfma_f32_16x16x32_bf16 v[28:31], a[140:143], v[76:79], v[28:31]\n"
"s_waitcnt lgkmcnt(0)\n"
"v_fma_f32 v91, v91, v82, s12\n"
"v_mul_f32 v84, v84, v32\n"
"v_mfma_f32_16x16x32_bf16 v[0:3], a[16:19], v[64:67], v[0:3]\n"
"ds_read_b128 v[72:75], v81 offset:3072\n"
"v_mul_f32 v85, v85, v33\n"
"v_mfma_f32_16x16x32_bf16 v[8:11], a[16:19], v[68:71], v[8:11]\n"
"ds_read_b128 v[76:79], v81 offset:19456\n"
"buffer_load_dwordx4 v80, s[60:63], s71 offen offset:3072 sc1 lds\n"
"v_mfma_f32_16x16x32_bf16 v[4:7], a[24:27], v[64:67], v[4:7]\n"
"v_mul_f32 v86, v86, v34\n"
"v_mul_f32 v87, v87, v35\n"
"v_mfma_f32_16x16x32_bf16 v[12:15], a[24:27], v[68:71], v[12:15]\n"
"v_mul_f32 v88, v88, v36\n"
"v_mul_f32 v89, v89, v37\n"
"v_mfma_f32_16x16x32_bf16 v[16:19], a[144:147], v[64:67], v[16:19]\n"
"v_mul_f32 v90, v90, v38\n"
"v_mul_f32 v91, v91, v39\n"
"v_mfma_f32_16x16x32_bf16 v[24:27], a[144:147], v[68:71], v[24:27]\n"
"s_add_u32 m0, m0, 4096\n"
"s_add_u32 s71, s71, 4096\n"
"buffer_load_dwordx4 v80, s[60:63], s71 offen offset:0 sc1 lds\n"
"v_mfma_f32_16x16x32_bf16 v[20:23], a[152:155], v[64:67], v[20:23]\n"
"v_exp_f32 v84, v84\n"
"v_mfma_f32_16x16x32_bf16 v[28:31], a[152:155], v[68:71], v[28:31]\n"
"s_waitcnt lgkmcnt(0)\n"
"v_exp_f32 v85, v85\n"
"v_mfma_f32_16x16x32_bf16 v[0:3], a[20:23], v[72:75], v[0:3]\n"
"ds_read_b128 v[64:67], v81 offset:4096\n"
"v_mfma_f32_16x16x32_bf16 v[8:11], a[20:23], v[76:79], v[8:11]\n"
"ds_read_b128 v[68:71], v81 offset:20480\n"
"v_mfma_f32_16x16x32_bf16 v[4:7], a[28:31], v[72:75], v[4:7]\n"
"v_exp_f32 v86, v86\n"
"v_mfma_f32_16x16x32_bf16 v[12:15], a[28:31], v[76:79], v[12:15]\n"
"v_exp_f32 v87, v87\n"
"v_mfma_f32_16x16x32_bf16 v[16:19], a[148:151], v[72:75], v[16:19]\n"
"v_exp_f32 v88, v88\n"
"v_mfma_f32_16x16x32_bf16 v[24:27], a[148:151], v[76:79], v[24:27]\n"
"v_exp_f32 v89, v89\n"
"v_mfma_f32_16x16x32_bf16 v[20:23], a[156:159], v[72:75], v[20:23]\n"
"buffer_load_dwordx4 v80, s[60:63], s71 offen offset:1024 sc1 lds\n"
"v_mfma_f32_16x16x32_bf16 v[28:31], a[156:159], v[76:79], v[28:31]\n"
"s_waitcnt lgkmcnt(0)\n"
"v_exp_f32 v90, v90\n"
"v_mfma_f32_16x16x32_bf16 v[0:3], a[32:35], v[64:67], v[0:3]\n"
"ds_read_b128 v[72:75], v81 offset:5120\n"
"v_mfma_f32_16x16x32_bf16 v[8:11], a[32:35], v[68:71], v[8:11]\n"
"ds_read_b128 v[76:79], v81 offset:21504\n"
"v_mfma_f32_16x16x32_bf16 v[4:7], a[40:43], v[64:67], v[4:7]\n"
"v_exp_f32 v91, v91\n"
"v_mfma_f32_16x16x32_bf16 v[12:15], a[40:43], v[68:71], v[12:15]\n"
"v_mul_f32 v48, v32, v48\n"
"v_mul_f32 v49, v33, v49\n"
"v_mfma_f32_16x16x32_bf16 v[16:19], a[160:163], v[64:67], v[16:19]\n"
"v_mul_f32 v50, v34, v50\n"
"v_mul_f32 v51, v35, v51\n"
"v_mfma_f32_16x16x32_bf16 v[24:27], a[160:163], v[68:71], v[24:27]\n"
"buffer_load_dwordx4 v80, s[60:63], s71 offen offset:2048 sc1 lds\n"
"v_mul_f32 v52, v36, v52\n"
"v_mfma_f32_16x16x32_bf16 v[20:23], a[168:171], v[64:67], v[20:23]\n"
"v_mul_f32 v53, v37, v53\n"
"v_mul_f32 v54, v38, v54\n"
"v_mfma_f32_16x16x32_bf16 v[28:31], a[168:171], v[68:71], v[28:31]\n"
"s_waitcnt lgkmcnt(0)\n"
"v_mul_f32 v55, v39, v55\n"
"v_add_f32 v84, 1.0, v84\n"
"v_mfma_f32_16x16x32_bf16 v[0:3], a[36:39], v[72:75], v[0:3]\n"
"ds_read_b128 v[64:67], v81 offset:6144\n"
"v_add_f32 v85, 1.0, v85\n"
"v_mfma_f32_16x16x32_bf16 v[8:11], a[36:39], v[76:79], v[8:11]\n"
"ds_read_b128 v[68:71], v81 offset:22528\n"
"buffer_load_dwordx4 v80, s[60:63], s71 offen offset:3072 sc1 lds\n"
"v_mfma_f32_16x16x32_bf16 v[4:7], a[44:47], v[72:75], v[4:7]\n"
"v_add_f32 v86, 1.0, v86\n"
"v_add_f32 v87, 1.0, v87\n"
"v_mfma_f32_16x16x32_bf16 v[12:15], a[44:47], v[76:79], v[12:15]\n"
"v_add_f32 v88, 1.0, v88\n"
"v_add_f32 v89, 1.0, v89\n"
"v_mfma_f32_16x16x32_bf16 v[16:19], a[164:167], v[72:75], v[16:19]\n"
"v_add_f32 v90, 1.0, v90\n"
"v_add_f32 v91, 1.0, v91\n"
"v_mfma_f32_16x16x32_bf16 v[24:27], a[164:167], v[76:79], v[24:27]\n"
"v_rcp_f32 v84, v84\n"
"v_mfma_f32_16x16x32_bf16 v[20:23], a[172:175], v[72:75], v[20:23]\n"
"v_rcp_f32 v85, v85\n"
"v_mfma_f32_16x16x32_bf16 v[28:31], a[172:175], v[76:79], v[28:31]\n"
"s_waitcnt lgkmcnt(0)\n"
"v_rcp_f32 v86, v86\n"
"s_waitcnt vmcnt(8)\n"
"v_mov_b32 v108, s34\n"
"v_mov_b32 v109, s35\n"
"s_mov_b64 exec, 1\n"
"buffer_store_dwordx2 v[108:109], off, s[60:63], s11 sc1\n"
"s_mov_b64 exec, -1\n"
"s_add_u32 s11, s11, 16384\n"
"v_mfma_f32_16x16x32_bf16 v[0:3], a[48:51], v[64:67], v[0:3]\n"
"ds_read_b128 v[72:75], v81 offset:7168\n"
"v_mfma_f32_16x16x32_bf16 v[8:11], a[48:51], v[68:71], v[8:11]\n"
"ds_read_b128 v[76:79], v81 offset:23552\n"
"v_mfma_f32_16x16x32_bf16 v[4:7], a[56:59], v[64:67], v[4:7]\n"
"v_rcp_f32 v87, v87\n"
"v_mfma_f32_16x16x32_bf16 v[12:15], a[56:59], v[68:71], v[12:15]\n"
"v_rcp_f32 v88, v88\n"
"v_mfma_f32_16x16x32_bf16 v[16:19], a[176:179], v[64:67], v[16:19]\n"
"v_rcp_f32 v89, v89\n"
"v_mfma_f32_16x16x32_bf16 v[24:27], a[176:179], v[68:71], v[24:27]\n"
"v_rcp_f32 v90, v90\n"
"v_mfma_f32_16x16x32_bf16 v[20:23], a[184:187], v[64:67], v[20:23]\n"
"v_rcp_f32 v91, v91\n"
"v_mfma_f32_16x16x32_bf16 v[28:31], a[184:187], v[68:71], v[28:31]\n"
"s_waitcnt lgkmcnt(0)\n"
"v_mul_f32 v84, v84, v48\n"
"v_mul_f32 v85, v85, v49\n"
"v_mfma_f32_16x16x32_bf16 v[0:3], a[52:55], v[72:75], v[0:3]\n"
"ds_read_b128 v[64:67], v81 offset:8192\n"
"v_mul_f32 v86, v86, v50\n"
"v_mfma_f32_16x16x32_bf16 v[8:11], a[52:55], v[76:79], v[8:11]\n"
"ds_read_b128 v[68:71], v81 offset:24576\n"
"v_mul_f32 v87, v87, v51\n"
"v_mfma_f32_16x16x32_bf16 v[4:7], a[60:63], v[72:75], v[4:7]\n"
"v_mul_f32 v88, v88, v52\n"
"v_mul_f32 v89, v89, v53\n"
"v_mfma_f32_16x16x32_bf16 v[12:15], a[60:63], v[76:79], v[12:15]\n"
"v_mul_f32 v90, v90, v54\n"
"v_mul_f32 v91, v91, v55\n"
"v_mfma_f32_16x16x32_bf16 v[16:19], a[180:183], v[72:75], v[16:19]\n"
"v_cvt_pk_bf16_f32 v100, v84, v85\n"
"v_cvt_pk_bf16_f32 v101, v86, v87\n"
"v_mfma_f32_16x16x32_bf16 v[24:27], a[180:183], v[76:79], v[24:27]\n"
"v_cvt_pk_bf16_f32 v102, v88, v89\n"
"v_cvt_pk_bf16_f32 v103, v90, v91\n"
"v_mfma_f32_16x16x32_bf16 v[20:23], a[188:191], v[72:75], v[20:23]\n"
"buffer_store_dwordx4 v[100:103], v80, s[60:63], s14 offen sc1\n"
"s_add_u32 s14, s14, 524288\n"
"v_mul_f32 v92, v40, v40\n"
"v_mfma_f32_16x16x32_bf16 v[28:31], a[188:191], v[76:79], v[28:31]\n"
"s_waitcnt lgkmcnt(0)\n"
"v_mul_f32 v93, v41, v41\n"
"v_mul_f32 v94, v42, v42\n"
"v_mfma_f32_16x16x32_bf16 v[0:3], a[64:67], v[64:67], v[0:3]\n"
"ds_read_b128 v[72:75], v81 offset:9216\n"
"v_mul_f32 v95, v43, v43\n"
"v_mfma_f32_16x16x32_bf16 v[8:11], a[64:67], v[68:71], v[8:11]\n"
"ds_read_b128 v[76:79], v81 offset:25600\n"
"v_mul_f32 v96, v44, v44\n"
"v_mfma_f32_16x16x32_bf16 v[4:7], a[72:75], v[64:67], v[4:7]\n"
"v_mul_f32 v97, v45, v45\n"
"v_mul_f32 v98, v46, v46\n"
"v_mfma_f32_16x16x32_bf16 v[12:15], a[72:75], v[68:71], v[12:15]\n"
"v_mul_f32 v99, v47, v47\n"
"v_fma_f32 v92, v92, v82, s12\n"
"v_mfma_f32_16x16x32_bf16 v[16:19], a[192:195], v[64:67], v[16:19]\n"
"v_fma_f32 v93, v93, v82, s12\n"
"v_fma_f32 v94, v94, v82, s12\n"
"v_mfma_f32_16x16x32_bf16 v[24:27], a[192:195], v[68:71], v[24:27]\n"
"v_fma_f32 v95, v95, v82, s12\n"
"v_fma_f32 v96, v96, v82, s12\n"
"v_mfma_f32_16x16x32_bf16 v[20:23], a[200:203], v[64:67], v[20:23]\n"
"v_fma_f32 v97, v97, v82, s12\n"
"v_fma_f32 v98, v98, v82, s12\n"
"v_mfma_f32_16x16x32_bf16 v[28:31], a[200:203], v[68:71], v[28:31]\n"
"s_waitcnt lgkmcnt(0)\n"
"v_fma_f32 v99, v99, v82, s12\n"
"v_mul_f32 v92, v92, v40\n"
"v_mfma_f32_16x16x32_bf16 v[0:3], a[68:71], v[72:75], v[0:3]\n"
"ds_read_b128 v[64:67], v81 offset:10240\n"
"v_mul_f32 v93, v93, v41\n"
"v_mfma_f32_16x16x32_bf16 v[8:11], a[68:71], v[76:79], v[8:11]\n"
"ds_read_b128 v[68:71], v81 offset:26624\n"
"v_mul_f32 v94, v94, v42\n"
"v_mfma_f32_16x16x32_bf16 v[4:7], a[76:79], v[72:75], v[4:7]\n"
"v_mul_f32 v95, v95, v43\n"
"v_mul_f32 v96, v96, v44\n"
"v_mfma_f32_16x16x32_bf16 v[12:15], a[76:79], v[76:79], v[12:15]\n"
"v_mul_f32 v97, v97, v45\n"
"v_mul_f32 v98, v98, v46\n"
"v_mfma_f32_16x16x32_bf16 v[16:19], a[196:199], v[72:75], v[16:19]\n"
"v_mul_f32 v99, v99, v47\n"
"v_mfma_f32_16x16x32_bf16 v[24:27], a[196:199], v[76:79], v[24:27]\n"
"v_exp_f32 v92, v92\n"
"v_mfma_f32_16x16x32_bf16 v[20:23], a[204:207], v[72:75], v[20:23]\n"
"v_exp_f32 v93, v93\n"
"v_mfma_f32_16x16x32_bf16 v[28:31], a[204:207], v[76:79], v[28:31]\n"
"s_waitcnt lgkmcnt(0)\n"
"v_exp_f32 v94, v94\n"
"v_mfma_f32_16x16x32_bf16 v[0:3], a[80:83], v[64:67], v[0:3]\n"
"ds_read_b128 v[72:75], v81 offset:11264\n"
"v_mfma_f32_16x16x32_bf16 v[8:11], a[80:83], v[68:71], v[8:11]\n"
"ds_read_b128 v[76:79], v81 offset:27648\n"
"v_mfma_f32_16x16x32_bf16 v[4:7], a[88:91], v[64:67], v[4:7]\n"
"v_exp_f32 v95, v95\n"
"v_mfma_f32_16x16x32_bf16 v[12:15], a[88:91], v[68:71], v[12:15]\n"
"v_exp_f32 v96, v96\n"
"v_mfma_f32_16x16x32_bf16 v[16:19], a[208:211], v[64:67], v[16:19]\n"
"v_exp_f32 v97, v97\n"
"v_mfma_f32_16x16x32_bf16 v[24:27], a[208:211], v[68:71], v[24:27]\n"
"v_exp_f32 v98, v98\n"
"v_mfma_f32_16x16x32_bf16 v[20:23], a[216:219], v[64:67], v[20:23]\n"
"v_exp_f32 v99, v99\n"
"v_mfma_f32_16x16x32_bf16 v[28:31], a[216:219], v[68:71], v[28:31]\n"
"s_waitcnt lgkmcnt(0)\n"
"v_mul_f32 v56, v40, v56\n"
"v_mul_f32 v57, v41, v57\n"
"v_mfma_f32_16x16x32_bf16 v[0:3], a[84:87], v[72:75], v[0:3]\n"
"ds_read_b128 v[64:67], v81 offset:12288\n"
"v_mul_f32 v58, v42, v58\n"
"v_mfma_f32_16x16x32_bf16 v[8:11], a[84:87], v[76:79], v[8:11]\n"
"ds_read_b128 v[68:71], v81 offset:28672\n"
"v_mul_f32 v59, v43, v59\n"
"v_mfma_f32_16x16x32_bf16 v[4:7], a[92:95], v[72:75], v[4:7]\n"
"v_mul_f32 v60, v44, v60\n"
"v_mul_f32 v61, v45, v61\n"
"v_mfma_f32_16x16x32_bf16 v[12:15], a[92:95], v[76:79], v[12:15]\n"
"v_mul_f32 v62, v46, v62\n"
"v_mul_f32 v63, v47, v63\n"
"v_mfma_f32_16x16x32_bf16 v[16:19], a[212:215], v[72:75], v[16:19]\n"
"v_add_f32 v92, 1.0, v92\n"
"v_add_f32 v93, 1.0, v93\n"
"v_mfma_f32_16x16x32_bf16 v[24:27], a[212:215], v[76:79], v[24:27]\n"
"v_add_f32 v94, 1.0, v94\n"
"v_add_f32 v95, 1.0, v95\n"
"v_mfma_f32_16x16x32_bf16 v[20:23], a[220:223], v[72:75], v[20:23]\n"
"v_add_f32 v96, 1.0, v96\n"
"v_add_f32 v97, 1.0, v97\n"
"v_mfma_f32_16x16x32_bf16 v[28:31], a[220:223], v[76:79], v[28:31]\n"
"s_waitcnt lgkmcnt(0)\n"
"v_add_f32 v98, 1.0, v98\n"
"v_add_f32 v99, 1.0, v99\n"
"v_mfma_f32_16x16x32_bf16 v[0:3], a[96:99], v[64:67], v[0:3]\n"
"ds_read_b128 v[72:75], v81 offset:13312\n"
"v_mfma_f32_16x16x32_bf16 v[8:11], a[96:99], v[68:71], v[8:11]\n"
"ds_read_b128 v[76:79], v81 offset:29696\n"
"v_mfma_f32_16x16x32_bf16 v[4:7], a[104:107], v[64:67], v[4:7]\n"
"v_rcp_f32 v92, v92\n"
"v_mfma_f32_16x16x32_bf16 v[12:15], a[104:107], v[68:71], v[12:15]\n"
"v_rcp_f32 v93, v93\n"
"v_mfma_f32_16x16x32_bf16 v[16:19], a[224:227], v[64:67], v[16:19]\n"
"v_rcp_f32 v94, v94\n"
"v_mfma_f32_16x16x32_bf16 v[24:27], a[224:227], v[68:71], v[24:27]\n"
"v_rcp_f32 v95, v95\n"
"v_mfma_f32_16x16x32_bf16 v[20:23], a[232:235], v[64:67], v[20:23]\n"
"v_rcp_f32 v96, v96\n"
"v_mfma_f32_16x16x32_bf16 v[28:31], a[232:235], v[68:71], v[28:31]\n"
"s_waitcnt lgkmcnt(0)\n"
"v_rcp_f32 v97, v97\n"
"v_mfma_f32_16x16x32_bf16 v[0:3], a[100:103], v[72:75], v[0:3]\n"
"ds_read_b128 v[64:67], v81 offset:14336\n"
"v_mfma_f32_16x16x32_bf16 v[8:11], a[100:103], v[76:79], v[8:11]\n"
"ds_read_b128 v[68:71], v81 offset:30720\n"
"v_mfma_f32_16x16x32_bf16 v[4:7], a[108:111], v[72:75], v[4:7]\n"
"v_rcp_f32 v98, v98\n"
"v_mfma_f32_16x16x32_bf16 v[12:15], a[108:111], v[76:79], v[12:15]\n"
"v_rcp_f32 v99, v99\n"
"v_mfma_f32_16x16x32_bf16 v[16:19], a[228:231], v[72:75], v[16:19]\n"
"v_mul_f32 v92, v92, v56\n"
"v_mul_f32 v93, v93, v57\n"
"v_mfma_f32_16x16x32_bf16 v[24:27], a[228:231], v[76:79], v[24:27]\n"
"v_mul_f32 v94, v94, v58\n"
"v_mul_f32 v95, v95, v59\n"
"v_mfma_f32_16x16x32_bf16 v[20:23], a[236:239], v[72:75], v[20:23]\n"
"v_mul_f32 v96, v96, v60\n"
"v_mul_f32 v97, v97, v61\n"
"v_mfma_f32_16x16x32_bf16 v[28:31], a[236:239], v[76:79], v[28:31]\n"
"s_waitcnt lgkmcnt(0)\n"
"v_mul_f32 v98, v98, v62\n"
"v_mul_f32 v99, v99, v63\n"
"v_mfma_f32_16x16x32_bf16 v[0:3], a[112:115], v[64:67], v[0:3]\n"
"ds_read_b128 v[72:75], v81 offset:15360\n"
"v_cvt_pk_bf16_f32 v104, v92, v93\n"
"v_mfma_f32_16x16x32_bf16 v[8:11], a[112:115], v[68:71], v[8:11]\n"
"ds_read_b128 v[76:79], v81 offset:31744\n"
"v_cvt_pk_bf16_f32 v105, v94, v95\n"
"v_mfma_f32_16x16x32_bf16 v[4:7], a[120:123], v[64:67], v[4:7]\n"
"v_cvt_pk_bf16_f32 v106, v96, v97\n"
"v_cvt_pk_bf16_f32 v107, v98, v99\n"
"v_mfma_f32_16x16x32_bf16 v[12:15], a[120:123], v[68:71], v[12:15]\n"
"buffer_store_dwordx4 v[104:107], v80, s[60:63], s14 offen sc1\n"
"s_add_u32 s14, s14, 524288\n"
"v_mfma_f32_16x16x32_bf16 v[16:19], a[240:243], v[64:67], v[16:19]\n"
"v_mfma_f32_16x16x32_bf16 v[24:27], a[240:243], v[68:71], v[24:27]\n"
"v_mfma_f32_16x16x32_bf16 v[20:23], a[248:251], v[64:67], v[20:23]\n"
"v_mfma_f32_16x16x32_bf16 v[28:31], a[248:251], v[68:71], v[28:31]\n"
"s_waitcnt lgkmcnt(0)\n"
"v_mfma_f32_16x16x32_bf16 v[0:3], a[116:119], v[72:75], v[0:3]\n"
"v_mfma_f32_16x16x32_bf16 v[8:11], a[116:119], v[76:79], v[8:11]\n"
"v_mfma_f32_16x16x32_bf16 v[4:7], a[124:127], v[72:75], v[4:7]\n"
"v_mfma_f32_16x16x32_bf16 v[12:15], a[124:127], v[76:79], v[12:15]\n"
"v_mfma_f32_16x16x32_bf16 v[16:19], a[244:247], v[72:75], v[16:19]\n"
"v_mfma_f32_16x16x32_bf16 v[24:27], a[244:247], v[76:79], v[24:27]\n"
"v_mfma_f32_16x16x32_bf16 v[20:23], a[252:255], v[72:75], v[20:23]\n"
"v_mfma_f32_16x16x32_bf16 v[28:31], a[252:255], v[76:79], v[28:31]\n"
"s_lshr_b32 s14, s8, 1\n"
"s_xnor_b32 s14, s14, s77\n"
"s_and_b32 s14, s14, 1\n"
"s_lshl_b32 s14, s14, 13\n"
"s_lshl_b32 s74, s8, 20\n"
"s_add_u32 s14, s14, s74\n"
"s_add_u32 s14, s14, s13\n"
"v_xor_b32 v81, 32768, v81\n"
"s_add_u32 s8, s8, 1\n"
"s_add_u32 s10, s8, 1\n"
"s_min_u32 s10, s10, 63\n"
"s_xor_b32 s9, s9, 32768\n"
"s_waitcnt vmcnt(2) lgkmcnt(0)\n"
"s_barrier\n"
"ds_read_b128 v[64:67], v81 offset:0\n"
"ds_read_b128 v[68:71], v81 offset:16384\n"
"s_waitcnt lgkmcnt(0)\n"
"v_mfma_f32_16x16x32_bf16 v[32:35], a[0:3], v[64:67], 0\n"
"ds_read_b128 v[72:75], v81 offset:1024\n"
"v_mfma_f32_16x16x32_bf16 v[40:43], a[0:3], v[68:71], 0\n"
"ds_read_b128 v[76:79], v81 offset:17408\n"
"v_mfma_f32_16x16x32_bf16 v[36:39], a[8:11], v[64:67], 0\n"
"s_lshl_b32 s71, s10, 15\n"
"s_lshl_b32 s65, s37, 13\n"
"s_add_u32 s71, s71, s65\n"
"s_add_u32 s71, s71, 4194304\n"
"s_add_u32 m0, s65, s9\n"
"v_mfma_f32_16x16x32_bf16 v[44:47], a[8:11], v[68:71], 0\n"
"v_mfma_f32_16x16x32_bf16 v[48:51], a[128:131], v[64:67], 0\n"
"v_mfma_f32_16x16x32_bf16 v[56:59], a[128:131], v[68:71], 0\n"
"buffer_load_dwordx4 v80, s[60:63], s71 offen offset:0 sc1 lds\n"
"v_mul_f32 v84, v0, v0\n"
"v_mfma_f32_16x16x32_bf16 v[52:55], a[136:139], v[64:67], 0\n"
"v_mul_f32 v85, v1, v1\n"
"v_mul_f32 v86, v2, v2\n"
"v_mfma_f32_16x16x32_bf16 v[60:63], a[136:139], v[68:71], 0\n"
"s_waitcnt lgkmcnt(0)\n"
"v_mul_f32 v87, v3, v3\n"
"v_mul_f32 v88, v4, v4\n"
"v_mfma_f32_16x16x32_bf16 v[32:35], a[4:7], v[72:75], v[32:35]\n"
"ds_read_b128 v[64:67], v81 offset:2048\n"
"v_mul_f32 v89, v5, v5\n"
"v_mfma_f32_16x16x32_bf16 v[40:43], a[4:7], v[76:79], v[40:43]\n"
"ds_read_b128 v[68:71], v81 offset:18432\n"
"buffer_load_dwordx4 v80, s[60:63], s71 offen offset:1024 sc1 lds\n"
"v_mfma_f32_16x16x32_bf16 v[36:39], a[12:15], v[72:75], v[36:39]\n"
"v_mul_f32 v90, v6, v6\n"
"v_mul_f32 v91, v7, v7\n"
"v_mfma_f32_16x16x32_bf16 v[44:47], a[12:15], v[76:79], v[44:47]\n"
"v_fma_f32 v84, v84, v82, s12\n"
"v_fma_f32 v85, v85, v82, s12\n"
"v_mfma_f32_16x16x32_bf16 v[48:51], a[132:135], v[72:75], v[48:51]\n"
"v_fma_f32 v86, v86, v82, s12\n"
"v_fma_f32 v87, v87, v82, s12\n"
"v_mfma_f32_16x16x32_bf16 v[56:59], a[132:135], v[76:79], v[56:59]\n"
"buffer_load_dwordx4 v80, s[60:63], s71 offen offset:2048 sc1 lds\n"
"v_fma_f32 v88, v88, v82, s12\n"
"v_mfma_f32_16x16x32_bf16 v[52:55], a[140:143], v[72:75], v[52:55]\n"
"v_fma_f32 v89, v89, v82, s12\n"
"v_fma_f32 v90, v90, v82, s12\n"
"v_mfma_f32_16x16x32_bf16 v[60:63], a[140:143], v[76:79], v[60:63]\n"
"s_waitcnt lgkmcnt(0)\n"
"v_fma_f32 v91, v91, v82, s12\n"
"v_mul_f32 v84, v84, v0\n"
"v_mfma_f32_16x16x32_bf16 v[32:35], a[16:19], v[64:67], v[32:35]\n"
"ds_read_b128 v[72:75], v81 offset:3072\n"
"v_mul_f32 v85, v85, v1\n"
"v_mfma_f32_16x16x32_bf16 v[40:43], a[16:19], v[68:71], v[40:43]\n"
"ds_read_b128 v[76:79], v81 offset:19456\n"
"buffer_load_dwordx4 v80, s[60:63], s71 offen offset:3072 sc1 lds\n"
"v_mfma_f32_16x16x32_bf16 v[36:39], a[24:27], v[64:67], v[36:39]\n"
"v_mul_f32 v86, v86, v2\n"
"v_mul_f32 v87, v87, v3\n"
"v_mfma_f32_16x16x32_bf16 v[44:47], a[24:27], v[68:71], v[44:47]\n"
"v_mul_f32 v88, v88, v4\n"
"v_mul_f32 v89, v89, v5\n"
"v_mfma_f32_16x16x32_bf16 v[48:51], a[144:147], v[64:67], v[48:51]\n"
"v_mul_f32 v90, v90, v6\n"
"v_mul_f32 v91, v91, v7\n"
"v_mfma_f32_16x16x32_bf16 v[56:59], a[144:147], v[68:71], v[56:59]\n"
"s_add_u32 m0, m0, 4096\n"
"s_add_u32 s71, s71, 4096\n"
"buffer_load_dwordx4 v80, s[60:63], s71 offen offset:0 sc1 lds\n"
"v_mfma_f32_16x16x32_bf16 v[52:55], a[152:155], v[64:67], v[52:55]\n"
"v_exp_f32 v84, v84\n"
"v_mfma_f32_16x16x32_bf16 v[60:63], a[152:155], v[68:71], v[60:63]\n"
"s_waitcnt lgkmcnt(0)\n"
"v_exp_f32 v85, v85\n"
"v_mfma_f32_16x16x32_bf16 v[32:35], a[20:23], v[72:75], v[32:35]\n"
"ds_read_b128 v[64:67], v81 offset:4096\n"
"v_mfma_f32_16x16x32_bf16 v[40:43], a[20:23], v[76:79], v[40:43]\n"
"ds_read_b128 v[68:71], v81 offset:20480\n"
"v_mfma_f32_16x16x32_bf16 v[36:39], a[28:31], v[72:75], v[36:39]\n"
"v_exp_f32 v86, v86\n"
"v_mfma_f32_16x16x32_bf16 v[44:47], a[28:31], v[76:79], v[44:47]\n"
"v_exp_f32 v87, v87\n"
"v_mfma_f32_16x16x32_bf16 v[48:51], a[148:151], v[72:75], v[48:51]\n"
"v_exp_f32 v88, v88\n"
"v_mfma_f32_16x16x32_bf16 v[56:59], a[148:151], v[76:79], v[56:59]\n"
"v_exp_f32 v89, v89\n"
"v_mfma_f32_16x16x32_bf16 v[52:55], a[156:159], v[72:75], v[52:55]\n"
"buffer_load_dwordx4 v80, s[60:63], s71 offen offset:1024 sc1 lds\n"
"v_mfma_f32_16x16x32_bf16 v[60:63], a[156:159], v[76:79], v[60:63]\n"
"s_waitcnt lgkmcnt(0)\n"
"v_exp_f32 v90, v90\n"
"v_mfma_f32_16x16x32_bf16 v[32:35], a[32:35], v[64:67], v[32:35]\n"
"ds_read_b128 v[72:75], v81 offset:5120\n"
"v_mfma_f32_16x16x32_bf16 v[40:43], a[32:35], v[68:71], v[40:43]\n"
"ds_read_b128 v[76:79], v81 offset:21504\n"
"v_mfma_f32_16x16x32_bf16 v[36:39], a[40:43], v[64:67], v[36:39]\n"
"v_exp_f32 v91, v91\n"
"v_mfma_f32_16x16x32_bf16 v[44:47], a[40:43], v[68:71], v[44:47]\n"
"v_mul_f32 v16, v0, v16\n"
"v_mul_f32 v17, v1, v17\n"
"v_mfma_f32_16x16x32_bf16 v[48:51], a[160:163], v[64:67], v[48:51]\n"
"v_mul_f32 v18, v2, v18\n"
"v_mul_f32 v19, v3, v19\n"
"v_mfma_f32_16x16x32_bf16 v[56:59], a[160:163], v[68:71], v[56:59]\n"
"buffer_load_dwordx4 v80, s[60:63], s71 offen offset:2048 sc1 lds\n"
"v_mul_f32 v20, v4, v20\n"
"v_mfma_f32_16x16x32_bf16 v[52:55], a[168:171], v[64:67], v[52:55]\n"
"v_mul_f32 v21, v5, v21\n"
"v_mul_f32 v22, v6, v22\n"
"v_mfma_f32_16x16x32_bf16 v[60:63], a[168:171], v[68:71], v[60:63]\n"
"s_waitcnt lgkmcnt(0)\n"
"v_mul_f32 v23, v7, v23\n"
"v_add_f32 v84, 1.0, v84\n"
"v_mfma_f32_16x16x32_bf16 v[32:35], a[36:39], v[72:75], v[32:35]\n"
"ds_read_b128 v[64:67], v81 offset:6144\n"
"v_add_f32 v85, 1.0, v85\n"
"v_mfma_f32_16x16x32_bf16 v[40:43], a[36:39], v[76:79], v[40:43]\n"
"ds_read_b128 v[68:71], v81 offset:22528\n"
"buffer_load_dwordx4 v80, s[60:63], s71 offen offset:3072 sc1 lds\n"
"v_mfma_f32_16x16x32_bf16 v[36:39], a[44:47], v[72:75], v[36:39]\n"
"v_add_f32 v86, 1.0, v86\n"
"v_add_f32 v87, 1.0, v87\n"
"v_mfma_f32_16x16x32_bf16 v[44:47], a[44:47], v[76:79], v[44:47]\n"
"v_add_f32 v88, 1.0, v88\n"
"v_add_f32 v89, 1.0, v89\n"
"v_mfma_f32_16x16x32_bf16 v[48:51], a[164:167], v[72:75], v[48:51]\n"
"v_add_f32 v90, 1.0, v90\n"
"v_add_f32 v91, 1.0, v91\n"
"v_mfma_f32_16x16x32_bf16 v[56:59], a[164:167], v[76:79], v[56:59]\n"
"v_rcp_f32 v84, v84\n"
"v_mfma_f32_16x16x32_bf16 v[52:55], a[172:175], v[72:75], v[52:55]\n"
"v_rcp_f32 v85, v85\n"
"v_mfma_f32_16x16x32_bf16 v[60:63], a[172:175], v[76:79], v[60:63]\n"
"s_waitcnt lgkmcnt(0)\n"
"v_rcp_f32 v86, v86\n"
"s_waitcnt vmcnt(8)\n"
"v_mov_b32 v108, s34\n"
"v_mov_b32 v109, s35\n"
"s_mov_b64 exec, 1\n"
"buffer_store_dwordx2 v[108:109], off, s[60:63], s11 sc1\n"
"s_mov_b64 exec, -1\n"
"s_add_u32 s11, s11, 16384\n"
"v_mfma_f32_16x16x32_bf16 v[32:35], a[48:51], v[64:67], v[32:35]\n"
"ds_read_b128 v[72:75], v81 offset:7168\n"
"v_mfma_f32_16x16x32_bf16 v[40:43], a[48:51], v[68:71], v[40:43]\n"
"ds_read_b128 v[76:79], v81 offset:23552\n"
"v_mfma_f32_16x16x32_bf16 v[36:39], a[56:59], v[64:67], v[36:39]\n"
"v_rcp_f32 v87, v87\n"
"v_mfma_f32_16x16x32_bf16 v[44:47], a[56:59], v[68:71], v[44:47]\n"
"v_rcp_f32 v88, v88\n"
"v_mfma_f32_16x16x32_bf16 v[48:51], a[176:179], v[64:67], v[48:51]\n"
"v_rcp_f32 v89, v89\n"
"v_mfma_f32_16x16x32_bf16 v[56:59], a[176:179], v[68:71], v[56:59]\n"
"v_rcp_f32 v90, v90\n"
"v_mfma_f32_16x16x32_bf16 v[52:55], a[184:187], v[64:67], v[52:55]\n"
"v_rcp_f32 v91, v91\n"
"v_mfma_f32_16x16x32_bf16 v[60:63], a[184:187], v[68:71], v[60:63]\n"
"s_waitcnt lgkmcnt(0)\n"
"v_mul_f32 v84, v84, v16\n"
"v_mul_f32 v85, v85, v17\n"
"v_mfma_f32_16x16x32_bf16 v[32:35], a[52:55], v[72:75], v[32:35]\n"
"ds_read_b128 v[64:67], v81 offset:8192\n"
"v_mul_f32 v86, v86, v18\n"
"v_mfma_f32_16x16x32_bf16 v[40:43], a[52:55], v[76:79], v[40:43]\n"
"ds_read_b128 v[68:71], v81 offset:24576\n"
"v_mul_f32 v87, v87, v19\n"
"v_mfma_f32_16x16x32_bf16 v[36:39], a[60:63], v[72:75], v[36:39]\n"
"v_mul_f32 v88, v88, v20\n"
"v_mul_f32 v89, v89, v21\n"
"v_mfma_f32_16x16x32_bf16 v[44:47], a[60:63], v[76:79], v[44:47]\n"
"v_mul_f32 v90, v90, v22\n"
"v_mul_f32 v91, v91, v23\n"
"v_mfma_f32_16x16x32_bf16 v[48:51], a[180:183], v[72:75], v[48:51]\n"
"v_cvt_pk_bf16_f32 v100, v84, v85\n"
"v_cvt_pk_bf16_f32 v101, v86, v87\n"
"v_mfma_f32_16x16x32_bf16 v[56:59], a[180:183], v[76:79], v[56:59]\n"
"v_cvt_pk_bf16_f32 v102, v88, v89\n"
"v_cvt_pk_bf16_f32 v103, v90, v91\n"
"v_mfma_f32_16x16x32_bf16 v[52:55], a[188:191], v[72:75], v[52:55]\n"
"buffer_store_dwordx4 v[100:103], v80, s[60:63], s14 offen sc1\n"
"s_add_u32 s14, s14, 524288\n"
"v_mul_f32 v92, v8, v8\n"
"v_mfma_f32_16x16x32_bf16 v[60:63], a[188:191], v[76:79], v[60:63]\n"
"s_waitcnt lgkmcnt(0)\n"
"v_mul_f32 v93, v9, v9\n"
"v_mul_f32 v94, v10, v10\n"
"v_mfma_f32_16x16x32_bf16 v[32:35], a[64:67], v[64:67], v[32:35]\n"
"ds_read_b128 v[72:75], v81 offset:9216\n"
"v_mul_f32 v95, v11, v11\n"
"v_mfma_f32_16x16x32_bf16 v[40:43], a[64:67], v[68:71], v[40:43]\n"
"ds_read_b128 v[76:79], v81 offset:25600\n"
"v_mul_f32 v96, v12, v12\n"
"v_mfma_f32_16x16x32_bf16 v[36:39], a[72:75], v[64:67], v[36:39]\n"
"v_mul_f32 v97, v13, v13\n"
"v_mul_f32 v98, v14, v14\n"
"v_mfma_f32_16x16x32_bf16 v[44:47], a[72:75], v[68:71], v[44:47]\n"
"v_mul_f32 v99, v15, v15\n"
"v_fma_f32 v92, v92, v82, s12\n"
"v_mfma_f32_16x16x32_bf16 v[48:51], a[192:195], v[64:67], v[48:51]\n"
"v_fma_f32 v93, v93, v82, s12\n"
"v_fma_f32 v94, v94, v82, s12\n"
"v_mfma_f32_16x16x32_bf16 v[56:59], a[192:195], v[68:71], v[56:59]\n"
"v_fma_f32 v95, v95, v82, s12\n"
"v_fma_f32 v96, v96, v82, s12\n"
"v_mfma_f32_16x16x32_bf16 v[52:55], a[200:203], v[64:67], v[52:55]\n"
"v_fma_f32 v97, v97, v82, s12\n"
"v_fma_f32 v98, v98, v82, s12\n"
"v_mfma_f32_16x16x32_bf16 v[60:63], a[200:203], v[68:71], v[60:63]\n"
"s_waitcnt lgkmcnt(0)\n"
"v_fma_f32 v99, v99, v82, s12\n"
"v_mul_f32 v92, v92, v8\n"
"v_mfma_f32_16x16x32_bf16 v[32:35], a[68:71], v[72:75], v[32:35]\n"
"ds_read_b128 v[64:67], v81 offset:10240\n"
"v_mul_f32 v93, v93, v9\n"
"v_mfma_f32_16x16x32_bf16 v[40:43], a[68:71], v[76:79], v[40:43]\n"
"ds_read_b128 v[68:71], v81 offset:26624\n"
"v_mul_f32 v94, v94, v10\n"
"v_mfma_f32_16x16x32_bf16 v[36:39], a[76:79], v[72:75], v[36:39]\n"
"v_mul_f32 v95, v95, v11\n"
"v_mul_f32 v96, v96, v12\n"
"v_mfma_f32_16x16x32_bf16 v[44:47], a[76:79], v[76:79], v[44:47]\n"
"v_mul_f32 v97, v97, v13\n"
"v_mul_f32 v98, v98, v14\n"
"v_mfma_f32_16x16x32_bf16 v[48:51], a[196:199], v[72:75], v[48:51]\n"
"v_mul_f32 v99, v99, v15\n"
"v_mfma_f32_16x16x32_bf16 v[56:59], a[196:199], v[76:79], v[56:59]\n"
"v_exp_f32 v92, v92\n"
"v_mfma_f32_16x16x32_bf16 v[52:55], a[204:207], v[72:75], v[52:55]\n"
"v_exp_f32 v93, v93\n"
"v_mfma_f32_16x16x32_bf16 v[60:63], a[204:207], v[76:79], v[60:63]\n"
"s_waitcnt lgkmcnt(0)\n"
"v_exp_f32 v94, v94\n"
"v_mfma_f32_16x16x32_bf16 v[32:35], a[80:83], v[64:67], v[32:35]\n"
"ds_read_b128 v[72:75], v81 offset:11264\n"
"v_mfma_f32_16x16x32_bf16 v[40:43], a[80:83], v[68:71], v[40:43]\n"
"ds_read_b128 v[76:79], v81 offset:27648\n"
"v_mfma_f32_16x16x32_bf16 v[36:39], a[88:91], v[64:67], v[36:39]\n"
"v_exp_f32 v95, v95\n"
"v_mfma_f32_16x16x32_bf16 v[44:47], a[88:91], v[68:71], v[44:47]\n"
"v_exp_f32 v96, v96\n"
"v_mfma_f32_16x16x32_bf16 v[48:51], a[208:211], v[64:67], v[48:51]\n"
"v_exp_f32 v97, v97\n"
"v_mfma_f32_16x16x32_bf16 v[56:59], a[208:211], v[68:71], v[56:59]\n"
"v_exp_f32 v98, v98\n"
"v_mfma_f32_16x16x32_bf16 v[52:55], a[216:219], v[64:67], v[52:55]\n"
"v_exp_f32 v99, v99\n"
"v_mfma_f32_16x16x32_bf16 v[60:63], a[216:219], v[68:71], v[60:63]\n"
"s_waitcnt lgkmcnt(0)\n"
"v_mul_f32 v24, v8, v24\n"
"v_mul_f32 v25, v9, v25\n"
"v_mfma_f32_16x16x32_bf16 v[32:35], a[84:87], v[72:75], v[32:35]\n"
"ds_read_b128 v[64:67], v81 offset:12288\n"
"v_mul_f32 v26, v10, v26\n"
"v_mfma_f32_16x16x32_bf16 v[40:43], a[84:87], v[76:79], v[40:43]\n"
"ds_read_b128 v[68:71], v81 offset:28672\n"
"v_mul_f32 v27, v11, v27\n"
"v_mfma_f32_16x16x32_bf16 v[36:39], a[92:95], v[72:75], v[36:39]\n"
"v_mul_f32 v28, v12, v28\n"
"v_mul_f32 v29, v13, v29\n"
"v_mfma_f32_16x16x32_bf16 v[44:47], a[92:95], v[76:79], v[44:47]\n"
"v_mul_f32 v30, v14, v30\n"
"v_mul_f32 v31, v15, v31\n"
"v_mfma_f32_16x16x32_bf16 v[48:51], a[212:215], v[72:75], v[48:51]\n"
"v_add_f32 v92, 1.0, v92\n"
"v_add_f32 v93, 1.0, v93\n"
"v_mfma_f32_16x16x32_bf16 v[56:59], a[212:215], v[76:79], v[56:59]\n"
"v_add_f32 v94, 1.0, v94\n"
"v_add_f32 v95, 1.0, v95\n"
"v_mfma_f32_16x16x32_bf16 v[52:55], a[220:223], v[72:75], v[52:55]\n"
"v_add_f32 v96, 1.0, v96\n"
"v_add_f32 v97, 1.0, v97\n"
"v_mfma_f32_16x16x32_bf16 v[60:63], a[220:223], v[76:79], v[60:63]\n"
"s_waitcnt lgkmcnt(0)\n"
"v_add_f32 v98, 1.0, v98\n"
"v_add_f32 v99, 1.0, v99\n"
"v_mfma_f32_16x16x32_bf16 v[32:35], a[96:99], v[64:67], v[32:35]\n"
"ds_read_b128 v[72:75], v81 offset:13312\n"
"v_mfma_f32_16x16x32_bf16 v[40:43], a[96:99], v[68:71], v[40:43]\n"
"ds_read_b128 v[76:79], v81 offset:29696\n"
"v_mfma_f32_16x16x32_bf16 v[36:39], a[104:107], v[64:67], v[36:39]\n"
"v_rcp_f32 v92, v92\n"
"v_mfma_f32_16x16x32_bf16 v[44:47], a[104:107], v[68:71], v[44:47]\n"
"v_rcp_f32 v93, v93\n"
"v_mfma_f32_16x16x32_bf16 v[48:51], a[224:227], v[64:67], v[48:51]\n"
"v_rcp_f32 v94, v94\n"
"v_mfma_f32_16x16x32_bf16 v[56:59], a[224:227], v[68:71], v[56:59]\n"
"v_rcp_f32 v95, v95\n"
"v_mfma_f32_16x16x32_bf16 v[52:55], a[232:235], v[64:67], v[52:55]\n"
"v_rcp_f32 v96, v96\n"
"v_mfma_f32_16x16x32_bf16 v[60:63], a[232:235], v[68:71], v[60:63]\n"
"s_waitcnt lgkmcnt(0)\n"
"v_rcp_f32 v97, v97\n"
"v_mfma_f32_16x16x32_bf16 v[32:35], a[100:103], v[72:75], v[32:35]\n"
"ds_read_b128 v[64:67], v81 offset:14336\n"
"v_mfma_f32_16x16x32_bf16 v[40:43], a[100:103], v[76:79], v[40:43]\n"
"ds_read_b128 v[68:71], v81 offset:30720\n"
"v_mfma_f32_16x16x32_bf16 v[36:39], a[108:111], v[72:75], v[36:39]\n"
"v_rcp_f32 v98, v98\n"
"v_mfma_f32_16x16x32_bf16 v[44:47], a[108:111], v[76:79], v[44:47]\n"
"v_rcp_f32 v99, v99\n"
"v_mfma_f32_16x16x32_bf16 v[48:51], a[228:231], v[72:75], v[48:51]\n"
"v_mul_f32 v92, v92, v24\n"
"v_mul_f32 v93, v93, v25\n"
"v_mfma_f32_16x16x32_bf16 v[56:59], a[228:231], v[76:79], v[56:59]\n"
"v_mul_f32 v94, v94, v26\n"
"v_mul_f32 v95, v95, v27\n"
"v_mfma_f32_16x16x32_bf16 v[52:55], a[236:239], v[72:75], v[52:55]\n"
"v_mul_f32 v96, v96, v28\n"
"v_mul_f32 v97, v97, v29\n"
"v_mfma_f32_16x16x32_bf16 v[60:63], a[236:239], v[76:79], v[60:63]\n"
"s_waitcnt lgkmcnt(0)\n"
"v_mul_f32 v98, v98, v30\n"
"v_mul_f32 v99, v99, v31\n"
"v_mfma_f32_16x16x32_bf16 v[32:35], a[112:115], v[64:67], v[32:35]\n"
"ds_read_b128 v[72:75], v81 offset:15360\n"
"v_cvt_pk_bf16_f32 v104, v92, v93\n"
"v_mfma_f32_16x16x32_bf16 v[40:43], a[112:115], v[68:71], v[40:43]\n"
"ds_read_b128 v[76:79], v81 offset:31744\n"
"v_cvt_pk_bf16_f32 v105, v94, v95\n"
"v_mfma_f32_16x16x32_bf16 v[36:39], a[120:123], v[64:67], v[36:39]\n"
"v_cvt_pk_bf16_f32 v106, v96, v97\n"
"v_cvt_pk_bf16_f32 v107, v98, v99\n"
"v_mfma_f32_16x16x32_bf16 v[44:47], a[120:123], v[68:71], v[44:47]\n"
"buffer_store_dwordx4 v[104:107], v80, s[60:63], s14 offen sc1\n"
"s_add_u32 s14, s14, 524288\n"
"v_mfma_f32_16x16x32_bf16 v[48:51], a[240:243], v[64:67], v[48:51]\n"
"v_mfma_f32_16x16x32_bf16 v[56:59], a[240:243], v[68:71], v[56:59]\n"
"v_mfma_f32_16x16x32_bf16 v[52:55], a[248:251], v[64:67], v[52:55]\n"
"v_mfma_f32_16x16x32_bf16 v[60:63], a[248:251], v[68:71], v[60:63]\n"
"s_waitcnt lgkmcnt(0)\n"
"v_mfma_f32_16x16x32_bf16 v[32:35], a[116:119], v[72:75], v[32:35]\n"
"v_mfma_f32_16x16x32_bf16 v[40:43], a[116:119], v[76:79], v[40:43]\n"
"v_mfma_f32_16x16x32_bf16 v[36:39], a[124:127], v[72:75], v[36:39]\n"
"v_mfma_f32_16x16x32_bf16 v[44:47], a[124:127], v[76:79], v[44:47]\n"
"v_mfma_f32_16x16x32_bf16 v[48:51], a[244:247], v[72:75], v[48:51]\n"
"v_mfma_f32_16x16x32_bf16 v[56:59], a[244:247], v[76:79], v[56:59]\n"
"v_mfma_f32_16x16x32_bf16 v[52:55], a[252:255], v[72:75], v[52:55]\n"
"v_mfma_f32_16x16x32_bf16 v[60:63], a[252:255], v[76:79], v[60:63]\n"
"s_lshr_b32 s14, s8, 1\n"
"s_xnor_b32 s14, s14, s77\n"
"s_and_b32 s14, s14, 1\n"
"s_lshl_b32 s14, s14, 13\n"
"s_lshl_b32 s74, s8, 20\n"
"s_add_u32 s14, s14, s74\n"
"s_add_u32 s14, s14, s13\n"
"v_xor_b32 v81, 32768, v81\n"
"s_add_u32 s8, s8, 1\n"
"s_cmp_lt_u32 s8, 64\n"
"s_cbranch_scc1 .Lutop105_%=\n"
"v_mul_f32 v84, v32, v32\n"
"v_mul_f32 v85, v33, v33\n"
"v_mul_f32 v86, v34, v34\n"
"v_mul_f32 v87, v35, v35\n"
"v_mul_f32 v88, v36, v36\n"
"v_mul_f32 v89, v37, v37\n"
"v_mul_f32 v90, v38, v38\n"
"v_mul_f32 v91, v39, v39\n"
"v_fma_f32 v84, v84, v82, s12\n"
"v_fma_f32 v85, v85, v82, s12\n"
"v_fma_f32 v86, v86, v82, s12\n"
"v_fma_f32 v87, v87, v82, s12\n"
"v_fma_f32 v88, v88, v82, s12\n"
"v_fma_f32 v89, v89, v82, s12\n"
"v_fma_f32 v90, v90, v82, s12\n"
"v_fma_f32 v91, v91, v82, s12\n"
"v_mul_f32 v84, v84, v32\n"
"v_mul_f32 v85, v85, v33\n"
"v_mul_f32 v86, v86, v34\n"
"v_mul_f32 v87, v87, v35\n"
"v_mul_f32 v88, v88, v36\n"
"v_mul_f32 v89, v89, v37\n"
"v_mul_f32 v90, v90, v38\n"
"v_mul_f32 v91, v91, v39\n"
"v_exp_f32 v84, v84\n"
"v_exp_f32 v85, v85\n"
"v_exp_f32 v86, v86\n"
"v_exp_f32 v87, v87\n"
"v_exp_f32 v88, v88\n"
"v_exp_f32 v89, v89\n"
"v_exp_f32 v90, v90\n"
"v_exp_f32 v91, v91\n"
"v_mul_f32 v48, v32, v48\n"
"v_mul_f32 v49, v33, v49\n"
"v_mul_f32 v50, v34, v50\n"
"v_mul_f32 v51, v35, v51\n"
"v_mul_f32 v52, v36, v52\n"
"v_mul_f32 v53, v37, v53\n"
"v_mul_f32 v54, v38, v54\n"
"v_mul_f32 v55, v39, v55\n"
"v_add_f32 v84, 1.0, v84\n"
"v_add_f32 v85, 1.0, v85\n"
"v_add_f32 v86, 1.0, v86\n"
"v_add_f32 v87, 1.0, v87\n"
"v_add_f32 v88, 1.0, v88\n"
"v_add_f32 v89, 1.0, v89\n"
"v_add_f32 v90, 1.0, v90\n"
"v_add_f32 v91, 1.0, v91\n"
"v_rcp_f32 v84, v84\n"
"v_rcp_f32 v85, v85\n"
"v_rcp_f32 v86, v86\n"
"v_rcp_f32 v87, v87\n"
"v_rcp_f32 v88, v88\n"
"v_rcp_f32 v89, v89\n"
"v_rcp_f32 v90, v90\n"
"v_rcp_f32 v91, v91\n"
"v_mul_f32 v84, v84, v48\n"
"v_mul_f32 v85, v85, v49\n"
"v_mul_f32 v86, v86, v50\n"
"v_mul_f32 v87, v87, v51\n"
"v_mul_f32 v88, v88, v52\n"
"v_mul_f32 v89, v89, v53\n"
"v_mul_f32 v90, v90, v54\n"
"v_mul_f32 v91, v91, v55\n"
"v_cvt_pk_bf16_f32 v100, v84, v85\n"
"v_cvt_pk_bf16_f32 v101, v86, v87\n"
"v_cvt_pk_bf16_f32 v102, v88, v89\n"
"v_cvt_pk_bf16_f32 v103, v90, v91\n"
"buffer_store_dwordx4 v[100:103], v80, s[60:63], s14 offen sc1\n"
"s_add_u32 s14, s14, 524288\n"
"v_mul_f32 v92, v40, v40\n"
"v_mul_f32 v93, v41, v41\n"
"v_mul_f32 v94, v42, v42\n"
"v_mul_f32 v95, v43, v43\n"
"v_mul_f32 v96, v44, v44\n"
"v_mul_f32 v97, v45, v45\n"
"v_mul_f32 v98, v46, v46\n"
"v_mul_f32 v99, v47, v47\n"
"v_fma_f32 v92, v92, v82, s12\n"
"v_fma_f32 v93, v93, v82, s12\n"
"v_fma_f32 v94, v94, v82, s12\n"
"v_fma_f32 v95, v95, v82, s12\n"
"v_fma_f32 v96, v96, v82, s12\n"
"v_fma_f32 v97, v97, v82, s12\n"
"v_fma_f32 v98, v98, v82, s12\n"
"v_fma_f32 v99, v99, v82, s12\n"
"v_mul_f32 v92, v92, v40\n"
"v_mul_f32 v93, v93, v41\n"
"v_mul_f32 v94, v94, v42\n"
"v_mul_f32 v95, v95, v43\n"
"v_mul_f32 v96, v96, v44\n"
"v_mul_f32 v97, v97, v45\n"
"v_mul_f32 v98, v98, v46\n"
"v_mul_f32 v99, v99, v47\n"
"v_exp_f32 v92, v92\n"
"v_exp_f32 v93, v93\n"
"v_exp_f32 v94, v94\n"
"v_exp_f32 v95, v95\n"
"v_exp_f32 v96, v96\n"
"v_exp_f32 v97, v97\n"
"v_exp_f32 v98, v98\n"
"v_exp_f32 v99, v99\n"
"v_mul_f32 v56, v40, v56\n"
"v_mul_f32 v57, v41, v57\n"
"v_mul_f32 v58, v42, v58\n"
"v_mul_f32 v59, v43, v59\n"
"v_mul_f32 v60, v44, v60\n"
"v_mul_f32 v61, v45, v61\n"
"v_mul_f32 v62, v46, v62\n"
"v_mul_f32 v63, v47, v63\n"
"v_add_f32 v92, 1.0, v92\n"
"v_add_f32 v93, 1.0, v93\n"
"v_add_f32 v94, 1.0, v94\n"
"v_add_f32 v95, 1.0, v95\n"
"v_add_f32 v96, 1.0, v96\n"
"v_add_f32 v97, 1.0, v97\n"
"v_add_f32 v98, 1.0, v98\n"
"v_add_f32 v99, 1.0, v99\n"
"v_rcp_f32 v92, v92\n"
"v_rcp_f32 v93, v93\n"
"v_rcp_f32 v94, v94\n"
"v_rcp_f32 v95, v95\n"
"v_rcp_f32 v96, v96\n"
"v_rcp_f32 v97, v97\n"
"v_rcp_f32 v98, v98\n"
"v_rcp_f32 v99, v99\n"
"v_mul_f32 v92, v92, v56\n"
"v_mul_f32 v93, v93, v57\n"
"v_mul_f32 v94, v94, v58\n"
"v_mul_f32 v95, v95, v59\n"
"v_mul_f32 v96, v96, v60\n"
"v_mul_f32 v97, v97, v61\n"
"v_mul_f32 v98, v98, v62\n"
"v_mul_f32 v99, v99, v63\n"
"v_cvt_pk_bf16_f32 v104, v92, v93\n"
"v_cvt_pk_bf16_f32 v105, v94, v95\n"
"v_cvt_pk_bf16_f32 v106, v96, v97\n"
"v_cvt_pk_bf16_f32 v107, v98, v99\n"
"buffer_store_dwordx4 v[104:107], v80, s[60:63], s14 offen sc1\n"
"s_add_u32 s14, s14, 524288\n"
"s_waitcnt vmcnt(2)\n"
"v_mov_b32 v108, s34\n"
"v_mov_b32 v109, s35\n"
"s_mov_b64 exec, 1\n"
"buffer_store_dwordx2 v[108:109], off, s[60:63], s11 sc1\n"
"s_mov_b64 exec, -1\n"
"s_add_u32 s11, s11, 16384\n"
"s_waitcnt vmcnt(0)\n"
"v_mov_b32 v108, s34\n"
"v_mov_b32 v109, s35\n"
"s_mov_b64 exec, 1\n"
"buffer_store_dwordx2 v[108:109], off, s[60:63], s11 sc1\n"
"s_mov_b64 exec, -1\n"
"s_add_u32 s11, s11, 16384\n"
"s_endpgm\n"
".Lfail5_%=:\n"
"s_mov_b32 s64, 3170560\n"
"v_mov_b32 v0, 1\n"
"s_mov_b64 exec, 1\n"
"buffer_store_dword v0, off, s[60:63], s64 sc1\n"
"s_mov_b64 exec, -1\n"
".Lend1_%=:\n"
"s_endpgm\n"
      :
      : "s"(__builtin_amdgcn_kernarg_segment_ptr()), "s"(blockIdx.x), "s"(wave)
      : "memory", "vcc", "scc",
"s8", "s9", "s10", "s11", "s12", "s13", "s14", "s15", "s16", "s17", "s18", "s19", "s20", "s21", "s22", "s23", "s24", "s25", "s26", "s27", "s28", "s29", "s30", "s31", "s32", "s33", "s34", "s35", "s36", "s37", "s38", "s39", "s40", "s41", "s42", "s43", "s44", "s45", "s46", "s47", "s48", "s49", "s50", "s51", "s52", "s53", "s54", "s55", "s56", "s57", "s58", "s59", "s60", "s61", "s62", "s63", "s64", "s65", "s66", "s67", "s68", "s69", "s70", "s71", "s72", "s73", "s74", "s75", "s76", "s77", "s78", "s79", "s80", "s81", "s82", "s83", "s84", "s85", "s86", "s87", "s88", "s89", "s90", "s91", "s92", "s93", "s94", "s95", "s96", "s97", "s98", "s99", "s100", "s101", "v254", "v255", "a254", "a255"
  );
}

static const unsigned g_tune[4] = {1690, 2620, 0, 0};

extern "C" void kernel_launch(void* const* d_in, const int* in_sizes, int n_in, void* d_out, int out_size, void* d_ws,
                              size_t ws_size, hipStream_t stream) {
  Params p;
  p.x = d_in[0]; p.mem0 = d_in[1]; p.wq = d_in[2]; p.wk = d_in[3]; p.wv = d_in[4]; p.wo = d_in[5];
  p.wg = d_in[8]; p.wu = d_in[9]; p.wd = d_in[10]; p.out = d_out;
  p.ws = (void*)(((uintptr_t)d_ws + 0x1fffff) & ~(uintptr_t)0x1fffff);
  p.p0 = g_tune[0]; p.p1 = g_tune[1]; p.p2 = g_tune[2]; p.p3 = g_tune[3];
  (void)hipFuncSetAttribute((const void*)rmt_main, hipFuncAttributeMaxDynamicSharedMemorySize, LDS_BYTES);
  rmt_main<<<256, 256, LDS_BYTES, stream>>>(p);
}
```
